```python
import torch

def _softmax(x, axis=-1):
    return torch.softmax(x, dim=axis)
def _gelu(x):
    return torch.nn.functional.gelu(x, approximate="tanh")
def _silu(x):
    return torch.nn.functional.silu(x)
def _sigmoid(x):
    return torch.sigmoid(x)
def _layernorm(x, axis=-1, eps=1e-5):
    m = x.mean(dim=axis, keepdim=True)
    v = x.var(dim=axis, keepdim=True, unbiased=False)
    return (x - m) / torch.sqrt(v + eps)
def _scatter_add(tgt, idx, src, axis=0):
    return tgt.clone().index_add_(axis, idx, src)
def _segment_sum(src, seg, n):
    out = torch.zeros((n,) + src.shape[1:], dtype=src.dtype)
    return out.index_add_(0, seg, src)
def _pad(x, pw):
    flat = tuple(v for pair in reversed(pw) for v in pair)
    return torch.nn.functional.pad(x, flat)

kernel_name = 'parallel_sum__grouped_kv_mqa__dense_activation_mish__prompt'

def setup_inputs():

    _w = (lambda a: (lambda t: t.to(torch.bfloat16) if t.is_floating_point() else t)(torch.as_tensor(a)))
    def _A_setup():
        B, S, D, H = 1, 2048, 512, 32
        Dh = D // H
        return {
            "x": _w((lambda *s: torch.randn(*s, dtype=torch.bfloat16) * 0.05)(B, S, D)),
            "wq": _w((lambda *s: torch.randn(*s, dtype=torch.bfloat16) * 0.05)(D, H * Dh)),
            "wk": _w((lambda *s: torch.randn(*s, dtype=torch.bfloat16) * 0.05)(D, Dh)),
            "wv": _w((lambda *s: torch.randn(*s, dtype=torch.bfloat16) * 0.05)(D, Dh)),
            "wo": _w((lambda *s: torch.randn(*s, dtype=torch.bfloat16) * 0.05)(H * Dh, D)),
        }
    _A_in = _A_setup()

    def _B_setup():
        B, S, D, F = 1, 2048, 512, 1792
        return {
            "x": _w((lambda *s: torch.randn(*s, dtype=torch.bfloat16) * 0.05)(B, S, D)),
            "w1": _w((lambda *s: torch.randn(*s, dtype=torch.bfloat16) * 0.8)(D, F) * (D ** -0.5)),
            "b1": _w((lambda *s: torch.randn(*s, dtype=torch.bfloat16) * 0.4)(F)),
            "w2": _w((lambda *s: torch.randn(*s, dtype=torch.bfloat16) * 0.2)(F, D) * (F ** -0.5)),
        }
    _B_in = _B_setup()

    return {
        "A_x": _A_in["x"],
        "A_wq": _A_in["wq"],
        "A_wk": _A_in["wk"],
        "A_wv": _A_in["wv"],
        "A_wo": _A_in["wo"],
        "B_x": _B_in["x"],
        "B_w1": _B_in["w1"],
        "B_b1": _B_in["b1"],
        "B_w2": _B_in["w2"],
    }

def reference(A_x, A_wq, A_wk, A_wv, A_wo, B_x, B_w1, B_b1, B_w2):
    def _A_ref(x, wq, wk, wv, wo):
        B, S, D = x.shape
        H = 32
        Dh = D // H
        q = (lambda x, *d: x.permute(*d))((x @ wq).reshape(B, S, H, Dh), 0, 2, 1, 3)
        k = x @ wk
        v = x @ wv
        scores = torch.einsum("bhqd,bkd->bhqk", q, k) * (Dh ** -0.5)
        p = _softmax(scores, axis=-1)
        o = torch.einsum("bhqk,bkd->bhqd", p, v)
        return (lambda x, *d: x.permute(*d))(o, 0, 2, 1, 3).reshape(B, S, H * Dh) @ wo

    def _B_ref(x, w1, b1, w2):
        h = torch.einsum("bsd,df->bsf", x, w1) + b1
        sp = torch.log1p(torch.exp(-torch.abs(h))) + torch.where(h > 0, h, torch.zeros_like(h))
        act = h * torch.tanh(sp)
        return torch.einsum("bsf,fd->bsd", act, w2)

    x = A_x
    xn = _layernorm(x, axis=-1)
    return x + _A_ref(xn, A_wq, A_wk, A_wv, A_wo) + _B_ref(xn, B_w1, B_b1, B_w2)
```

```cpp
#include <hip/hip_runtime.h>
#include <hip/hip_bf16.h>
typedef unsigned short u16;
__device__ inline float b2f(u16 v){ return __uint_as_float(((unsigned)v)<<16); }
__device__ inline u16 f2b(float f){ __hip_bfloat16 h=__float2bfloat16(f); return *(u16*)&h; }
constexpr int S=2048,D=512,F=1792,H=32,DH=16;
__global__ void k_ln(const u16* x, float* xn){ int t=blockIdx.x; __shared__ float red[2][8]; int i=threadIdx.x; float v=b2f(x[t*D+i]); float s=v,q=v*v;
  for(int o=32;o;o>>=1){ s+=__shfl_xor(s,o); q+=__shfl_xor(q,o);} if((i&63)==0){red[0][i>>6]=s; red[1][i>>6]=q;} __syncthreads(); s=0;q=0; for(int j=0;j<8;++j){s+=red[0][j]; q+=red[1][j];}
  float m=s/D, var=q/D-m*m; xn[t*D+i]=b2f(f2b((v-m)*rsqrtf(var+1e-5f))); }
template<int MODE> __global__ void k_gemm(const float* A, const u16* B, float* C, int K, int N, const u16* bias){ int n=blockIdx.x*64+(threadIdx.x&63); int m0=(blockIdx.y*4+(threadIdx.x>>6))*8; if(n>=N) return; float acc[8]={};
  for(int k=0;k<K;++k){ float b=b2f(B[k*N+n]); for(int r=0;r<8;++r) acc[r]+=A[(m0+r)*K+k]*b; }
  for(int r=0;r<8;++r){ float v=acc[r]; if(MODE==1){ v+=b2f(bias[n]); float sp=log1pf(expf(-fabsf(v)))+fmaxf(v,0.f); v=v*tanhf(sp);} C[(m0+r)*N+n]=b2f(f2b(v)); } }
__global__ void k_attn(const float* q,const float* k,const float* v,float* o){ int row=blockIdx.x*256+threadIdx.x; int t=row/H,h=row%H; float qq[DH]; for(int d=0;d<DH;++d) qq[d]=q[t*D+h*DH+d]*0.25f;
  float m=-1e30f,l=0,acc[DH]={}; for(int j=0;j<S;++j){ float s=0; for(int d=0;d<DH;++d) s+=qq[d]*k[j*DH+d]; float mn=fmaxf(m,s); float a=__expf(m-mn), p=__expf(s-mn); l=l*a+p; for(int d=0;d<DH;++d) acc[d]=acc[d]*a+p*v[j*DH+d]; m=mn; }
  for(int d=0;d<DH;++d) o[t*D+h*DH+d]=b2f(f2b(acc[d]/l)); }
__global__ void k_fin(const u16* x,const float* a,const float* b,u16* out){ int i=blockIdx.x*256+threadIdx.x; out[i]=f2b(b2f(x[i])+a[i]+b[i]); }
extern "C" void kernel_launch(void* const* d_in, const int* in_sizes, int n_in, void* d_out, int out_size, void* d_ws, size_t ws_size, hipStream_t stream) {
  const u16 *x=(const u16*)d_in[0],*wq=(const u16*)d_in[1],*wk=(const u16*)d_in[2],*wv=(const u16*)d_in[3],*wo=(const u16*)d_in[4],*w1=(const u16*)d_in[6],*b1=(const u16*)d_in[7],*w2=(const u16*)d_in[8];
  float* ws=(float*)d_ws; float *xn=ws,*q=xn+S*D,*k=q+S*D,*v=k+S*DH,*o=v+S*DH,*act=o+S*D,*a=act+S*F,*b=a+S*D;
  k_ln<<<S,512,0,stream>>>(x,xn);
  k_gemm<0><<<dim3(8,64),256,0,stream>>>(xn,wq,q,D,D,nullptr);
  k_gemm<0><<<dim3(1,64),256,0,stream>>>(xn,wk,k,D,DH,nullptr);
  k_gemm<0><<<dim3(1,64),256,0,stream>>>(xn,wv,v,D,DH,nullptr);
  k_gemm<1><<<dim3(28,64),256,0,stream>>>(xn,w1,act,D,F,b1);
  k_attn<<<S*H/256,256,0,stream>>>(q,k,v,o);
  k_gemm<0><<<dim3(8,64),256,0,stream>>>(o,wo,a,D,D,nullptr);
  k_gemm<0><<<dim3(8,64),256,0,stream>>>(act,w2,b,F,D,nullptr);
  k_fin<<<S*D/256,256,0,stream>>>(x,a,b,(u16*)d_out);
}
```

```cpp
#include <hip/hip_runtime.h>

__global__ __launch_bounds__(512, 2) void fused_block_kernel(const void* x, const void* wq, const void* wk, const void* wv,
                                                             const void* wo, const void* w1, const void* b1, const void* w2,
                                                             void* out, void* ws) {
  __shared__ __attribute__((aligned(1024))) char lds[163840];
  const unsigned wave = __builtin_amdgcn_readfirstlane(threadIdx.x >> 6);
  asm volatile(
      "LSTART:\n"
      "s_load_dwordx16 s[8:23], %0, 0\n"
      "s_load_dwordx4 s[24:27], %0, 64\n"
      "s_setprio 3\n"
      "s_mov_b32 s77, %1\n"
      "s_add_u32 s54, %2, 3\n"
      "s_and_b32 s54, s54, 3\n"
      "s_and_b32 s60, %2, 4\n"
      "s_or_b32 s54, s54, s60\n"
      "v_mbcnt_lo_u32_b32 v244, -1, 0\n"
      "v_mbcnt_hi_u32_b32 v244, -1, v244\n"
      "s_getpc_b64 s[58:59]\n"
      "LPC:\n"
      "s_and_b32 s60, s77, 0xf8\n"
      "s_or_b32 s60, s60, s54\n"
      "s_mul_i32 s60, s60, 192\n"
      "v_lshl_add_u32 v245, v244, 7, s60\n"
      "v_min_u32 v245, LEND-LPC-4, v245\n"
      "s_mov_b64 exec, 15\n"
      "global_load_dword v55, v245, s[58:59]\n"
      "s_mov_b64 exec, -1\n"
      "v_lshlrev_b32 v247, 4, v244\n"
      "s_bfe_u32 s52, s77, 0x20001\n"
      "s_lshl_b32 s52, s52, 3\n"
      "s_lshr_b32 s60, s77, 5\n"
      "s_or_b32 s52, s52, s60\n"
      "s_and_b32 s53, s77, 1\n"
      "s_lshl_b32 s53, s53, 2\n"
      "s_bfe_u32 s55, s77, 0x20003\n"
      "s_or_b32 s53, s53, s55\n"
      "s_and_b32 s56, s54, 3\n"
      "s_lshl_b32 s60, s52, 16\n"
      "s_lshl_b32 s61, s55, 14\n"
      "s_lshl_b32 s62, s56, 12\n"
      "s_add_u32 s75, s61, s62\n"
      "s_mov_b32 s64, 0x3f803f80\n"
      "s_mov_b32 s65, 0xffff0000\n"
      "v_mov_b32 v49, 0x3ef5fdf0\n"
      "s_lshl_b32 s100, s52, 1\n"
      "s_and_b32 s100, s100, 15\n"
      "s_lshl_b32 s61, s100, 6\n"
      "v_add_u32 v54, s61, v247\n"
      "v_and_b32 v54, 0x3ff, v54\n"
      "v_lshrrev_b32 v52, 1, v247\n"
      "v_add_u32 v52, 161792, v52\n"
      "v_mov_b64 v[50:51], 0\n"
      "ds_write_b64 v52, v[50:51]\n"
      "s_waitcnt lgkmcnt(0)\n"
      "s_add_u32 s28, s8, s60\n"
      "s_addc_u32 s29, s9, 0\n"
      "s_and_b32 s29, s29, 0xffff\n"
      "s_mov_b32 s30, 65536\n"
      "s_mov_b32 s31, 0x20000\n"
      "s_cmp_ge_u32 s54, 4\n"
      "s_cbranch_scc1 LHEAD2\n"
      "buffer_load_dwordx4 v[0:3], v54, s[28:31], s75 offen offset:0\n"
      "buffer_load_dwordx4 v[4:7], v54, s[28:31], s75 offen offset:1024\n"
      "buffer_load_dwordx4 v[8:11], v54, s[28:31], s75 offen offset:2048\n"
      "buffer_load_dwordx4 v[12:15], v54, s[28:31], s75 offen offset:3072\n"
      "s_branch LATT\n"
      "LHEAD2:\n"
      "s_cmp_lt_u32 s54, 7\n"
      "s_cbranch_scc1 LHELP\n"
      "s_bitcmp1_b32 s53, 2\n"
      "s_cselect_b64 s[12:13], s[14:15], s[12:13]\n"
      "s_mov_b32 s44, s12\n"
      "s_and_b32 s45, s13, 0xffff\n"
      "s_mov_b32 s46, 16384\n"
      "s_mov_b32 s47, 0x20000\n"
      "s_add_u32 s68, s55, 3\n"
      "s_and_b32 s68, s68, 3\n"
      "s_lshl_b32 s68, s68, 14\n"
      "s_lshl_b32 s67, s100, 10\n"
      "v_and_b32 v221, 16, v244\n"
      "v_lshlrev_b32 v221, 3, v221\n"
      "v_xor_b32 v246, v221, v247\n"
      "s_add_u32 m0, s68, 0\n"
      "s_nop 0\n"
      "buffer_load_dwordx4 v246, s[44:47], s67 offen offset:0 lds\n"
      "buffer_load_dwordx4 v246, s[44:47], s67 offen offset:1024 lds\n"
      "s_add_u32 m0, s68, 2048\n"
      "s_add_u32 s67, s67, 2048\n"
      "s_and_b32 s67, s67, 0x3c00\n"
      "buffer_load_dwordx4 v246, s[44:47], s67 offen offset:0 lds\n"
      "buffer_load_dwordx4 v246, s[44:47], s67 offen offset:1024 lds\n"
      "s_add_u32 m0, s68, 4096\n"
      "s_add_u32 s67, s67, 2048\n"
      "s_and_b32 s67, s67, 0x3c00\n"
      "buffer_load_dwordx4 v246, s[44:47], s67 offen offset:0 lds\n"
      "buffer_load_dwordx4 v246, s[44:47], s67 offen offset:1024 lds\n"
      "s_add_u32 m0, s68, 6144\n"
      "s_add_u32 s67, s67, 2048\n"
      "s_and_b32 s67, s67, 0x3c00\n"
      "buffer_load_dwordx4 v246, s[44:47], s67 offen offset:0 lds\n"
      "buffer_load_dwordx4 v246, s[44:47], s67 offen offset:1024 lds\n"
      "s_add_u32 m0, s68, 8192\n"
      "s_add_u32 s67, s67, 2048\n"
      "s_and_b32 s67, s67, 0x3c00\n"
      "buffer_load_dwordx4 v246, s[44:47], s67 offen offset:0 lds\n"
      "buffer_load_dwordx4 v246, s[44:47], s67 offen offset:1024 lds\n"
      "s_add_u32 m0, s68, 10240\n"
      "s_add_u32 s67, s67, 2048\n"
      "s_and_b32 s67, s67, 0x3c00\n"
      "buffer_load_dwordx4 v246, s[44:47], s67 offen offset:0 lds\n"
      "buffer_load_dwordx4 v246, s[44:47], s67 offen offset:1024 lds\n"
      "s_add_u32 m0, s68, 12288\n"
      "s_add_u32 s67, s67, 2048\n"
      "s_and_b32 s67, s67, 0x3c00\n"
      "buffer_load_dwordx4 v246, s[44:47], s67 offen offset:0 lds\n"
      "buffer_load_dwordx4 v246, s[44:47], s67 offen offset:1024 lds\n"
      "s_add_u32 m0, s68, 14336\n"
      "s_add_u32 s67, s67, 2048\n"
      "s_and_b32 s67, s67, 0x3c00\n"
      "buffer_load_dwordx4 v246, s[44:47], s67 offen offset:0 lds\n"
      "buffer_load_dwordx4 v246, s[44:47], s67 offen offset:1024 lds\n"
      "s_mov_b32 s32, s26\n"
      "s_and_b32 s33, s27, 0xffff\n"
      "s_mov_b32 s34, 12582912\n"
      "s_mov_b32 s35, 0x20000\n"
      "v_mov_b32 v245, 0\n"
      "buffer_load_dwordx2 v[222:223], v245, s[32:35], 0 offen sc1\n"
      "s_setprio 1\n"
      "s_barrier\n"
      "v_lshrrev_b32 v224, 4, v244\n"
      "v_bfe_u32 v64, v244, 2, 2\n"
      "v_lshl_add_u32 v224, v224, 3, v64\n"
      "v_and_b32 v64, 3, v244\n"
      "v_lshlrev_b32 v64, 3, v64\n"
      "v_lshl_or_b32 v224, v224, 5, v64\n"
      "v_add_u32 v224, s68, v224\n"
      "v_xor_b32 v224, v221, v224\n"
      "v_xor_b32 v221, 128, v224\n"
      "s_lshl_b32 s58, s56, 2\n"
      "v_xor_b32 v64, s58, v244\n"
      "v_lshlrev_b32 v64, 4, v64\n"
      "s_lshl_b32 s59, s56, 12\n"
      "s_add_u32 s58, s55, 3\n"
      "s_and_b32 s58, s58, 3\n"
      "s_lshl_b32 s58, s58, 14\n"
      "s_or_b32 s58, s58, s59\n"
      "v_or_b32 v56, s58, v64\n"
      "v_xor_b32 v57, 1040, v56\n"
      "v_xor_b32 v58, 2080, v56\n"
      "v_xor_b32 v59, 3120, v56\n"
      "v_mov_b32 v64, s64\n"
      "s_mov_b32 vcc_lo, 0x10001\n"
      "s_mov_b32 vcc_hi, 0x10001\n"
      "v_cndmask_b32 v192, 0, v64, vcc\n"
      "v_cndmask_b32 v193, 0, v64, vcc\n"
      "v_mov_b64 v[194:195], v[192:193]\n"
      "s_mov_b32 vcc_lo, 0x100010\n"
      "s_mov_b32 vcc_hi, 0x100010\n"
      "v_cndmask_b32 v196, 0, v64, vcc\n"
      "v_cndmask_b32 v197, 0, v64, vcc\n"
      "v_mov_b64 v[198:199], v[196:197]\n"
      "s_mov_b32 vcc_lo, 0x1000100\n"
      "s_mov_b32 vcc_hi, 0x1000100\n"
      "v_cndmask_b32 v200, 0, v64, vcc\n"
      "v_cndmask_b32 v201, 0, v64, vcc\n"
      "v_mov_b64 v[202:203], v[200:201]\n"
      "s_mov_b32 vcc_lo, 0x10001000\n"
      "s_mov_b32 vcc_hi, 0x10001000\n"
      "v_cndmask_b32 v204, 0, v64, vcc\n"
      "v_cndmask_b32 v205, 0, v64, vcc\n"
      "v_mov_b64 v[206:207], v[204:205]\n"
      "s_lshl_b32 s59, s55, 14\n"
      "v_and_b32 v64, 15, v244\n"
      "v_lshrrev_b32 v65, 4, v244\n"
      "v_xor_b32 v65, v65, v64\n"
      "v_lshlrev_b32 v65, 4, v65\n"
      "v_lshl_or_b32 v228, v64, 10, v65\n"
      "v_add_u32 v228, s59, v228\n"
      "v_xor_b32 v229, 64, v228\n"
      "v_xor_b32 v230, 128, v228\n"
      "v_xor_b32 v231, 192, v228\n"
      "s_lshl_b32 s60, s52, 1\n"
      "s_lshr_b32 s61, s55, 1\n"
      "s_add_u32 s60, s60, s61\n"
      "s_lshl_b32 s60, s60, 11\n"
      "s_and_b32 s61, s55, 1\n"
      "s_lshl_b32 s61, s61, 8\n"
      "s_add_u32 s60, s60, s61\n"
      "s_lshr_b32 s61, s53, 2\n"
      "s_lshl_b32 s61, s61, 10\n"
      "s_add_u32 s60, s60, s61\n"
      "s_add_u32 s60, s60, 1048576\n"
      "v_lshrrev_b32 v225, 5, v244\n"
      "v_lshlrev_b32 v225, 9, v225\n"
      "v_and_b32 v226, 15, v244\n"
      "v_lshl_or_b32 v225, v226, 4, v225\n"
      "v_bfe_u32 v226, v244, 4, 1\n"
      "v_lshl_or_b32 v225, v226, 3, v225\n"
      "v_add_u32 v225, s60, v225\n"
      "s_lshl_b32 s60, s52, 3\n"
      "s_add_u32 s60, s60, s53\n"
      "s_mul_i32 s60, s60, 64\n"
      "s_add_u32 s60, s60, 4096\n"
      "v_mov_b32 v226, s60\n"
      "s_mov_b32 s68, 131072\n"
      "s_mov_b32 s40, s20\n"
      "s_and_b32 s41, s21, 0xffff\n"
      "s_mov_b32 s42, 3584\n"
      "s_mov_b32 s43, 0x20000\n"
      "s_lshl_b32 s60, s53, 3\n"
      "s_mul_i32 s61, s53, 6\n"
      "s_add_u32 s61, s61, 8\n"
      "s_min_u32 s60, s60, s61\n"
      "s_add_u32 s69, s60, s56\n"
      "s_lshl_b32 s69, s69, 6\n"
      "s_and_b32 s61, s53, 4\n"
      "s_or_b32 s61, s61, s56\n"
      "s_cmp_ge_u32 s61, 6\n"
      "s_cselect_b32 s60, 0, 256\n"
      "s_cselect_b32 s61, 0x7ffffff0, 0\n"
      "s_add_u32 s15, s69, s60\n"
      "v_lshrrev_b32 v64, 2, v244\n"
      "v_mul_u32_u24 v64, 3584, v64\n"
      "v_lshrrev_b32 v65, 4, v244\n"
      "v_and_b32 v65, 2, v65\n"
      "v_and_b32 v66, 3, v244\n"
      "v_xor_b32 v65, v65, v66\n"
      "v_lshl_add_u32 v32, v65, 4, v64\n"
      "v_add_u32 v33, 56320, v32\n"
      "v_or_b32 v50, s61, v32\n"
      "v_or_b32 v51, s61, v33\n"
      "v_lshrrev_b32 v64, 4, v244\n"
      "v_bfe_u32 v65, v244, 2, 2\n"
      "v_and_b32 v66, 3, v244\n"
      "v_lshlrev_b32 v66, 3, v66\n"
      "v_lshl_add_u32 v34, v64, 3, v65\n"
      "v_lshl_or_b32 v34, v34, 6, v66\n"
      "v_and_b32 v64, 1, v64\n"
      "v_lshl_or_b32 v34, v64, 5, v34\n"
      "v_add_u32 v34, s68, v34\n"
      "v_xor_b32 v35, 32, v34\n"
      "v_and_b32 v64, 15, v244\n"
      "v_lshrrev_b32 v65, 4, v244\n"
      "v_xor_b32 v65, v65, v64\n"
      "v_lshlrev_b32 v65, 4, v65\n"
      "v_lshl_or_b32 v36, v64, 10, v65\n"
      "v_xor_b32 v37, 64, v36\n"
      "v_xor_b32 v38, 128, v36\n"
      "v_xor_b32 v39, 192, v36\n"
      "v_mov_b32 v210, 0x7ffffff0\n"
      "v_mov_b32 v211, 0x7ffffff0\n"
      "s_mov_b32 s36, s18\n"
      "s_and_b32 s37, s19, 0xffff\n"
      "s_mov_b32 s38, 1835008\n"
      "s_mov_b32 s39, 0x20000\n"
      "s_mul_i32 s67, s100, 114688\n"
      "s_add_u32 s67, s67, s69\n"
      "v_lshrrev_b32 v48, 4, v244\n"
      "v_lshlrev_b32 v48, 3, v48\n"
      "v_and_b32 v52, 3, v244\n"
      "v_lshlrev_b32 v52, 2, v52\n"
      "v_add_u32 v52, 161792, v52\n"
      "v_mov_b32 v53, 1\n"
      "s_setprio 3\n"
      "s_waitcnt vmcnt(1)\n"
      "ds_read_b64_tr_b16 v[64:65], v224 offset:0\n"
      "ds_read_b64_tr_b16 v[66:67], v221 offset:0\n"
      "ds_read_b64_tr_b16 v[68:69], v224 offset:1024\n"
      "ds_read_b64_tr_b16 v[70:71], v221 offset:1024\n"
      "ds_read_b64_tr_b16 v[72:73], v224 offset:2048\n"
      "ds_read_b64_tr_b16 v[74:75], v221 offset:2048\n"
      "ds_read_b64_tr_b16 v[76:77], v224 offset:3072\n"
      "ds_read_b64_tr_b16 v[78:79], v221 offset:3072\n"
      "ds_read_b64_tr_b16 v[80:81], v224 offset:4096\n"
      "ds_read_b64_tr_b16 v[82:83], v221 offset:4096\n"
      "ds_read_b64_tr_b16 v[84:85], v224 offset:5120\n"
      "ds_read_b64_tr_b16 v[86:87], v221 offset:5120\n"
      "s_waitcnt lgkmcnt(12)\n"
      "ds_read_b64_tr_b16 v[88:89], v224 offset:6144\n"
      "ds_read_b64_tr_b16 v[90:91], v221 offset:6144\n"
      "s_waitcnt lgkmcnt(12)\n"
      "ds_read_b64_tr_b16 v[92:93], v224 offset:7168\n"
      "ds_read_b64_tr_b16 v[94:95], v221 offset:7168\n"
      "s_waitcnt lgkmcnt(12)\n"
      "ds_read_b64_tr_b16 v[96:97], v224 offset:8192\n"
      "ds_read_b64_tr_b16 v[98:99], v221 offset:8192\n"
      "s_waitcnt lgkmcnt(12)\n"
      "ds_read_b64_tr_b16 v[100:101], v224 offset:9216\n"
      "ds_read_b64_tr_b16 v[102:103], v221 offset:9216\n"
      "s_waitcnt lgkmcnt(12)\n"
      "ds_read_b64_tr_b16 v[104:105], v224 offset:10240\n"
      "ds_read_b64_tr_b16 v[106:107], v221 offset:10240\n"
      "s_waitcnt lgkmcnt(12)\n"
      "ds_read_b64_tr_b16 v[108:109], v224 offset:11264\n"
      "ds_read_b64_tr_b16 v[110:111], v221 offset:11264\n"
      "s_waitcnt lgkmcnt(12)\n"
      "ds_read_b64_tr_b16 v[112:113], v224 offset:12288\n"
      "ds_read_b64_tr_b16 v[114:115], v221 offset:12288\n"
      "s_waitcnt lgkmcnt(12)\n"
      "ds_read_b64_tr_b16 v[116:117], v224 offset:13312\n"
      "ds_read_b64_tr_b16 v[118:119], v221 offset:13312\n"
      "s_waitcnt lgkmcnt(12)\n"
      "ds_read_b64_tr_b16 v[120:121], v224 offset:14336\n"
      "ds_read_b64_tr_b16 v[122:123], v221 offset:14336\n"
      "s_waitcnt lgkmcnt(12)\n"
      "ds_read_b64_tr_b16 v[124:125], v224 offset:15360\n"
      "ds_read_b64_tr_b16 v[126:127], v221 offset:15360\n"
      "s_waitcnt lgkmcnt(6)\n"
      "ds_write_b32 v52, v53 offset:144\n"
      "s_add_u32 s75, s75, 49152\n"
      "s_and_b32 s75, s75, 0xffff\n"
      "buffer_load_dwordx4 v[16:19], v54, s[28:31], s75 offen offset:0\n"
      "buffer_load_dwordx4 v[20:23], v54, s[28:31], s75 offen offset:1024\n"
      "buffer_load_dwordx4 v[24:27], v54, s[28:31], s75 offen offset:2048\n"
      "buffer_load_dwordx4 v[28:31], v54, s[28:31], s75 offen offset:3072\n"
      "s_mov_b32 s66, 0\n"
      "Ll1_1:\n"
      "ds_read_b32 v0, v52 offset:112\n"
      "ds_read_b128 v[128:131], v228 offset:0\n"
      "ds_read_b128 v[132:135], v229 offset:0\n"
      "ds_read_b128 v[136:139], v230 offset:0\n"
      "ds_read_b128 v[140:143], v231 offset:0\n"
      "s_waitcnt lgkmcnt(4)\n"
      "v_cmp_eq_u32 vcc, 0, v0\n"
      "s_cbranch_vccz Ll1d_2\n"
      "s_waitcnt lgkmcnt(0)\n"
      "s_add_u32 s66, s66, 1\n"
      "s_cmp_lt_u32 s66, 131072\n"
      "s_cbranch_scc1 Ll1_1\n"
      "Ll1d_2:\n"
      "ds_read_b128 v[144:147], v228 offset:256\n"
      "ds_read_b128 v[148:151], v229 offset:256\n"
      "ds_read_b128 v[152:155], v230 offset:256\n"
      "ds_read_b128 v[156:159], v231 offset:256\n"
      "ds_read_b128 v[160:163], v228 offset:512\n"
      "ds_read_b128 v[164:167], v229 offset:512\n"
      "ds_read_b128 v[168:171], v230 offset:512\n"
      "ds_read_b128 v[172:175], v231 offset:512\n"
      "s_bitcmp1_b32 s53, 2\n"
      "s_cbranch_scc1 LKV_V\n"
      "s_waitcnt lgkmcnt(11)\n"
      "ds_read_b128 v[176:179], v228 offset:768\n"
      "v_mfma_f32_16x16x32_bf16 v[0:3], v[64:67], v[128:131], 0\n"
      "s_waitcnt lgkmcnt(11)\n"
      "ds_read_b128 v[180:183], v229 offset:768\n"
      "v_mfma_f32_16x16x32_bf16 v[0:3], v[68:71], v[132:135], v[0:3]\n"
      "s_waitcnt lgkmcnt(11)\n"
      "ds_read_b128 v[184:187], v230 offset:768\n"
      "v_mfma_f32_16x16x32_bf16 v[0:3], v[72:75], v[136:139], v[0:3]\n"
      "s_waitcnt lgkmcnt(11)\n"
      "ds_read_b128 v[188:191], v231 offset:768\n"
      "v_mfma_f32_16x16x32_bf16 v[0:3], v[76:79], v[140:143], v[0:3]\n"
      "s_waitcnt lgkmcnt(11)\n"
      "v_mfma_f32_16x16x32_bf16 v[0:3], v[80:83], v[144:147], v[0:3]\n"
      "s_waitcnt lgkmcnt(10)\n"
      "v_mfma_f32_16x16x32_bf16 v[0:3], v[84:87], v[148:151], v[0:3]\n"
      "s_waitcnt lgkmcnt(9)\n"
      "v_mfma_f32_16x16x32_bf16 v[0:3], v[88:91], v[152:155], v[0:3]\n"
      "s_waitcnt lgkmcnt(8)\n"
      "v_mfma_f32_16x16x32_bf16 v[0:3], v[92:95], v[156:159], v[0:3]\n"
      "s_waitcnt lgkmcnt(7)\n"
      "v_mfma_f32_16x16x32_bf16 v[0:3], v[96:99], v[160:163], v[0:3]\n"
      "s_waitcnt lgkmcnt(6)\n"
      "v_mfma_f32_16x16x32_bf16 v[0:3], v[100:103], v[164:167], v[0:3]\n"
      "s_waitcnt lgkmcnt(5)\n"
      "v_mfma_f32_16x16x32_bf16 v[0:3], v[104:107], v[168:171], v[0:3]\n"
      "s_waitcnt lgkmcnt(4)\n"
      "v_mfma_f32_16x16x32_bf16 v[0:3], v[108:111], v[172:175], v[0:3]\n"
      "s_waitcnt lgkmcnt(3)\n"
      "v_mfma_f32_16x16x32_bf16 v[0:3], v[112:115], v[176:179], v[0:3]\n"
      "s_waitcnt lgkmcnt(2)\n"
      "v_mfma_f32_16x16x32_bf16 v[0:3], v[116:119], v[180:183], v[0:3]\n"
      "s_waitcnt lgkmcnt(1)\n"
      "v_mfma_f32_16x16x32_bf16 v[0:3], v[120:123], v[184:187], v[0:3]\n"
      "s_waitcnt lgkmcnt(0)\n"
      "v_mfma_f32_16x16x32_bf16 v[0:3], v[124:127], v[188:191], v[0:3]\n"
      "s_nop 6\n"
      "s_branch LKV_E\n"
      "LKV_V:\n"
      "s_waitcnt lgkmcnt(11)\n"
      "ds_read_b128 v[176:179], v228 offset:768\n"
      "v_mfma_f32_16x16x32_bf16 v[0:3], v[128:131], v[64:67], 0\n"
      "s_waitcnt lgkmcnt(11)\n"
      "ds_read_b128 v[180:183], v229 offset:768\n"
      "v_mfma_f32_16x16x32_bf16 v[0:3], v[132:135], v[68:71], v[0:3]\n"
      "s_waitcnt lgkmcnt(11)\n"
      "ds_read_b128 v[184:187], v230 offset:768\n"
      "v_mfma_f32_16x16x32_bf16 v[0:3], v[136:139], v[72:75], v[0:3]\n"
      "s_waitcnt lgkmcnt(11)\n"
      "ds_read_b128 v[188:191], v231 offset:768\n"
      "v_mfma_f32_16x16x32_bf16 v[0:3], v[140:143], v[76:79], v[0:3]\n"
      "s_waitcnt lgkmcnt(11)\n"
      "v_mfma_f32_16x16x32_bf16 v[0:3], v[144:147], v[80:83], v[0:3]\n"
      "s_waitcnt lgkmcnt(10)\n"
      "v_mfma_f32_16x16x32_bf16 v[0:3], v[148:151], v[84:87], v[0:3]\n"
      "s_waitcnt lgkmcnt(9)\n"
      "v_mfma_f32_16x16x32_bf16 v[0:3], v[152:155], v[88:91], v[0:3]\n"
      "s_waitcnt lgkmcnt(8)\n"
      "v_mfma_f32_16x16x32_bf16 v[0:3], v[156:159], v[92:95], v[0:3]\n"
      "s_waitcnt lgkmcnt(7)\n"
      "v_mfma_f32_16x16x32_bf16 v[0:3], v[160:163], v[96:99], v[0:3]\n"
      "s_waitcnt lgkmcnt(6)\n"
      "v_mfma_f32_16x16x32_bf16 v[0:3], v[164:167], v[100:103], v[0:3]\n"
      "s_waitcnt lgkmcnt(5)\n"
      "v_mfma_f32_16x16x32_bf16 v[0:3], v[168:171], v[104:107], v[0:3]\n"
      "s_waitcnt lgkmcnt(4)\n"
      "v_mfma_f32_16x16x32_bf16 v[0:3], v[172:175], v[108:111], v[0:3]\n"
      "s_waitcnt lgkmcnt(3)\n"
      "v_mfma_f32_16x16x32_bf16 v[0:3], v[176:179], v[112:115], v[0:3]\n"
      "s_waitcnt lgkmcnt(2)\n"
      "v_mfma_f32_16x16x32_bf16 v[0:3], v[180:183], v[116:119], v[0:3]\n"
      "s_waitcnt lgkmcnt(1)\n"
      "v_mfma_f32_16x16x32_bf16 v[0:3], v[184:187], v[120:123], v[0:3]\n"
      "s_waitcnt lgkmcnt(0)\n"
      "v_mfma_f32_16x16x32_bf16 v[0:3], v[188:191], v[124:127], v[0:3]\n"
      "s_nop 7\n"
      "LKV_E:\n"
      "v_cvt_pk_bf16_f32 v4, v0, v1\n"
      "v_cvt_pk_bf16_f32 v5, v2, v3\n"
      "buffer_store_dwordx2 v[4:5], v225, s[32:35], 0 offen sc1\n"
      "s_waitcnt vmcnt(5)\n"
      "v_add_co_u32 v222, vcc, 0x10000, v222\n"
      "s_nop 1\n"
      "v_addc_co_u32 v223, vcc, 0, v223, vcc\n"
      "v_and_b32 v222, 0xffff0000, v222\n"
      "v_mov_b32 v227, 162304\n"
      "ds_write_b64 v227, v[222:223]\n"
      "v_readfirstlane_b32 s70, v222\n"
      "v_readfirstlane_b32 s71, v223\n"
      "v_mul_f32 v6, v0, v0\n"
      "v_fmac_f32 v6, v1, v1\n"
      "v_fmac_f32 v6, v2, v2\n"
      "v_fmac_f32 v6, v3, v3\n"
      "v_mov_b32 v7, v6\n"
      "s_nop 1\n"
      "v_permlane32_swap_b32 v6, v7\n"
      "v_add_f32 v6, v6, v7\n"
      "v_mov_b32 v7, v6\n"
      "s_nop 1\n"
      "v_permlane16_swap_b32 v6, v7\n"
      "v_add_f32 v6, v6, v7\n"
      "v_min_u32 v6, 0x7f800000, v6\n"
      "s_nop 1\n"
      "v_max_u32_dpp v6, v6, v6 row_ror:8 row_mask:0xf bank_mask:0xf\n"
      "s_nop 1\n"
      "v_max_u32_dpp v6, v6, v6 row_ror:4 row_mask:0xf bank_mask:0xf\n"
      "s_nop 1\n"
      "v_max_u32_dpp v6, v6, v6 row_ror:2 row_mask:0xf bank_mask:0xf\n"
      "s_nop 1\n"
      "v_max_u32_dpp v6, v6, v6 row_ror:1 row_mask:0xf bank_mask:0xf\n"
      "v_add_u32 v6, 0xffff, v6\n"
      "v_lshrrev_b32 v6, 16, v6\n"
      "s_waitcnt vmcnt(0)\n"
      "v_or_b32 v0, s70, v6\n"
      "v_mov_b32 v1, s71\n"
      "s_mov_b64 exec, 1\n"
      "buffer_store_dwordx2 v[0:1], v226, s[32:35], 0 offen sc1\n"
      "s_mov_b64 exec, -1\n"
      "s_setprio 0\n"
      "buffer_load_dwordx2 v[40:41], v48, s[40:43], s69 offen offset:0\n"
      "buffer_load_dwordx2 v[42:43], v48, s[40:43], s69 offen offset:32\n"
      "buffer_load_dwordx2 v[44:45], v48, s[40:43], s15 offen offset:0\n"
      "buffer_load_dwordx2 v[46:47], v48, s[40:43], s15 offen offset:32\n"
      "s_mov_b32 m0, s68\n"
      "s_nop 0\n"
      "buffer_load_dwordx4 v32, s[36:39], s67 offen offset:0 lds\n"
      "buffer_load_dwordx4 v33, s[36:39], s67 offen offset:1024 lds\n"
      "s_add_u32 s67, s67, 114688\n"
      "s_cmp_ge_u32 s67, 1835008\n"
      "s_cselect_b32 s67, s69, s67\n"
      "s_add_u32 m0, s68, 2048\n"
      "s_nop 0\n"
      "buffer_load_dwordx4 v32, s[36:39], s67 offen offset:0 lds\n"
      "buffer_load_dwordx4 v33, s[36:39], s67 offen offset:1024 lds\n"
      "s_add_u32 s67, s67, 114688\n"
      "s_cmp_ge_u32 s67, 1835008\n"
      "s_cselect_b32 s67, s69, s67\n"
      "s_add_u32 m0, s68, 4096\n"
      "s_nop 0\n"
      "buffer_load_dwordx4 v32, s[36:39], s67 offen offset:0 lds\n"
      "buffer_load_dwordx4 v33, s[36:39], s67 offen offset:1024 lds\n"
      "s_add_u32 s67, s67, 114688\n"
      "s_cmp_ge_u32 s67, 1835008\n"
      "s_cselect_b32 s67, s69, s67\n"
      "v_dot2_f32_bf16 v68, v16, v16, 0\n"
      "v_dot2_f32_bf16 v68, v17, v17, v68\n"
      "v_dot2_f32_bf16 v68, v18, v18, v68\n"
      "v_dot2_f32_bf16 v68, v19, v19, v68\n"
      "v_dot2_f32_bf16 v69, v20, v20, 0\n"
      "v_dot2_f32_bf16 v69, v21, v21, v69\n"
      "v_dot2_f32_bf16 v69, v22, v22, v69\n"
      "v_dot2_f32_bf16 v69, v23, v23, v69\n"
      "v_dot2_f32_bf16 v70, v24, v24, 0\n"
      "v_dot2_f32_bf16 v70, v25, v25, v70\n"
      "v_dot2_f32_bf16 v70, v26, v26, v70\n"
      "v_dot2_f32_bf16 v70, v27, v27, v70\n"
      "v_dot2_f32_bf16 v71, v28, v28, 0\n"
      "v_dot2_f32_bf16 v71, v29, v29, v71\n"
      "v_dot2_f32_bf16 v71, v30, v30, v71\n"
      "v_dot2_f32_bf16 v71, v31, v31, v71\n"
      "v_mfma_f32_16x16x32_bf16 v[64:67], v[192:195], v[16:19], 0\n"
      "v_permlane32_swap_b32 v68, v70\n"
      "v_mfma_f32_16x16x32_bf16 v[64:67], v[196:199], v[20:23], v[64:67]\n"
      "v_permlane32_swap_b32 v69, v71\n"
      "v_mfma_f32_16x16x32_bf16 v[64:67], v[200:203], v[24:27], v[64:67]\n"
      "v_add_f32 v68, v68, v70\n"
      "v_add_f32 v69, v69, v71\n"
      "v_mfma_f32_16x16x32_bf16 v[64:67], v[204:207], v[28:31], v[64:67]\n"
      "v_lshlrev_b32 v72, 16, v16\n"
      "v_permlane16_swap_b32 v68, v69\n"
      "v_add_f32 v68, v68, v69\n"
      "v_and_b32 v73, s65, v16\n"
      "v_lshlrev_b32 v74, 16, v17\n"
      "v_add_f32_dpp v68, v68, v68 row_ror:8 row_mask:0xf bank_mask:0xf\n"
      "v_and_b32 v75, s65, v17\n"
      "v_lshlrev_b32 v76, 16, v18\n"
      "v_add_f32_dpp v64, v64, v64 row_ror:8 row_mask:0xf bank_mask:0xf\n"
      "v_add_f32_dpp v68, v68, v68 row_ror:4 row_mask:0xf bank_mask:0xf\n"
      "v_and_b32 v77, s65, v18\n"
      "v_add_f32_dpp v64, v64, v64 row_ror:4 row_mask:0xf bank_mask:0xf\n"
      "v_add_f32_dpp v68, v68, v68 row_ror:2 row_mask:0xf bank_mask:0xf\n"
      "v_lshlrev_b32 v78, 16, v19\n"
      "v_add_f32_dpp v64, v64, v64 row_ror:2 row_mask:0xf bank_mask:0xf\n"
      "v_add_f32_dpp v68, v68, v68 row_ror:1 row_mask:0xf bank_mask:0xf\n"
      "v_and_b32 v79, s65, v19\n"
      "v_add_f32_dpp v64, v64, v64 row_ror:1 row_mask:0xf bank_mask:0xf\n"
      "v_mul_f32 v64, 0x3b000000, v64\n"
      "v_mul_f32 v68, 0x3b000000, v68\n"
      "v_fma_f32 v68, -v64, v64, v68\n"
      "v_max_f32 v68, 0, v68\n"
      "v_fmaak_f32 v68, v49, v68, 0x36a1369a\n"
      "v_rsq_f32 v68, v68\n"
      "v_lshlrev_b32 v80, 16, v20\n"
      "v_mul_f32 v64, -v64, v68\n"
      "v_readlane_b32 s58, v68, 0\n"
      "v_readlane_b32 s59, v64, 0\n"
      "v_readlane_b32 s60, v68, 16\n"
      "v_readlane_b32 s61, v64, 16\n"
      "v_readlane_b32 s62, v68, 32\n"
      "v_readlane_b32 s63, v64, 32\n"
      "v_readlane_b32 s72, v68, 48\n"
      "v_readlane_b32 s73, v64, 48\n"
      "v_and_b32 v81, s65, v20\n"
      "v_lshlrev_b32 v82, 16, v21\n"
      "v_and_b32 v83, s65, v21\n"
      "v_lshlrev_b32 v84, 16, v22\n"
      "v_and_b32 v85, s65, v22\n"
      "v_lshlrev_b32 v86, 16, v23\n"
      "v_and_b32 v87, s65, v23\n"
      "v_pk_fma_f32 v[72:73], v[72:73], s[58:59], s[58:59] op_sel:[0,0,1] op_sel_hi:[1,0,1]\n"
      "v_pk_fma_f32 v[74:75], v[74:75], s[58:59], s[58:59] op_sel:[0,0,1] op_sel_hi:[1,0,1]\n"
      "v_pk_fma_f32 v[76:77], v[76:77], s[58:59], s[58:59] op_sel:[0,0,1] op_sel_hi:[1,0,1]\n"
      "v_pk_fma_f32 v[78:79], v[78:79], s[58:59], s[58:59] op_sel:[0,0,1] op_sel_hi:[1,0,1]\n"
      "v_cvt_pk_bf16_f32 v16, v72, v73\n"
      "v_cvt_pk_bf16_f32 v17, v74, v75\n"
      "v_cvt_pk_bf16_f32 v18, v76, v77\n"
      "v_cvt_pk_bf16_f32 v19, v78, v79\n"
      "ds_write_b128 v56, v[16:19]\n"
      "v_pk_fma_f32 v[80:81], v[80:81], s[60:61], s[60:61] op_sel:[0,0,1] op_sel_hi:[1,0,1]\n"
      "v_pk_fma_f32 v[82:83], v[82:83], s[60:61], s[60:61] op_sel:[0,0,1] op_sel_hi:[1,0,1]\n"
      "v_pk_fma_f32 v[84:85], v[84:85], s[60:61], s[60:61] op_sel:[0,0,1] op_sel_hi:[1,0,1]\n"
      "v_pk_fma_f32 v[86:87], v[86:87], s[60:61], s[60:61] op_sel:[0,0,1] op_sel_hi:[1,0,1]\n"
      "v_cvt_pk_bf16_f32 v20, v80, v81\n"
      "v_cvt_pk_bf16_f32 v21, v82, v83\n"
      "v_cvt_pk_bf16_f32 v22, v84, v85\n"
      "v_cvt_pk_bf16_f32 v23, v86, v87\n"
      "ds_write_b128 v57, v[20:23]\n"
      "v_lshlrev_b32 v72, 16, v24\n"
      "v_and_b32 v73, s65, v24\n"
      "v_lshlrev_b32 v74, 16, v25\n"
      "v_and_b32 v75, s65, v25\n"
      "v_lshlrev_b32 v76, 16, v26\n"
      "v_and_b32 v77, s65, v26\n"
      "v_lshlrev_b32 v78, 16, v27\n"
      "v_and_b32 v79, s65, v27\n"
      "v_pk_fma_f32 v[72:73], v[72:73], s[62:63], s[62:63] op_sel:[0,0,1] op_sel_hi:[1,0,1]\n"
      "v_pk_fma_f32 v[74:75], v[74:75], s[62:63], s[62:63] op_sel:[0,0,1] op_sel_hi:[1,0,1]\n"
      "v_pk_fma_f32 v[76:77], v[76:77], s[62:63], s[62:63] op_sel:[0,0,1] op_sel_hi:[1,0,1]\n"
      "v_pk_fma_f32 v[78:79], v[78:79], s[62:63], s[62:63] op_sel:[0,0,1] op_sel_hi:[1,0,1]\n"
      "v_cvt_pk_bf16_f32 v24, v72, v73\n"
      "v_cvt_pk_bf16_f32 v25, v74, v75\n"
      "v_cvt_pk_bf16_f32 v26, v76, v77\n"
      "v_cvt_pk_bf16_f32 v27, v78, v79\n"
      "ds_write_b128 v58, v[24:27]\n"
      "v_lshlrev_b32 v80, 16, v28\n"
      "v_and_b32 v81, s65, v28\n"
      "v_lshlrev_b32 v82, 16, v29\n"
      "v_and_b32 v83, s65, v29\n"
      "v_lshlrev_b32 v84, 16, v30\n"
      "v_and_b32 v85, s65, v30\n"
      "v_lshlrev_b32 v86, 16, v31\n"
      "v_and_b32 v87, s65, v31\n"
      "v_pk_fma_f32 v[80:81], v[80:81], s[72:73], s[72:73] op_sel:[0,0,1] op_sel_hi:[1,0,1]\n"
      "v_pk_fma_f32 v[82:83], v[82:83], s[72:73], s[72:73] op_sel:[0,0,1] op_sel_hi:[1,0,1]\n"
      "v_pk_fma_f32 v[84:85], v[84:85], s[72:73], s[72:73] op_sel:[0,0,1] op_sel_hi:[1,0,1]\n"
      "v_pk_fma_f32 v[86:87], v[86:87], s[72:73], s[72:73] op_sel:[0,0,1] op_sel_hi:[1,0,1]\n"
      "v_cvt_pk_bf16_f32 v28, v80, v81\n"
      "v_cvt_pk_bf16_f32 v29, v82, v83\n"
      "v_cvt_pk_bf16_f32 v30, v84, v85\n"
      "v_cvt_pk_bf16_f32 v31, v86, v87\n"
      "ds_write_b128 v59, v[28:31]\n"
      "s_waitcnt lgkmcnt(0)\n"
      "s_barrier\n"
      "s_setprio 0\n"
      "s_mov_b32 s62, 129024\n"
      "v_lshrrev_b32 v64, 3, v244\n"
      "v_and_b32 v65, 6, v64\n"
      "v_mul_u32_u24 v64, 1024, v64\n"
      "v_and_b32 v66, 7, v244\n"
      "v_xor_b32 v65, v65, v66\n"
      "v_lshl_add_u32 v232, v65, 4, v64\n"
      "v_mov_b32 v233, v232\n"
      "v_add_u32 v234, 14336, v232\n"
      "v_add_u32 v235, 21504, v233\n"
      "v_add_u32 v233, 7168, v233\n"
      "v_lshrrev_b32 v64, 4, v244\n"
      "v_bfe_u32 v65, v244, 2, 2\n"
      "v_and_b32 v66, 3, v244\n"
      "v_lshlrev_b32 v66, 3, v66\n"
      "v_lshl_add_u32 v236, v64, 2, v65\n"
      "v_lshl_or_b32 v236, v236, 7, v66\n"
      "v_and_b32 v64, 1, v64\n"
      "v_lshrrev_b32 v65, 1, v65\n"
      "v_lshl_or_b32 v64, v64, 1, v65\n"
      "v_lshl_or_b32 v236, v64, 5, v236\n"
      "v_add_u32 v236, s62, v236\n"
      "v_xor_b32 v237, 32, v236\n"
      "v_xor_b32 v238, 64, v236\n"
      "v_xor_b32 v239, 96, v236\n"
      "v_and_b32 v246, 15, v244\n"
      "v_lshlrev_b32 v246, 2, v246\n"
      "v_bfe_u32 v64, v244, 4, 1\n"
      "v_lshl_or_b32 v245, v64, 7, v246\n"
      "v_lshrrev_b32 v64, 5, v244\n"
      "v_lshl_or_b32 v245, v64, 10, v245\n"
      "v_lshlrev_b32 v247, 4, v244\n"
      "v_and_b32 v254, 3, v244\n"
      "v_lshlrev_b32 v254, 2, v254\n"
      "v_add_u32 v254, 161792, v254\n"
      "v_and_b32 v64, 15, v244\n"
      "v_lshrrev_b32 v65, 4, v244\n"
      "v_lshlrev_b32 v252, 10, v244\n"
      "v_lshlrev_b32 v240, 10, v65\n"
      "v_lshl_or_b32 v240, v64, 3, v240\n"
      "s_lshl_b32 s60, s56, 12\n"
      "s_lshl_b32 s61, s53, 7\n"
      "s_add_u32 s61, s60, s61\n"
      "v_add_u32 v240, s61, v240\n"
      "v_xor_b32 v66, v64, v65\n"
      "v_lshlrev_b32 v66, 4, v66\n"
      "v_lshl_or_b32 v66, v65, 8, v66\n"
      "s_add_u32 s60, s60, 0\n"
      "v_add_u32 v248, s60, v66\n"
      "v_xor_b32 v249, 64, v248\n"
      "v_xor_b32 v250, 128, v248\n"
      "v_xor_b32 v251, 192, v248\n"
      "s_lshl_b32 s60, s56, 2\n"
      "v_add_u32 v65, s60, v65\n"
      "v_xor_b32 v65, v65, v64\n"
      "v_lshlrev_b32 v65, 4, v65\n"
      "v_lshl_or_b32 v65, v64, 8, v65\n"
      "v_add_u32 v241, 0, v65\n"
      "s_lshl_b32 s60, s52, 12\n"
      "s_add_u32 s60, s60, 65536\n"
      "v_and_b32 v64, 31, v244\n"
      "v_lshl_add_u32 v217, v64, 6, s60\n"
      "v_lshrrev_b32 v65, 3, v64\n"
      "v_and_b32 v65, 2, v65\n"
      "v_bfi_b32 v64, v65, 0, v64\n"
      "s_add_u32 s60, s60, 2048\n"
      "v_lshl_add_u32 v216, v64, 6, s60\n"
      "s_lshl_b32 s60, s52, 16\n"
      "s_add_u32 s48, s24, s60\n"
      "s_addc_u32 s49, s25, 0\n"
      "s_and_b32 s49, s49, 0xffff\n"
      "s_mov_b32 s50, 65536\n"
      "s_mov_b32 s51, 0x20000\n"
      "s_lshl_b32 s60, s53, 7\n"
      "s_sub_u32 s61, 1835008, s60\n"
      "s_add_u32 s8, s22, s60\n"
      "s_addc_u32 s9, s23, 0\n"
      "s_and_b32 s9, s9, 0xffff\n"
      "s_mov_b32 s10, s61\n"
      "s_mov_b32 s11, 0x20000\n"
      "s_sub_u32 s61, 524288, s60\n"
      "s_add_u32 s24, s16, s60\n"
      "s_addc_u32 s25, s17, 0\n"
      "s_and_b32 s25, s25, 0xffff\n"
      "s_mov_b32 s26, s61\n"
      "s_mov_b32 s27, 0x20000\n"
      "s_lshl_b32 s63, s56, 3\n"
      "s_or_b32 s63, s63, 0x80000\n"
      "s_mov_b32 s76, 0x3020100\n"
      "s_bitcmp1_b32 s53, 2\n"
      "s_cselect_b32 s76, 0x10906, s76\n"
      "s_bfe_u32 s76, s76, s63\n"
      "v_lshl_add_u32 v253, s76, 4, v254\n"
      "s_mov_b32 s21, 0x14141414\n"
      "s_bitcmp1_b32 s53, 2\n"
      "s_cselect_b32 s21, 0x26260202, s21\n"
      "s_bfe_u32 s21, s21, s63\n"
      "s_mov_b32 s58, 0x8080808\n"
      "s_bitcmp1_b32 s53, 2\n"
      "s_cselect_b32 s58, 0xa0a0606, s58\n"
      "s_bfe_u32 s58, s58, s63\n"
      "s_mov_b32 s59, 0x6060606\n"
      "s_bitcmp1_b32 s53, 2\n"
      "s_cselect_b32 s59, 0x8080404, s59\n"
      "s_bfe_u32 s59, s59, s63\n"
      "s_lshl_b32 s60, s55, 1\n"
      "s_lshr_b32 s61, s100, 1\n"
      "s_add_u32 s60, s60, s61\n"
      "s_and_b32 s60, s60, 7\n"
      "s_mul_i32 s61, s60, s59\n"
      "s_lshr_b32 s61, s61, 3\n"
      "s_sub_u32 s61, s61, s58\n"
      "s_mul_i32 s60, s60, s58\n"
      "s_lshr_b32 s60, s60, 3\n"
      "v_add_u32 v64, s60, v244\n"
      "v_subrev_u32 v65, s58, v64\n"
      "v_min_u32 v64, v64, v65\n"
      "v_add_u32 v65, s61, v244\n"
      "v_subrev_u32 v66, s59, v65\n"
      "v_min_u32 v65, v65, v66\n"
      "v_cmp_gt_u32 vcc, s58, v244\n"
      "s_mov_b32 s60, 0x18100800\n"
      "s_bitcmp1_b32 s53, 2\n"
      "s_cselect_b32 s60, 0x160c0600, s60\n"
      "s_bfe_u32 s60, s60, s63\n"
      "s_mov_b32 s61, 0x120c0600\n"
      "s_bitcmp1_b32 s53, 2\n"
      "s_cselect_b32 s61, 0x10080400, s61\n"
      "s_bfe_u32 s61, s61, s63\n"
      "v_add_u32 v64, s60, v64\n"
      "v_add_u32 v65, s61, v65\n"
      "v_and_b32 v66, -4, v64\n"
      "v_lshrrev_b32 v67, 1, v66\n"
      "v_add_u32 v67, 8, v67\n"
      "v_min_u32 v66, v66, v67\n"
      "v_add_u32 v64, v64, v66\n"
      "v_and_b32 v66, -4, v65\n"
      "v_and_b32 v67, -2, v65\n"
      "v_lshlrev_b32 v67, 1, v67\n"
      "v_subrev_u32 v67, 16, v67\n"
      "v_max_i32 v66, v66, v67\n"
      "v_add3_u32 v65, v65, v66, 4\n"
      "v_cndmask_b32 v64, v65, v64, vcc\n"
      "v_and_b32 v64, 63, v64\n"
      "v_lshlrev_b32 v65, 3, v244\n"
      "v_and_b32 v65, 24, v65\n"
      "v_lshlrev_b32 v64, v65, v64\n"
      "s_nop 1\n"
      "v_or_b32_dpp v64, v64, v64 quad_perm:[1,0,3,2] row_mask:0xf bank_mask:0xf\n"
      "s_nop 1\n"
      "v_or_b32_dpp v64, v64, v64 quad_perm:[2,3,0,1] row_mask:0xf bank_mask:0xf\n"
      "s_nop 0\n"
      "v_readlane_b32 s16, v64, 0\n"
      "v_readlane_b32 s17, v64, 4\n"
      "v_readlane_b32 s18, v64, 8\n"
      "v_readlane_b32 s19, v64, 12\n"
      "v_readlane_b32 s20, v64, 16\n"
      "s_branch LF1PRE\n"
      "LHELP:\n"
      "s_setprio 1\n"
      "v_lshrrev_b32 v64, 3, v244\n"
      "v_lshrrev_b32 v65, 2, v244\n"
      "v_and_b32 v65, 4, v65\n"
      "v_and_b32 v66, 7, v244\n"
      "v_xor_b32 v65, v65, v66\n"
      "v_lshlrev_b32 v65, 4, v65\n"
      "v_lshl_or_b32 v60, v64, 10, v65\n"
      "v_add_u32 v61, 7168, v60\n"
      "s_lshl_b32 s60, s53, 7\n"
      "s_sub_u32 s61, 524288, s60\n"
      "s_add_u32 s44, s10, s60\n"
      "s_addc_u32 s45, s11, 0\n"
      "s_and_b32 s45, s45, 0xffff\n"
      "s_mov_b32 s46, s61\n"
      "s_mov_b32 s47, 0x20000\n"
      "s_mov_b32 s12, s56\n"
      "s_barrier\n"
      "s_lshl_b32 s13, s12, 11\n"
      "s_add_u32 m0, s13, 65536\n"
      "s_lshl1_add_u32 s13, s100, s12\n"
      "s_and_b32 s13, s13, 31\n"
      "s_lshl_b32 s13, s13, 14\n"
      "buffer_load_dwordx4 v60, s[44:47], s13 offen offset:0 lds\n"
      "buffer_load_dwordx4 v61, s[44:47], s13 offen offset:1024 lds\n"
      "s_add_u32 s12, s12, 3\n"
      "s_lshl_b32 s13, s12, 11\n"
      "s_add_u32 m0, s13, 65536\n"
      "s_lshl1_add_u32 s13, s100, s12\n"
      "s_and_b32 s13, s13, 31\n"
      "s_lshl_b32 s13, s13, 14\n"
      "buffer_load_dwordx4 v60, s[44:47], s13 offen offset:0 lds\n"
      "buffer_load_dwordx4 v61, s[44:47], s13 offen offset:1024 lds\n"
      "s_mov_b32 s32, s26\n"
      "s_and_b32 s33, s27, 0xffff\n"
      "s_mov_b32 s34, 12582912\n"
      "s_mov_b32 s35, 0x20000\n"
      "s_lshl_b32 s58, s56, 2\n"
      "v_xor_b32 v64, s58, v244\n"
      "v_lshlrev_b32 v64, 4, v64\n"
      "s_lshl_b32 s59, s56, 12\n"
      "s_add_u32 s58, s55, 3\n"
      "s_and_b32 s58, s58, 3\n"
      "s_lshl_b32 s58, s58, 14\n"
      "s_or_b32 s58, s58, s59\n"
      "v_or_b32 v56, s58, v64\n"
      "v_xor_b32 v57, 1040, v56\n"
      "v_xor_b32 v58, 2080, v56\n"
      "v_xor_b32 v59, 3120, v56\n"
      "v_mov_b32 v64, s64\n"
      "s_mov_b32 vcc_lo, 0x10001\n"
      "s_mov_b32 vcc_hi, 0x10001\n"
      "v_cndmask_b32 v192, 0, v64, vcc\n"
      "v_cndmask_b32 v193, 0, v64, vcc\n"
      "v_mov_b64 v[194:195], v[192:193]\n"
      "s_mov_b32 vcc_lo, 0x100010\n"
      "s_mov_b32 vcc_hi, 0x100010\n"
      "v_cndmask_b32 v196, 0, v64, vcc\n"
      "v_cndmask_b32 v197, 0, v64, vcc\n"
      "v_mov_b64 v[198:199], v[196:197]\n"
      "s_mov_b32 vcc_lo, 0x1000100\n"
      "s_mov_b32 vcc_hi, 0x1000100\n"
      "v_cndmask_b32 v200, 0, v64, vcc\n"
      "v_cndmask_b32 v201, 0, v64, vcc\n"
      "v_mov_b64 v[202:203], v[200:201]\n"
      "s_mov_b32 vcc_lo, 0x10001000\n"
      "s_mov_b32 vcc_hi, 0x10001000\n"
      "v_cndmask_b32 v204, 0, v64, vcc\n"
      "v_cndmask_b32 v205, 0, v64, vcc\n"
      "v_mov_b64 v[206:207], v[204:205]\n"
      "s_lshl_b32 s68, s56, 13\n"
      "s_add_u32 s68, s68, 137216\n"
      "s_mov_b32 s40, s20\n"
      "s_and_b32 s41, s21, 0xffff\n"
      "s_mov_b32 s42, 3584\n"
      "s_mov_b32 s43, 0x20000\n"
      "s_lshl_b32 s60, s53, 3\n"
      "s_mul_i32 s61, s53, 6\n"
      "s_add_u32 s61, s61, 8\n"
      "s_min_u32 s60, s60, s61\n"
      "s_add_u32 s69, s60, s56\n"
      "s_lshl_b32 s69, s69, 6\n"
      "s_and_b32 s61, s53, 4\n"
      "s_or_b32 s61, s61, s56\n"
      "s_cmp_ge_u32 s61, 6\n"
      "s_cselect_b32 s60, 0, 256\n"
      "s_cselect_b32 s61, 0x7ffffff0, 0\n"
      "s_add_u32 s15, s69, s60\n"
      "v_lshrrev_b32 v66, 2, v244\n"
      "v_mul_u32_u24 v66, 3584, v66\n"
      "v_lshrrev_b32 v67, 4, v244\n"
      "v_and_b32 v67, 2, v67\n"
      "v_and_b32 v68, 3, v244\n"
      "v_xor_b32 v67, v67, v68\n"
      "v_lshl_add_u32 v32, v67, 4, v66\n"
      "v_add_u32 v33, 56320, v32\n"
      "v_or_b32 v50, s61, v32\n"
      "v_or_b32 v51, s61, v33\n"
      "v_lshrrev_b32 v66, 4, v244\n"
      "v_bfe_u32 v67, v244, 2, 2\n"
      "v_and_b32 v68, 3, v244\n"
      "v_lshlrev_b32 v68, 3, v68\n"
      "v_lshl_add_u32 v34, v66, 3, v67\n"
      "v_lshl_or_b32 v34, v34, 6, v68\n"
      "v_and_b32 v66, 1, v66\n"
      "v_lshl_or_b32 v34, v66, 5, v34\n"
      "v_add_u32 v34, s68, v34\n"
      "v_xor_b32 v35, 32, v34\n"
      "v_and_b32 v66, 15, v244\n"
      "v_lshrrev_b32 v67, 4, v244\n"
      "v_xor_b32 v67, v67, v66\n"
      "v_lshlrev_b32 v67, 4, v67\n"
      "v_lshl_or_b32 v36, v66, 10, v67\n"
      "v_xor_b32 v37, 64, v36\n"
      "v_xor_b32 v38, 128, v36\n"
      "v_xor_b32 v39, 192, v36\n"
      "v_mov_b32 v210, 0x7ffffff0\n"
      "v_mov_b32 v211, 0x7ffffff0\n"
      "s_mov_b32 s36, s18\n"
      "s_and_b32 s37, s19, 0xffff\n"
      "s_mov_b32 s38, 1835008\n"
      "s_mov_b32 s39, 0x20000\n"
      "s_mul_i32 s67, s100, 114688\n"
      "s_add_u32 s67, s67, s69\n"
      "v_lshrrev_b32 v48, 4, v244\n"
      "v_lshlrev_b32 v48, 3, v48\n"
      "v_lshrrev_b32 v66, 3, v244\n"
      "v_and_b32 v67, 6, v66\n"
      "v_mul_u32_u24 v66, 1024, v66\n"
      "v_and_b32 v68, 7, v244\n"
      "v_xor_b32 v67, v67, v68\n"
      "v_lshl_add_u32 v232, v67, 4, v66\n"
      "v_mov_b32 v233, v232\n"
      "v_add_u32 v234, 14336, v232\n"
      "v_add_u32 v235, 21504, v233\n"
      "v_add_u32 v233, 7168, v233\n"
      "v_lshrrev_b32 v66, 4, v244\n"
      "v_bfe_u32 v67, v244, 2, 2\n"
      "v_and_b32 v68, 3, v244\n"
      "v_lshlrev_b32 v68, 3, v68\n"
      "v_lshl_add_u32 v236, v66, 2, v67\n"
      "v_lshl_or_b32 v236, v236, 7, v68\n"
      "v_and_b32 v66, 1, v66\n"
      "v_lshrrev_b32 v67, 1, v67\n"
      "v_lshl_or_b32 v66, v66, 1, v67\n"
      "v_lshl_or_b32 v236, v66, 5, v236\n"
      "v_add_u32 v236, s68, v236\n"
      "v_xor_b32 v237, 32, v236\n"
      "v_xor_b32 v238, 64, v236\n"
      "v_xor_b32 v239, 96, v236\n"
      "v_and_b32 v246, 15, v244\n"
      "v_lshlrev_b32 v246, 2, v246\n"
      "v_bfe_u32 v66, v244, 4, 1\n"
      "v_lshl_or_b32 v245, v66, 7, v246\n"
      "v_lshrrev_b32 v66, 5, v244\n"
      "v_lshl_or_b32 v245, v66, 10, v245\n"
      "v_lshlrev_b32 v247, 4, v244\n"
      "v_and_b32 v254, 3, v244\n"
      "v_lshlrev_b32 v254, 2, v254\n"
      "v_add_u32 v254, 161792, v254\n"
      "v_and_b32 v66, 15, v244\n"
      "v_lshrrev_b32 v67, 4, v244\n"
      "v_lshlrev_b32 v252, 10, v244\n"
      "v_lshlrev_b32 v240, 10, v67\n"
      "v_lshl_or_b32 v240, v66, 3, v240\n"
      "s_lshl_b32 s60, s56, 12\n"
      "s_lshl_b32 s61, s53, 7\n"
      "s_add_u32 s61, s60, s61\n"
      "v_add_u32 v240, s61, v240\n"
      "v_xor_b32 v68, v66, v67\n"
      "v_lshlrev_b32 v68, 4, v68\n"
      "v_lshl_or_b32 v68, v67, 8, v68\n"
      "s_add_u32 s60, s60, 0\n"
      "v_add_u32 v248, s60, v68\n"
      "v_xor_b32 v249, 64, v248\n"
      "v_xor_b32 v250, 128, v248\n"
      "v_xor_b32 v251, 192, v248\n"
      "s_lshl_b32 s60, s56, 2\n"
      "v_add_u32 v67, s60, v67\n"
      "v_xor_b32 v67, v67, v66\n"
      "v_lshlrev_b32 v67, 4, v67\n"
      "v_lshl_or_b32 v67, v66, 8, v67\n"
      "v_add_u32 v241, 0, v67\n"
      "s_lshl_b32 s60, s52, 12\n"
      "s_add_u32 s60, s60, 65536\n"
      "v_and_b32 v66, 31, v244\n"
      "v_lshl_add_u32 v217, v66, 6, s60\n"
      "v_lshrrev_b32 v67, 3, v66\n"
      "v_and_b32 v67, 2, v67\n"
      "v_bfi_b32 v66, v67, 0, v66\n"
      "s_add_u32 s60, s60, 2048\n"
      "v_lshl_add_u32 v216, v66, 6, s60\n"
      "s_lshl_b32 s60, s52, 16\n"
      "s_add_u32 s48, s24, s60\n"
      "s_addc_u32 s49, s25, 0\n"
      "s_and_b32 s49, s49, 0xffff\n"
      "s_mov_b32 s50, 65536\n"
      "s_mov_b32 s51, 0x20000\n"
      "s_lshl_b32 s60, s53, 7\n"
      "s_sub_u32 s61, 1835008, s60\n"
      "s_add_u32 s8, s22, s60\n"
      "s_addc_u32 s9, s23, 0\n"
      "s_and_b32 s9, s9, 0xffff\n"
      "s_mov_b32 s10, s61\n"
      "s_mov_b32 s11, 0x20000\n"
      "s_sub_u32 s61, 524288, s60\n"
      "s_add_u32 s24, s16, s60\n"
      "s_addc_u32 s25, s17, 0\n"
      "s_and_b32 s25, s25, 0xffff\n"
      "s_mov_b32 s26, s61\n"
      "s_mov_b32 s27, 0x20000\n"
      "s_lshl_b32 s63, s56, 3\n"
      "s_or_b32 s63, s63, 0x80000\n"
      "s_mov_b32 s76, 0x3020100\n"
      "s_bitcmp1_b32 s53, 2\n"
      "s_cselect_b32 s76, 0x10906, s76\n"
      "s_bfe_u32 s76, s76, s63\n"
      "v_lshl_add_u32 v253, s76, 4, v254\n"
      "s_mov_b32 s21, 0x14141414\n"
      "s_bitcmp1_b32 s53, 2\n"
      "s_cselect_b32 s21, 0x26260202, s21\n"
      "s_bfe_u32 s21, s21, s63\n"
      "s_mov_b32 s58, 0x8080808\n"
      "s_bitcmp1_b32 s53, 2\n"
      "s_cselect_b32 s58, 0xa0a0606, s58\n"
      "s_bfe_u32 s58, s58, s63\n"
      "s_mov_b32 s59, 0x6060606\n"
      "s_bitcmp1_b32 s53, 2\n"
      "s_cselect_b32 s59, 0x8080404, s59\n"
      "s_bfe_u32 s59, s59, s63\n"
      "s_lshl_b32 s60, s55, 1\n"
      "s_lshr_b32 s61, s100, 1\n"
      "s_add_u32 s60, s60, s61\n"
      "s_and_b32 s60, s60, 7\n"
      "s_mul_i32 s61, s60, s59\n"
      "s_lshr_b32 s61, s61, 3\n"
      "s_sub_u32 s61, s61, s58\n"
      "s_mul_i32 s60, s60, s58\n"
      "s_lshr_b32 s60, s60, 3\n"
      "v_add_u32 v66, s60, v244\n"
      "v_subrev_u32 v67, s58, v66\n"
      "v_min_u32 v66, v66, v67\n"
      "v_add_u32 v67, s61, v244\n"
      "v_subrev_u32 v68, s59, v67\n"
      "v_min_u32 v67, v67, v68\n"
      "v_cmp_gt_u32 vcc, s58, v244\n"
      "s_mov_b32 s60, 0x18100800\n"
      "s_bitcmp1_b32 s53, 2\n"
      "s_cselect_b32 s60, 0x160c0600, s60\n"
      "s_bfe_u32 s60, s60, s63\n"
      "s_mov_b32 s61, 0x120c0600\n"
      "s_bitcmp1_b32 s53, 2\n"
      "s_cselect_b32 s61, 0x10080400, s61\n"
      "s_bfe_u32 s61, s61, s63\n"
      "v_add_u32 v66, s60, v66\n"
      "v_add_u32 v67, s61, v67\n"
      "v_and_b32 v68, -4, v66\n"
      "v_lshrrev_b32 v69, 1, v68\n"
      "v_add_u32 v69, 8, v69\n"
      "v_min_u32 v68, v68, v69\n"
      "v_add_u32 v66, v66, v68\n"
      "v_and_b32 v68, -4, v67\n"
      "v_and_b32 v69, -2, v67\n"
      "v_lshlrev_b32 v69, 1, v69\n"
      "v_subrev_u32 v69, 16, v69\n"
      "v_max_i32 v68, v68, v69\n"
      "v_add3_u32 v67, v67, v68, 4\n"
      "v_cndmask_b32 v66, v67, v66, vcc\n"
      "v_and_b32 v66, 63, v66\n"
      "v_lshlrev_b32 v67, 3, v244\n"
      "v_and_b32 v67, 24, v67\n"
      "v_lshlrev_b32 v66, v67, v66\n"
      "s_nop 1\n"
      "v_or_b32_dpp v66, v66, v66 quad_perm:[1,0,3,2] row_mask:0xf bank_mask:0xf\n"
      "s_nop 1\n"
      "v_or_b32_dpp v66, v66, v66 quad_perm:[2,3,0,1] row_mask:0xf bank_mask:0xf\n"
      "s_nop 0\n"
      "v_readlane_b32 s16, v66, 0\n"
      "v_readlane_b32 s17, v66, 4\n"
      "v_readlane_b32 s18, v66, 8\n"
      "v_readlane_b32 s19, v66, 12\n"
      "v_readlane_b32 s20, v66, 16\n"
      "s_setprio 3\n"
      "s_mov_b32 s66, 0\n"
      "Lw4_3:\n"
      "ds_read_b32 v64, v254 offset:160\n"
      "s_waitcnt lgkmcnt(0)\n"
      "v_cmp_gt_u32 vcc, 1, v64\n"
      "s_cbranch_vccz Lw4d_4\n"
      "s_sleep 1\n"
      "s_add_u32 s66, s66, 1\n"
      "s_cmp_lt_u32 s66, 131072\n"
      "s_cbranch_scc1 Lw4_3\n"
      "Lw4d_4:\n"
      "s_setprio 1\n"
      "s_add_u32 s75, s75, 49152\n"
      "s_and_b32 s75, s75, 0xffff\n"
      "buffer_load_dwordx4 v[16:19], v54, s[28:31], s75 offen offset:0\n"
      "buffer_load_dwordx4 v[20:23], v54, s[28:31], s75 offen offset:1024\n"
      "buffer_load_dwordx4 v[24:27], v54, s[28:31], s75 offen offset:2048\n"
      "buffer_load_dwordx4 v[28:31], v54, s[28:31], s75 offen offset:3072\n"
      "s_add_u32 s12, s12, 3\n"
      "s_lshl_b32 s13, s12, 11\n"
      "s_add_u32 m0, s13, 65536\n"
      "s_lshl1_add_u32 s13, s100, s12\n"
      "s_and_b32 s13, s13, 31\n"
      "s_lshl_b32 s13, s13, 14\n"
      "s_waitcnt vmcnt(6)\n"
      "buffer_load_dwordx4 v60, s[44:47], s13 offen offset:0 lds\n"
      "buffer_load_dwordx4 v61, s[44:47], s13 offen offset:1024 lds\n"
      "s_add_u32 s12, s12, 3\n"
      "s_lshl_b32 s13, s12, 11\n"
      "s_add_u32 m0, s13, 65536\n"
      "s_lshl1_add_u32 s13, s100, s12\n"
      "s_and_b32 s13, s13, 31\n"
      "s_lshl_b32 s13, s13, 14\n"
      "s_waitcnt vmcnt(6)\n"
      "buffer_load_dwordx4 v60, s[44:47], s13 offen offset:0 lds\n"
      "buffer_load_dwordx4 v61, s[44:47], s13 offen offset:1024 lds\n"
      "s_add_u32 s12, s12, 3\n"
      "s_lshl_b32 s13, s12, 11\n"
      "s_add_u32 m0, s13, 65536\n"
      "s_lshl1_add_u32 s13, s100, s12\n"
      "s_and_b32 s13, s13, 31\n"
      "s_lshl_b32 s13, s13, 14\n"
      "s_waitcnt vmcnt(6)\n"
      "buffer_load_dwordx4 v60, s[44:47], s13 offen offset:0 lds\n"
      "buffer_load_dwordx4 v61, s[44:47], s13 offen offset:1024 lds\n"
      "s_add_u32 s12, s12, 3\n"
      "s_lshl_b32 s13, s12, 11\n"
      "s_add_u32 m0, s13, 65536\n"
      "s_lshl1_add_u32 s13, s100, s12\n"
      "s_and_b32 s13, s13, 31\n"
      "s_lshl_b32 s13, s13, 14\n"
      "s_waitcnt vmcnt(6)\n"
      "buffer_load_dwordx4 v60, s[44:47], s13 offen offset:0 lds\n"
      "buffer_load_dwordx4 v61, s[44:47], s13 offen offset:1024 lds\n"
      "s_add_u32 s12, s12, 3\n"
      "s_lshl_b32 s13, s12, 11\n"
      "s_add_u32 m0, s13, 65536\n"
      "s_lshl1_add_u32 s13, s100, s12\n"
      "s_and_b32 s13, s13, 31\n"
      "s_lshl_b32 s13, s13, 14\n"
      "s_waitcnt vmcnt(6)\n"
      "buffer_load_dwordx4 v60, s[44:47], s13 offen offset:0 lds\n"
      "buffer_load_dwordx4 v61, s[44:47], s13 offen offset:1024 lds\n"
      "s_setprio 3\n"
      "s_mov_b32 s66, 0\n"
      "Lw4_5:\n"
      "ds_read_b32 v64, v254 offset:144\n"
      "s_waitcnt lgkmcnt(0)\n"
      "v_cmp_gt_u32 vcc, 1, v64\n"
      "s_cbranch_vccz Lw4d_6\n"
      "s_sleep 1\n"
      "s_add_u32 s66, s66, 1\n"
      "s_cmp_lt_u32 s66, 131072\n"
      "s_cbranch_scc1 Lw4_5\n"
      "Lw4d_6:\n"
      "s_setprio 3\n"
      "v_dot2_f32_bf16 v68, v16, v16, 0\n"
      "s_add_u32 s12, s12, 3\n"
      "s_lshl_b32 s13, s12, 11\n"
      "s_add_u32 m0, s13, 65536\n"
      "s_lshl1_add_u32 s13, s100, s12\n"
      "s_and_b32 s13, s13, 31\n"
      "s_lshl_b32 s13, s13, 14\n"
      "s_waitcnt vmcnt(6)\n"
      "buffer_load_dwordx4 v60, s[44:47], s13 offen offset:0 lds\n"
      "buffer_load_dwordx4 v61, s[44:47], s13 offen offset:1024 lds\n"
      "v_dot2_f32_bf16 v68, v17, v17, v68\n"
      "v_dot2_f32_bf16 v68, v18, v18, v68\n"
      "v_dot2_f32_bf16 v68, v19, v19, v68\n"
      "v_dot2_f32_bf16 v69, v20, v20, 0\n"
      "v_dot2_f32_bf16 v69, v21, v21, v69\n"
      "v_dot2_f32_bf16 v69, v22, v22, v69\n"
      "v_dot2_f32_bf16 v69, v23, v23, v69\n"
      "v_dot2_f32_bf16 v70, v24, v24, 0\n"
      "v_dot2_f32_bf16 v70, v25, v25, v70\n"
      "v_dot2_f32_bf16 v70, v26, v26, v70\n"
      "v_dot2_f32_bf16 v70, v27, v27, v70\n"
      "v_dot2_f32_bf16 v71, v28, v28, 0\n"
      "v_dot2_f32_bf16 v71, v29, v29, v71\n"
      "v_dot2_f32_bf16 v71, v30, v30, v71\n"
      "v_dot2_f32_bf16 v71, v31, v31, v71\n"
      "v_mfma_f32_16x16x32_bf16 v[64:67], v[192:195], v[16:19], 0\n"
      "v_permlane32_swap_b32 v68, v70\n"
      "v_mfma_f32_16x16x32_bf16 v[64:67], v[196:199], v[20:23], v[64:67]\n"
      "v_permlane32_swap_b32 v69, v71\n"
      "v_mfma_f32_16x16x32_bf16 v[64:67], v[200:203], v[24:27], v[64:67]\n"
      "s_add_u32 s12, s12, 3\n"
      "s_lshl_b32 s13, s12, 11\n"
      "s_add_u32 m0, s13, 65536\n"
      "s_lshl1_add_u32 s13, s100, s12\n"
      "s_and_b32 s13, s13, 31\n"
      "s_lshl_b32 s13, s13, 14\n"
      "s_waitcnt vmcnt(6)\n"
      "buffer_load_dwordx4 v60, s[44:47], s13 offen offset:0 lds\n"
      "buffer_load_dwordx4 v61, s[44:47], s13 offen offset:1024 lds\n"
      "v_add_f32 v68, v68, v70\n"
      "v_add_f32 v69, v69, v71\n"
      "v_mfma_f32_16x16x32_bf16 v[64:67], v[204:207], v[28:31], v[64:67]\n"
      "v_lshlrev_b32 v72, 16, v16\n"
      "v_permlane16_swap_b32 v68, v69\n"
      "v_add_f32 v68, v68, v69\n"
      "v_and_b32 v73, s65, v16\n"
      "v_lshlrev_b32 v74, 16, v17\n"
      "v_add_f32_dpp v68, v68, v68 row_ror:8 row_mask:0xf bank_mask:0xf\n"
      "v_and_b32 v75, s65, v17\n"
      "v_lshlrev_b32 v76, 16, v18\n"
      "v_add_f32_dpp v64, v64, v64 row_ror:8 row_mask:0xf bank_mask:0xf\n"
      "v_add_f32_dpp v68, v68, v68 row_ror:4 row_mask:0xf bank_mask:0xf\n"
      "v_and_b32 v77, s65, v18\n"
      "v_add_f32_dpp v64, v64, v64 row_ror:4 row_mask:0xf bank_mask:0xf\n"
      "v_add_f32_dpp v68, v68, v68 row_ror:2 row_mask:0xf bank_mask:0xf\n"
      "v_lshlrev_b32 v78, 16, v19\n"
      "v_add_f32_dpp v64, v64, v64 row_ror:2 row_mask:0xf bank_mask:0xf\n"
      "v_add_f32_dpp v68, v68, v68 row_ror:1 row_mask:0xf bank_mask:0xf\n"
      "v_and_b32 v79, s65, v19\n"
      "s_add_u32 s12, s12, 3\n"
      "s_lshl_b32 s13, s12, 11\n"
      "s_add_u32 m0, s13, 65536\n"
      "s_lshl1_add_u32 s13, s100, s12\n"
      "s_and_b32 s13, s13, 31\n"
      "s_lshl_b32 s13, s13, 14\n"
      "s_waitcnt vmcnt(6)\n"
      "buffer_load_dwordx4 v60, s[44:47], s13 offen offset:0 lds\n"
      "buffer_load_dwordx4 v61, s[44:47], s13 offen offset:1024 lds\n"
      "v_add_f32_dpp v64, v64, v64 row_ror:1 row_mask:0xf bank_mask:0xf\n"
      "v_mul_f32 v64, 0x3b000000, v64\n"
      "v_mul_f32 v68, 0x3b000000, v68\n"
      "v_fma_f32 v68, -v64, v64, v68\n"
      "v_max_f32 v68, 0, v68\n"
      "v_fmaak_f32 v68, v49, v68, 0x36a1369a\n"
      "v_rsq_f32 v68, v68\n"
      "v_lshlrev_b32 v80, 16, v20\n"
      "v_mul_f32 v64, -v64, v68\n"
      "v_readlane_b32 s58, v68, 0\n"
      "v_readlane_b32 s59, v64, 0\n"
      "v_readlane_b32 s60, v68, 16\n"
      "v_readlane_b32 s61, v64, 16\n"
      "v_readlane_b32 s62, v68, 32\n"
      "v_readlane_b32 s63, v64, 32\n"
      "v_readlane_b32 s72, v68, 48\n"
      "v_readlane_b32 s73, v64, 48\n"
      "v_and_b32 v81, s65, v20\n"
      "v_lshlrev_b32 v82, 16, v21\n"
      "v_and_b32 v83, s65, v21\n"
      "s_add_u32 s12, s12, 3\n"
      "s_min_u32 s12, s12, 31\n"
      "s_lshl_b32 s13, s12, 11\n"
      "s_add_u32 m0, s13, 65536\n"
      "s_lshl1_add_u32 s13, s100, s12\n"
      "s_and_b32 s13, s13, 31\n"
      "s_lshl_b32 s13, s13, 14\n"
      "s_waitcnt vmcnt(6)\n"
      "buffer_load_dwordx4 v60, s[44:47], s13 offen offset:0 lds\n"
      "buffer_load_dwordx4 v61, s[44:47], s13 offen offset:1024 lds\n"
      "v_lshlrev_b32 v84, 16, v22\n"
      "v_and_b32 v85, s65, v22\n"
      "v_lshlrev_b32 v86, 16, v23\n"
      "v_and_b32 v87, s65, v23\n"
      "v_pk_fma_f32 v[72:73], v[72:73], s[58:59], s[58:59] op_sel:[0,0,1] op_sel_hi:[1,0,1]\n"
      "v_pk_fma_f32 v[74:75], v[74:75], s[58:59], s[58:59] op_sel:[0,0,1] op_sel_hi:[1,0,1]\n"
      "v_pk_fma_f32 v[76:77], v[76:77], s[58:59], s[58:59] op_sel:[0,0,1] op_sel_hi:[1,0,1]\n"
      "v_pk_fma_f32 v[78:79], v[78:79], s[58:59], s[58:59] op_sel:[0,0,1] op_sel_hi:[1,0,1]\n"
      "v_cvt_pk_bf16_f32 v16, v72, v73\n"
      "v_cvt_pk_bf16_f32 v17, v74, v75\n"
      "v_cvt_pk_bf16_f32 v18, v76, v77\n"
      "v_cvt_pk_bf16_f32 v19, v78, v79\n"
      "ds_write_b128 v56, v[16:19]\n"
      "v_pk_fma_f32 v[80:81], v[80:81], s[60:61], s[60:61] op_sel:[0,0,1] op_sel_hi:[1,0,1]\n"
      "v_pk_fma_f32 v[82:83], v[82:83], s[60:61], s[60:61] op_sel:[0,0,1] op_sel_hi:[1,0,1]\n"
      "v_pk_fma_f32 v[84:85], v[84:85], s[60:61], s[60:61] op_sel:[0,0,1] op_sel_hi:[1,0,1]\n"
      "v_pk_fma_f32 v[86:87], v[86:87], s[60:61], s[60:61] op_sel:[0,0,1] op_sel_hi:[1,0,1]\n"
      "v_cvt_pk_bf16_f32 v20, v80, v81\n"
      "v_cvt_pk_bf16_f32 v21, v82, v83\n"
      "v_cvt_pk_bf16_f32 v22, v84, v85\n"
      "v_cvt_pk_bf16_f32 v23, v86, v87\n"
      "ds_write_b128 v57, v[20:23]\n"
      "v_lshlrev_b32 v72, 16, v24\n"
      "v_and_b32 v73, s65, v24\n"
      "v_lshlrev_b32 v74, 16, v25\n"
      "v_and_b32 v75, s65, v25\n"
      "v_lshlrev_b32 v76, 16, v26\n"
      "v_and_b32 v77, s65, v26\n"
      "v_lshlrev_b32 v78, 16, v27\n"
      "v_and_b32 v79, s65, v27\n"
      "v_pk_fma_f32 v[72:73], v[72:73], s[62:63], s[62:63] op_sel:[0,0,1] op_sel_hi:[1,0,1]\n"
      "v_pk_fma_f32 v[74:75], v[74:75], s[62:63], s[62:63] op_sel:[0,0,1] op_sel_hi:[1,0,1]\n"
      "v_pk_fma_f32 v[76:77], v[76:77], s[62:63], s[62:63] op_sel:[0,0,1] op_sel_hi:[1,0,1]\n"
      "v_pk_fma_f32 v[78:79], v[78:79], s[62:63], s[62:63] op_sel:[0,0,1] op_sel_hi:[1,0,1]\n"
      "v_cvt_pk_bf16_f32 v24, v72, v73\n"
      "v_cvt_pk_bf16_f32 v25, v74, v75\n"
      "v_cvt_pk_bf16_f32 v26, v76, v77\n"
      "v_cvt_pk_bf16_f32 v27, v78, v79\n"
      "ds_write_b128 v58, v[24:27]\n"
      "v_lshlrev_b32 v80, 16, v28\n"
      "v_and_b32 v81, s65, v28\n"
      "v_lshlrev_b32 v82, 16, v29\n"
      "v_and_b32 v83, s65, v29\n"
      "v_lshlrev_b32 v84, 16, v30\n"
      "v_and_b32 v85, s65, v30\n"
      "v_lshlrev_b32 v86, 16, v31\n"
      "v_and_b32 v87, s65, v31\n"
      "v_pk_fma_f32 v[80:81], v[80:81], s[72:73], s[72:73] op_sel:[0,0,1] op_sel_hi:[1,0,1]\n"
      "v_pk_fma_f32 v[82:83], v[82:83], s[72:73], s[72:73] op_sel:[0,0,1] op_sel_hi:[1,0,1]\n"
      "v_pk_fma_f32 v[84:85], v[84:85], s[72:73], s[72:73] op_sel:[0,0,1] op_sel_hi:[1,0,1]\n"
      "v_pk_fma_f32 v[86:87], v[86:87], s[72:73], s[72:73] op_sel:[0,0,1] op_sel_hi:[1,0,1]\n"
      "v_cvt_pk_bf16_f32 v28, v80, v81\n"
      "v_cvt_pk_bf16_f32 v29, v82, v83\n"
      "v_cvt_pk_bf16_f32 v30, v84, v85\n"
      "v_cvt_pk_bf16_f32 v31, v86, v87\n"
      "ds_write_b128 v59, v[28:31]\n"
      "buffer_load_dwordx2 v[40:41], v48, s[40:43], s69 offen offset:0\n"
      "buffer_load_dwordx2 v[42:43], v48, s[40:43], s69 offen offset:32\n"
      "buffer_load_dwordx2 v[44:45], v48, s[40:43], s15 offen offset:0\n"
      "buffer_load_dwordx2 v[46:47], v48, s[40:43], s15 offen offset:32\n"
      "s_mov_b32 m0, s68\n"
      "s_nop 0\n"
      "buffer_load_dwordx4 v32, s[36:39], s67 offen offset:0 lds\n"
      "buffer_load_dwordx4 v33, s[36:39], s67 offen offset:1024 lds\n"
      "s_add_u32 s67, s67, 114688\n"
      "s_cmp_ge_u32 s67, 1835008\n"
      "s_cselect_b32 s67, s69, s67\n"
      "s_add_u32 m0, s68, 2048\n"
      "s_nop 0\n"
      "buffer_load_dwordx4 v32, s[36:39], s67 offen offset:0 lds\n"
      "buffer_load_dwordx4 v33, s[36:39], s67 offen offset:1024 lds\n"
      "s_add_u32 s67, s67, 114688\n"
      "s_cmp_ge_u32 s67, 1835008\n"
      "s_cselect_b32 s67, s69, s67\n"
      "s_add_u32 m0, s68, 4096\n"
      "s_nop 0\n"
      "buffer_load_dwordx4 v32, s[36:39], s67 offen offset:0 lds\n"
      "buffer_load_dwordx4 v33, s[36:39], s67 offen offset:1024 lds\n"
      "s_add_u32 s67, s67, 114688\n"
      "s_cmp_ge_u32 s67, 1835008\n"
      "s_cselect_b32 s67, s69, s67\n"
      "s_waitcnt vmcnt(10)\n"
      "s_waitcnt lgkmcnt(0)\n"
      "s_barrier\n"
      "s_setprio 0\n"
      "v_mov_b32 v66, 162304\n"
      "ds_read_b64 v[64:65], v66\n"
      "s_waitcnt lgkmcnt(0)\n"
      "v_readfirstlane_b32 s70, v64\n"
      "v_readfirstlane_b32 s71, v65\n"
      "LF1PRE:\n"
      "s_mul_i32 s75, s52, 294912\n"
      "s_lshl_b32 s61, s53, 13\n"
      "s_add_u32 s75, s75, s61\n"
      "s_add_u32 s75, s75, 2326528\n"
      "v_mov_b64 v[88:89], -1\n"
      "v_mov_b64 v[90:91], -1\n"
      "s_waitcnt vmcnt(0)\n"
      "s_mov_b32 s46, 0\n"
      "v_mov_b32 v208, s70\n"
      "v_mov_b32 v209, s71\n"
      "s_mov_b32 s96, 0x3fb8aa3b\n"
      "v_lshlrev_b32 v128, 16, v40\n"
      "v_and_b32 v129, s65, v40\n"
      "v_lshlrev_b32 v130, 16, v41\n"
      "v_and_b32 v131, s65, v41\n"
      "v_lshlrev_b32 v132, 16, v42\n"
      "v_and_b32 v133, s65, v42\n"
      "v_lshlrev_b32 v134, 16, v43\n"
      "v_and_b32 v135, s65, v43\n"
      "v_lshlrev_b32 v136, 16, v44\n"
      "v_and_b32 v137, s65, v44\n"
      "v_lshlrev_b32 v138, 16, v45\n"
      "v_and_b32 v139, s65, v45\n"
      "v_lshlrev_b32 v140, 16, v46\n"
      "v_and_b32 v141, s65, v46\n"
      "v_lshlrev_b32 v142, 16, v47\n"
      "v_and_b32 v143, s65, v47\n"
      "v_mul_f32 v128, s96, v128\n"
      "v_mul_f32 v129, s96, v129\n"
      "v_mul_f32 v130, s96, v130\n"
      "v_mul_f32 v131, s96, v131\n"
      "v_mul_f32 v132, s96, v132\n"
      "v_mul_f32 v133, s96, v133\n"
      "v_mul_f32 v134, s96, v134\n"
      "v_mul_f32 v135, s96, v135\n"
      "v_mul_f32 v136, s96, v136\n"
      "v_mul_f32 v137, s96, v137\n"
      "v_mul_f32 v138, s96, v138\n"
      "v_mul_f32 v139, s96, v139\n"
      "v_mul_f32 v140, s96, v140\n"
      "v_mul_f32 v141, s96, v141\n"
      "v_mul_f32 v142, s96, v142\n"
      "v_mul_f32 v143, s96, v143\n"
      "LF1:\n"
      "s_setprio 0\n"
      "ds_read_b64_tr_b16 v[64:65], v34 offset:0\n"
      "ds_read_b64_tr_b16 v[66:67], v34 offset:256\n"
      "ds_read_b64_tr_b16 v[68:69], v35 offset:0\n"
      "ds_read_b64_tr_b16 v[70:71], v35 offset:256\n"
      "ds_read_b128 v[96:99], v36 offset:0\n"
      "ds_read_b128 v[100:103], v36 offset:16384\n"
      "ds_read_b128 v[104:107], v36 offset:32768\n"
      "ds_read_b128 v[108:111], v36 offset:49152\n"
      "s_waitcnt lgkmcnt(4)\n"
      "s_mov_b32 m0, s68\n"
      "s_nop 0\n"
      "buffer_load_dwordx4 v32, s[36:39], s67 offen offset:0 lds\n"
      "buffer_load_dwordx4 v33, s[36:39], s67 offen offset:1024 lds\n"
      "s_add_u32 s67, s67, 114688\n"
      "s_cmp_ge_u32 s67, 1835008\n"
      "s_cselect_b32 s67, s69, s67\n"
      "s_waitcnt lgkmcnt(0)\n"
      "v_mfma_f32_16x16x32_bf16 v[0:3], v[64:67], v[96:99], v[128:131]\n"
      "ds_read_b64_tr_b16 v[72:73], v34 offset:2048\n"
      "ds_read_b64_tr_b16 v[74:75], v34 offset:2304\n"
      "v_mfma_f32_16x16x32_bf16 v[4:7], v[68:71], v[96:99], v[132:135]\n"
      "ds_read_b64_tr_b16 v[76:77], v35 offset:2048\n"
      "ds_read_b64_tr_b16 v[78:79], v35 offset:2304\n"
      "v_mfma_f32_16x16x32_bf16 v[8:11], v[64:67], v[100:103], v[128:131]\n"
      "ds_read_b128 v[112:115], v37 offset:0\n"
      "ds_read_b128 v[116:119], v37 offset:16384\n"
      "v_mfma_f32_16x16x32_bf16 v[12:15], v[68:71], v[100:103], v[132:135]\n"
      "ds_read_b128 v[120:123], v37 offset:32768\n"
      "ds_read_b128 v[124:127], v37 offset:49152\n"
      "v_mfma_f32_16x16x32_bf16 v[16:19], v[64:67], v[104:107], v[128:131]\n"
      "v_mfma_f32_16x16x32_bf16 v[20:23], v[68:71], v[104:107], v[132:135]\n"
      "s_waitcnt lgkmcnt(4)\n"
      "s_add_u32 m0, s68, 2048\n"
      "s_nop 0\n"
      "buffer_load_dwordx4 v32, s[36:39], s67 offen offset:0 lds\n"
      "v_mfma_f32_16x16x32_bf16 v[24:27], v[64:67], v[108:111], v[128:131]\n"
      "buffer_load_dwordx4 v33, s[36:39], s67 offen offset:1024 lds\n"
      "s_add_u32 s67, s67, 114688\n"
      "s_cmp_ge_u32 s67, 1835008\n"
      "s_cselect_b32 s67, s69, s67\n"
      "v_mfma_f32_16x16x32_bf16 v[28:31], v[68:71], v[108:111], v[132:135]\n"
      "s_cmp_lg_u32 s46, 0\n"
      "s_cbranch_scc1 LNOBAR3\n"
      "s_barrier\n"
      "LNOBAR3:\n"
      "s_waitcnt lgkmcnt(0)\n"
      "v_mfma_f32_16x16x32_bf16 v[0:3], v[72:75], v[112:115], v[0:3]\n"
      "ds_read_b64_tr_b16 v[64:65], v34 offset:4096\n"
      "ds_read_b64_tr_b16 v[66:67], v34 offset:4352\n"
      "v_mfma_f32_16x16x32_bf16 v[4:7], v[76:79], v[112:115], v[4:7]\n"
      "ds_read_b64_tr_b16 v[68:69], v35 offset:4096\n"
      "ds_read_b64_tr_b16 v[70:71], v35 offset:4352\n"
      "v_mfma_f32_16x16x32_bf16 v[8:11], v[72:75], v[116:119], v[8:11]\n"
      "ds_read_b128 v[96:99], v38 offset:0\n"
      "ds_read_b128 v[100:103], v38 offset:16384\n"
      "v_mfma_f32_16x16x32_bf16 v[12:15], v[76:79], v[116:119], v[12:15]\n"
      "ds_read_b128 v[104:107], v38 offset:32768\n"
      "ds_read_b128 v[108:111], v38 offset:49152\n"
      "v_mfma_f32_16x16x32_bf16 v[16:19], v[72:75], v[120:123], v[16:19]\n"
      "v_mfma_f32_16x16x32_bf16 v[20:23], v[76:79], v[120:123], v[20:23]\n"
      "s_waitcnt lgkmcnt(4)\n"
      "s_add_u32 m0, s68, 4096\n"
      "s_nop 0\n"
      "buffer_load_dwordx4 v32, s[36:39], s67 offen offset:0 lds\n"
      "v_mfma_f32_16x16x32_bf16 v[24:27], v[72:75], v[124:127], v[24:27]\n"
      "buffer_load_dwordx4 v33, s[36:39], s67 offen offset:1024 lds\n"
      "s_add_u32 s67, s67, 114688\n"
      "s_cmp_ge_u32 s67, 1835008\n"
      "s_cselect_b32 s67, s69, s67\n"
      "v_mfma_f32_16x16x32_bf16 v[28:31], v[76:79], v[124:127], v[28:31]\n"
      "s_waitcnt lgkmcnt(0)\n"
      "v_mfma_f32_16x16x32_bf16 v[0:3], v[64:67], v[96:99], v[0:3]\n"
      "s_waitcnt vmcnt(4)\n"
      "ds_read_b64_tr_b16 v[72:73], v34 offset:0\n"
      "ds_read_b64_tr_b16 v[74:75], v34 offset:256\n"
      "v_mfma_f32_16x16x32_bf16 v[4:7], v[68:71], v[96:99], v[4:7]\n"
      "ds_read_b64_tr_b16 v[76:77], v35 offset:0\n"
      "ds_read_b64_tr_b16 v[78:79], v35 offset:256\n"
      "v_mfma_f32_16x16x32_bf16 v[8:11], v[64:67], v[100:103], v[8:11]\n"
      "ds_read_b128 v[112:115], v39 offset:0\n"
      "ds_read_b128 v[116:119], v39 offset:16384\n"
      "v_mfma_f32_16x16x32_bf16 v[12:15], v[68:71], v[100:103], v[12:15]\n"
      "ds_read_b128 v[120:123], v39 offset:32768\n"
      "ds_read_b128 v[124:127], v39 offset:49152\n"
      "v_mfma_f32_16x16x32_bf16 v[16:19], v[64:67], v[104:107], v[16:19]\n"
      "s_mov_b64 exec, 1\n"
      "buffer_store_dwordx2 v[208:209], v210, s[32:35], 0 offen sc1\n"
      "s_mov_b64 exec, -1\n"
      "v_mfma_f32_16x16x32_bf16 v[20:23], v[68:71], v[104:107], v[20:23]\n"
      "s_waitcnt lgkmcnt(4)\n"
      "s_mov_b32 m0, s68\n"
      "s_nop 0\n"
      "buffer_load_dwordx4 v32, s[36:39], s67 offen offset:0 lds\n"
      "v_mfma_f32_16x16x32_bf16 v[24:27], v[64:67], v[108:111], v[24:27]\n"
      "buffer_load_dwordx4 v33, s[36:39], s67 offen offset:1024 lds\n"
      "s_add_u32 s67, s67, 114688\n"
      "s_cmp_ge_u32 s67, 1835008\n"
      "s_cselect_b32 s67, s69, s67\n"
      "v_mfma_f32_16x16x32_bf16 v[28:31], v[68:71], v[108:111], v[28:31]\n"
      "s_waitcnt lgkmcnt(0)\n"
      "v_mfma_f32_16x16x32_bf16 v[0:3], v[72:75], v[112:115], v[0:3]\n"
      "s_waitcnt vmcnt(5)\n"
      "ds_read_b64_tr_b16 v[64:65], v34 offset:2048\n"
      "ds_read_b64_tr_b16 v[66:67], v34 offset:2304\n"
      "v_mfma_f32_16x16x32_bf16 v[4:7], v[76:79], v[112:115], v[4:7]\n"
      "ds_read_b64_tr_b16 v[68:69], v35 offset:2048\n"
      "ds_read_b64_tr_b16 v[70:71], v35 offset:2304\n"
      "v_mfma_f32_16x16x32_bf16 v[8:11], v[72:75], v[116:119], v[8:11]\n"
      "ds_read_b128 v[96:99], v36 offset:256\n"
      "ds_read_b128 v[100:103], v36 offset:16640\n"
      "v_mfma_f32_16x16x32_bf16 v[12:15], v[76:79], v[116:119], v[12:15]\n"
      "ds_read_b128 v[104:107], v36 offset:33024\n"
      "ds_read_b128 v[108:111], v36 offset:49408\n"
      "v_mfma_f32_16x16x32_bf16 v[16:19], v[72:75], v[120:123], v[16:19]\n"
      "v_mfma_f32_16x16x32_bf16 v[20:23], v[76:79], v[120:123], v[20:23]\n"
      "s_waitcnt lgkmcnt(4)\n"
      "s_add_u32 m0, s68, 2048\n"
      "s_nop 0\n"
      "buffer_load_dwordx4 v32, s[36:39], s67 offen offset:0 lds\n"
      "v_mfma_f32_16x16x32_bf16 v[24:27], v[72:75], v[124:127], v[24:27]\n"
      "buffer_load_dwordx4 v33, s[36:39], s67 offen offset:1024 lds\n"
      "s_add_u32 s67, s67, 114688\n"
      "s_cmp_ge_u32 s67, 1835008\n"
      "s_cselect_b32 s67, s69, s67\n"
      "v_mfma_f32_16x16x32_bf16 v[28:31], v[76:79], v[124:127], v[28:31]\n"
      "s_waitcnt lgkmcnt(0)\n"
      "v_mfma_f32_16x16x32_bf16 v[0:3], v[64:67], v[96:99], v[0:3]\n"
      "s_waitcnt vmcnt(5)\n"
      "ds_read_b64_tr_b16 v[72:73], v34 offset:4096\n"
      "ds_read_b64_tr_b16 v[74:75], v34 offset:4352\n"
      "v_mfma_f32_16x16x32_bf16 v[4:7], v[68:71], v[96:99], v[4:7]\n"
      "ds_read_b64_tr_b16 v[76:77], v35 offset:4096\n"
      "ds_read_b64_tr_b16 v[78:79], v35 offset:4352\n"
      "v_mfma_f32_16x16x32_bf16 v[8:11], v[64:67], v[100:103], v[8:11]\n"
      "ds_read_b128 v[112:115], v37 offset:256\n"
      "ds_read_b128 v[116:119], v37 offset:16640\n"
      "v_mfma_f32_16x16x32_bf16 v[12:15], v[68:71], v[100:103], v[12:15]\n"
      "ds_read_b128 v[120:123], v37 offset:33024\n"
      "ds_read_b128 v[124:127], v37 offset:49408\n"
      "v_mfma_f32_16x16x32_bf16 v[16:19], v[64:67], v[104:107], v[16:19]\n"
      "v_mfma_f32_16x16x32_bf16 v[20:23], v[68:71], v[104:107], v[20:23]\n"
      "s_waitcnt lgkmcnt(4)\n"
      "s_add_u32 m0, s68, 4096\n"
      "s_nop 0\n"
      "buffer_load_dwordx4 v32, s[36:39], s67 offen offset:0 lds\n"
      "v_mfma_f32_16x16x32_bf16 v[24:27], v[64:67], v[108:111], v[24:27]\n"
      "buffer_load_dwordx4 v33, s[36:39], s67 offen offset:1024 lds\n"
      "s_add_u32 s67, s67, 114688\n"
      "s_cmp_ge_u32 s67, 1835008\n"
      "s_cselect_b32 s67, s69, s67\n"
      "v_mfma_f32_16x16x32_bf16 v[28:31], v[68:71], v[108:111], v[28:31]\n"
      "s_waitcnt lgkmcnt(0)\n"
      "v_mfma_f32_16x16x32_bf16 v[0:3], v[72:75], v[112:115], v[0:3]\n"
      "s_waitcnt vmcnt(4)\n"
      "ds_read_b64_tr_b16 v[64:65], v34 offset:0\n"
      "ds_read_b64_tr_b16 v[66:67], v34 offset:256\n"
      "v_mfma_f32_16x16x32_bf16 v[4:7], v[76:79], v[112:115], v[4:7]\n"
      "ds_read_b64_tr_b16 v[68:69], v35 offset:0\n"
      "ds_read_b64_tr_b16 v[70:71], v35 offset:256\n"
      "v_mfma_f32_16x16x32_bf16 v[8:11], v[72:75], v[116:119], v[8:11]\n"
      "ds_read_b128 v[96:99], v38 offset:256\n"
      "ds_read_b128 v[100:103], v38 offset:16640\n"
      "v_mfma_f32_16x16x32_bf16 v[12:15], v[76:79], v[116:119], v[12:15]\n"
      "ds_read_b128 v[104:107], v38 offset:33024\n"
      "ds_read_b128 v[108:111], v38 offset:49408\n"
      "v_mfma_f32_16x16x32_bf16 v[16:19], v[72:75], v[120:123], v[16:19]\n"
      "v_mfma_f32_16x16x32_bf16 v[20:23], v[76:79], v[120:123], v[20:23]\n"
      "s_waitcnt lgkmcnt(4)\n"
      "s_mov_b32 m0, s68\n"
      "s_nop 0\n"
      "buffer_load_dwordx4 v32, s[36:39], s67 offen offset:0 lds\n"
      "v_mfma_f32_16x16x32_bf16 v[24:27], v[72:75], v[124:127], v[24:27]\n"
      "buffer_load_dwordx4 v33, s[36:39], s67 offen offset:1024 lds\n"
      "s_add_u32 s67, s67, 114688\n"
      "s_cmp_ge_u32 s67, 1835008\n"
      "s_cselect_b32 s67, s69, s67\n"
      "v_mfma_f32_16x16x32_bf16 v[28:31], v[76:79], v[124:127], v[28:31]\n"
      "s_waitcnt lgkmcnt(0)\n"
      "v_mfma_f32_16x16x32_bf16 v[0:3], v[64:67], v[96:99], v[0:3]\n"
      "s_waitcnt vmcnt(4)\n"
      "ds_read_b64_tr_b16 v[72:73], v34 offset:2048\n"
      "ds_read_b64_tr_b16 v[74:75], v34 offset:2304\n"
      "v_mfma_f32_16x16x32_bf16 v[4:7], v[68:71], v[96:99], v[4:7]\n"
      "ds_read_b64_tr_b16 v[76:77], v35 offset:2048\n"
      "ds_read_b64_tr_b16 v[78:79], v35 offset:2304\n"
      "v_mfma_f32_16x16x32_bf16 v[8:11], v[64:67], v[100:103], v[8:11]\n"
      "ds_read_b128 v[112:115], v39 offset:256\n"
      "ds_read_b128 v[116:119], v39 offset:16640\n"
      "v_mfma_f32_16x16x32_bf16 v[12:15], v[68:71], v[100:103], v[12:15]\n"
      "ds_read_b128 v[120:123], v39 offset:33024\n"
      "ds_read_b128 v[124:127], v39 offset:49408\n"
      "v_mfma_f32_16x16x32_bf16 v[16:19], v[64:67], v[104:107], v[16:19]\n"
      "v_mfma_f32_16x16x32_bf16 v[20:23], v[68:71], v[104:107], v[20:23]\n"
      "s_waitcnt lgkmcnt(4)\n"
      "s_add_u32 m0, s68, 2048\n"
      "s_nop 0\n"
      "buffer_load_dwordx4 v32, s[36:39], s67 offen offset:0 lds\n"
      "v_mfma_f32_16x16x32_bf16 v[24:27], v[64:67], v[108:111], v[24:27]\n"
      "buffer_load_dwordx4 v33, s[36:39], s67 offen offset:1024 lds\n"
      "s_add_u32 s67, s67, 114688\n"
      "s_cmp_ge_u32 s67, 1835008\n"
      "s_cselect_b32 s67, s69, s67\n"
      "v_mfma_f32_16x16x32_bf16 v[28:31], v[68:71], v[108:111], v[28:31]\n"
      "s_waitcnt lgkmcnt(0)\n"
      "v_mfma_f32_16x16x32_bf16 v[0:3], v[72:75], v[112:115], v[0:3]\n"
      "s_waitcnt vmcnt(4)\n"
      "ds_read_b64_tr_b16 v[64:65], v34 offset:4096\n"
      "ds_read_b64_tr_b16 v[66:67], v34 offset:4352\n"
      "v_mfma_f32_16x16x32_bf16 v[4:7], v[76:79], v[112:115], v[4:7]\n"
      "ds_read_b64_tr_b16 v[68:69], v35 offset:4096\n"
      "ds_read_b64_tr_b16 v[70:71], v35 offset:4352\n"
      "v_mfma_f32_16x16x32_bf16 v[8:11], v[72:75], v[116:119], v[8:11]\n"
      "ds_read_b128 v[96:99], v36 offset:512\n"
      "ds_read_b128 v[100:103], v36 offset:16896\n"
      "v_mfma_f32_16x16x32_bf16 v[12:15], v[76:79], v[116:119], v[12:15]\n"
      "ds_read_b128 v[104:107], v36 offset:33280\n"
      "ds_read_b128 v[108:111], v36 offset:49664\n"
      "v_mfma_f32_16x16x32_bf16 v[16:19], v[72:75], v[120:123], v[16:19]\n"
      "v_mfma_f32_16x16x32_bf16 v[20:23], v[76:79], v[120:123], v[20:23]\n"
      "s_waitcnt lgkmcnt(4)\n"
      "s_add_u32 m0, s68, 4096\n"
      "s_nop 0\n"
      "buffer_load_dwordx4 v32, s[36:39], s67 offen offset:0 lds\n"
      "v_mfma_f32_16x16x32_bf16 v[24:27], v[72:75], v[124:127], v[24:27]\n"
      "buffer_load_dwordx4 v33, s[36:39], s67 offen offset:1024 lds\n"
      "s_add_u32 s67, s67, 114688\n"
      "s_cmp_ge_u32 s67, 1835008\n"
      "s_cselect_b32 s67, s69, s67\n"
      "v_mfma_f32_16x16x32_bf16 v[28:31], v[76:79], v[124:127], v[28:31]\n"
      "s_waitcnt lgkmcnt(0)\n"
      "v_mfma_f32_16x16x32_bf16 v[0:3], v[64:67], v[96:99], v[0:3]\n"
      "s_waitcnt vmcnt(4)\n"
      "ds_read_b64_tr_b16 v[72:73], v34 offset:0\n"
      "ds_read_b64_tr_b16 v[74:75], v34 offset:256\n"
      "v_mfma_f32_16x16x32_bf16 v[4:7], v[68:71], v[96:99], v[4:7]\n"
      "ds_read_b64_tr_b16 v[76:77], v35 offset:0\n"
      "ds_read_b64_tr_b16 v[78:79], v35 offset:256\n"
      "v_mfma_f32_16x16x32_bf16 v[8:11], v[64:67], v[100:103], v[8:11]\n"
      "ds_read_b128 v[112:115], v37 offset:512\n"
      "ds_read_b128 v[116:119], v37 offset:16896\n"
      "v_mfma_f32_16x16x32_bf16 v[12:15], v[68:71], v[100:103], v[12:15]\n"
      "ds_read_b128 v[120:123], v37 offset:33280\n"
      "ds_read_b128 v[124:127], v37 offset:49664\n"
      "v_mfma_f32_16x16x32_bf16 v[16:19], v[64:67], v[104:107], v[16:19]\n"
      "v_mfma_f32_16x16x32_bf16 v[20:23], v[68:71], v[104:107], v[20:23]\n"
      "s_waitcnt lgkmcnt(4)\n"
      "s_mov_b32 m0, s68\n"
      "s_nop 0\n"
      "buffer_load_dwordx4 v32, s[36:39], s67 offen offset:0 lds\n"
      "v_mfma_f32_16x16x32_bf16 v[24:27], v[64:67], v[108:111], v[24:27]\n"
      "buffer_load_dwordx4 v33, s[36:39], s67 offen offset:1024 lds\n"
      "s_add_u32 s67, s67, 114688\n"
      "s_cmp_ge_u32 s67, 1835008\n"
      "s_cselect_b32 s67, s69, s67\n"
      "v_mfma_f32_16x16x32_bf16 v[28:31], v[68:71], v[108:111], v[28:31]\n"
      "s_waitcnt lgkmcnt(0)\n"
      "v_mfma_f32_16x16x32_bf16 v[0:3], v[72:75], v[112:115], v[0:3]\n"
      "s_waitcnt vmcnt(4)\n"
      "ds_read_b64_tr_b16 v[64:65], v34 offset:2048\n"
      "ds_read_b64_tr_b16 v[66:67], v34 offset:2304\n"
      "v_mfma_f32_16x16x32_bf16 v[4:7], v[76:79], v[112:115], v[4:7]\n"
      "ds_read_b64_tr_b16 v[68:69], v35 offset:2048\n"
      "ds_read_b64_tr_b16 v[70:71], v35 offset:2304\n"
      "v_mfma_f32_16x16x32_bf16 v[8:11], v[72:75], v[116:119], v[8:11]\n"
      "ds_read_b128 v[96:99], v38 offset:512\n"
      "ds_read_b128 v[100:103], v38 offset:16896\n"
      "v_mfma_f32_16x16x32_bf16 v[12:15], v[76:79], v[116:119], v[12:15]\n"
      "ds_read_b128 v[104:107], v38 offset:33280\n"
      "ds_read_b128 v[108:111], v38 offset:49664\n"
      "v_mfma_f32_16x16x32_bf16 v[16:19], v[72:75], v[120:123], v[16:19]\n"
      "v_mfma_f32_16x16x32_bf16 v[20:23], v[76:79], v[120:123], v[20:23]\n"
      "s_waitcnt lgkmcnt(4)\n"
      "s_add_u32 m0, s68, 2048\n"
      "s_nop 0\n"
      "buffer_load_dwordx4 v32, s[36:39], s67 offen offset:0 lds\n"
      "v_mfma_f32_16x16x32_bf16 v[24:27], v[72:75], v[124:127], v[24:27]\n"
      "buffer_load_dwordx4 v33, s[36:39], s67 offen offset:1024 lds\n"
      "s_add_u32 s67, s67, 114688\n"
      "s_cmp_ge_u32 s67, 1835008\n"
      "s_cselect_b32 s67, s69, s67\n"
      "v_mfma_f32_16x16x32_bf16 v[28:31], v[76:79], v[124:127], v[28:31]\n"
      "s_waitcnt lgkmcnt(0)\n"
      "v_mfma_f32_16x16x32_bf16 v[0:3], v[64:67], v[96:99], v[0:3]\n"
      "s_waitcnt vmcnt(4)\n"
      "ds_read_b64_tr_b16 v[72:73], v34 offset:4096\n"
      "ds_read_b64_tr_b16 v[74:75], v34 offset:4352\n"
      "v_mfma_f32_16x16x32_bf16 v[4:7], v[68:71], v[96:99], v[4:7]\n"
      "ds_read_b64_tr_b16 v[76:77], v35 offset:4096\n"
      "ds_read_b64_tr_b16 v[78:79], v35 offset:4352\n"
      "v_mfma_f32_16x16x32_bf16 v[8:11], v[64:67], v[100:103], v[8:11]\n"
      "ds_read_b128 v[112:115], v39 offset:512\n"
      "ds_read_b128 v[116:119], v39 offset:16896\n"
      "v_mfma_f32_16x16x32_bf16 v[12:15], v[68:71], v[100:103], v[12:15]\n"
      "ds_read_b128 v[120:123], v39 offset:33280\n"
      "ds_read_b128 v[124:127], v39 offset:49664\n"
      "v_mfma_f32_16x16x32_bf16 v[16:19], v[64:67], v[104:107], v[16:19]\n"
      "v_mfma_f32_16x16x32_bf16 v[20:23], v[68:71], v[104:107], v[20:23]\n"
      "s_waitcnt lgkmcnt(4)\n"
      "s_add_u32 m0, s68, 4096\n"
      "s_nop 0\n"
      "buffer_load_dwordx4 v32, s[36:39], s67 offen offset:0 lds\n"
      "v_mfma_f32_16x16x32_bf16 v[24:27], v[64:67], v[108:111], v[24:27]\n"
      "buffer_load_dwordx4 v33, s[36:39], s67 offen offset:1024 lds\n"
      "s_add_u32 s67, s67, 114688\n"
      "s_cmp_ge_u32 s67, 1835008\n"
      "s_cselect_b32 s67, s69, s67\n"
      "v_mfma_f32_16x16x32_bf16 v[28:31], v[68:71], v[108:111], v[28:31]\n"
      "s_waitcnt lgkmcnt(0)\n"
      "v_mfma_f32_16x16x32_bf16 v[0:3], v[72:75], v[112:115], v[0:3]\n"
      "s_waitcnt vmcnt(4)\n"
      "ds_read_b64_tr_b16 v[64:65], v34 offset:0\n"
      "ds_read_b64_tr_b16 v[66:67], v34 offset:256\n"
      "v_mfma_f32_16x16x32_bf16 v[4:7], v[76:79], v[112:115], v[4:7]\n"
      "ds_read_b64_tr_b16 v[68:69], v35 offset:0\n"
      "ds_read_b64_tr_b16 v[70:71], v35 offset:256\n"
      "v_mfma_f32_16x16x32_bf16 v[8:11], v[72:75], v[116:119], v[8:11]\n"
      "ds_read_b128 v[96:99], v36 offset:768\n"
      "ds_read_b128 v[100:103], v36 offset:17152\n"
      "v_mfma_f32_16x16x32_bf16 v[12:15], v[76:79], v[116:119], v[12:15]\n"
      "ds_read_b128 v[104:107], v36 offset:33536\n"
      "ds_read_b128 v[108:111], v36 offset:49920\n"
      "v_mfma_f32_16x16x32_bf16 v[16:19], v[72:75], v[120:123], v[16:19]\n"
      "v_mfma_f32_16x16x32_bf16 v[20:23], v[76:79], v[120:123], v[20:23]\n"
      "s_waitcnt lgkmcnt(4)\n"
      "s_mov_b32 m0, s68\n"
      "s_nop 0\n"
      "buffer_load_dwordx4 v32, s[36:39], s67 offen offset:0 lds\n"
      "v_mfma_f32_16x16x32_bf16 v[24:27], v[72:75], v[124:127], v[24:27]\n"
      "buffer_load_dwordx4 v33, s[36:39], s67 offen offset:1024 lds\n"
      "s_add_u32 s67, s67, 114688\n"
      "s_cmp_ge_u32 s67, 1835008\n"
      "s_cselect_b32 s67, s69, s67\n"
      "v_mfma_f32_16x16x32_bf16 v[28:31], v[76:79], v[124:127], v[28:31]\n"
      "s_waitcnt lgkmcnt(0)\n"
      "v_mfma_f32_16x16x32_bf16 v[0:3], v[64:67], v[96:99], v[0:3]\n"
      "s_waitcnt vmcnt(4)\n"
      "ds_read_b64_tr_b16 v[72:73], v34 offset:2048\n"
      "ds_read_b64_tr_b16 v[74:75], v34 offset:2304\n"
      "v_mfma_f32_16x16x32_bf16 v[4:7], v[68:71], v[96:99], v[4:7]\n"
      "ds_read_b64_tr_b16 v[76:77], v35 offset:2048\n"
      "ds_read_b64_tr_b16 v[78:79], v35 offset:2304\n"
      "v_mfma_f32_16x16x32_bf16 v[8:11], v[64:67], v[100:103], v[8:11]\n"
      "ds_read_b128 v[112:115], v37 offset:768\n"
      "ds_read_b128 v[116:119], v37 offset:17152\n"
      "v_mfma_f32_16x16x32_bf16 v[12:15], v[68:71], v[100:103], v[12:15]\n"
      "ds_read_b128 v[120:123], v37 offset:33536\n"
      "ds_read_b128 v[124:127], v37 offset:49920\n"
      "v_mfma_f32_16x16x32_bf16 v[16:19], v[64:67], v[104:107], v[16:19]\n"
      "v_mfma_f32_16x16x32_bf16 v[20:23], v[68:71], v[104:107], v[20:23]\n"
      "s_waitcnt lgkmcnt(4)\n"
      "s_add_u32 s58, s100, 1\n"
      "s_and_b32 s58, s58, 15\n"
      "s_mul_i32 s58, s58, 114688\n"
      "s_add_u32 s67, s58, s15\n"
      "s_add_u32 m0, s68, 2048\n"
      "s_nop 0\n"
      "buffer_load_dwordx4 v50, s[36:39], s67 offen offset:0 lds\n"
      "v_mfma_f32_16x16x32_bf16 v[24:27], v[64:67], v[108:111], v[24:27]\n"
      "buffer_load_dwordx4 v51, s[36:39], s67 offen offset:1024 lds\n"
      "v_mfma_f32_16x16x32_bf16 v[28:31], v[68:71], v[108:111], v[28:31]\n"
      "s_waitcnt lgkmcnt(0)\n"
      "v_mfma_f32_16x16x32_bf16 v[0:3], v[72:75], v[112:115], v[0:3]\n"
      "s_waitcnt vmcnt(4)\n"
      "ds_read_b64_tr_b16 v[64:65], v34 offset:4096\n"
      "ds_read_b64_tr_b16 v[66:67], v34 offset:4352\n"
      "v_mfma_f32_16x16x32_bf16 v[4:7], v[76:79], v[112:115], v[4:7]\n"
      "ds_read_b64_tr_b16 v[68:69], v35 offset:4096\n"
      "ds_read_b64_tr_b16 v[70:71], v35 offset:4352\n"
      "v_mfma_f32_16x16x32_bf16 v[8:11], v[72:75], v[116:119], v[8:11]\n"
      "ds_read_b128 v[96:99], v38 offset:768\n"
      "ds_read_b128 v[100:103], v38 offset:17152\n"
      "v_mfma_f32_16x16x32_bf16 v[12:15], v[76:79], v[116:119], v[12:15]\n"
      "ds_read_b128 v[104:107], v38 offset:33536\n"
      "ds_read_b128 v[108:111], v38 offset:49920\n"
      "v_mfma_f32_16x16x32_bf16 v[16:19], v[72:75], v[120:123], v[16:19]\n"
      "buffer_load_dwordx2 v[212:213], v211, s[32:35], 0 offen sc1\n"
      "v_mfma_f32_16x16x32_bf16 v[20:23], v[76:79], v[120:123], v[20:23]\n"
      "s_waitcnt lgkmcnt(4)\n"
      "s_add_u32 s58, s100, 2\n"
      "s_and_b32 s58, s58, 15\n"
      "s_mul_i32 s58, s58, 114688\n"
      "s_add_u32 s67, s58, s15\n"
      "s_add_u32 m0, s68, 4096\n"
      "s_nop 0\n"
      "buffer_load_dwordx4 v50, s[36:39], s67 offen offset:0 lds\n"
      "v_mfma_f32_16x16x32_bf16 v[24:27], v[72:75], v[124:127], v[24:27]\n"
      "buffer_load_dwordx4 v51, s[36:39], s67 offen offset:1024 lds\n"
      "v_mfma_f32_16x16x32_bf16 v[28:31], v[76:79], v[124:127], v[28:31]\n"
      "s_waitcnt lgkmcnt(0)\n"
      "v_mfma_f32_16x16x32_bf16 v[0:3], v[64:67], v[96:99], v[0:3]\n"
      "s_waitcnt vmcnt(5)\n"
      "ds_read_b64_tr_b16 v[72:73], v34 offset:0\n"
      "ds_read_b64_tr_b16 v[74:75], v34 offset:256\n"
      "v_mfma_f32_16x16x32_bf16 v[4:7], v[68:71], v[96:99], v[4:7]\n"
      "ds_read_b64_tr_b16 v[76:77], v35 offset:0\n"
      "ds_read_b64_tr_b16 v[78:79], v35 offset:256\n"
      "v_mfma_f32_16x16x32_bf16 v[8:11], v[64:67], v[100:103], v[8:11]\n"
      "ds_read_b128 v[112:115], v39 offset:768\n"
      "ds_read_b128 v[116:119], v39 offset:17152\n"
      "v_mfma_f32_16x16x32_bf16 v[12:15], v[68:71], v[100:103], v[12:15]\n"
      "ds_read_b128 v[120:123], v39 offset:33536\n"
      "ds_read_b128 v[124:127], v39 offset:49920\n"
      "v_mfma_f32_16x16x32_bf16 v[16:19], v[64:67], v[104:107], v[16:19]\n"
      "v_mfma_f32_16x16x32_bf16 v[20:23], v[68:71], v[104:107], v[20:23]\n"
      "s_waitcnt lgkmcnt(4)\n"
      "s_add_u32 s58, s100, 0\n"
      "s_and_b32 s58, s58, 15\n"
      "s_mul_i32 s58, s58, 114688\n"
      "s_add_u32 s67, s58, s15\n"
      "s_mov_b32 m0, s68\n"
      "s_nop 0\n"
      "buffer_load_dwordx4 v50, s[36:39], s67 offen offset:0 lds\n"
      "v_mfma_f32_16x16x32_bf16 v[24:27], v[64:67], v[108:111], v[24:27]\n"
      "buffer_load_dwordx4 v51, s[36:39], s67 offen offset:1024 lds\n"
      "v_mfma_f32_16x16x32_bf16 v[28:31], v[68:71], v[108:111], v[28:31]\n"
      "s_waitcnt lgkmcnt(0)\n"
      "v_mfma_f32_16x16x32_bf16 v[0:3], v[72:75], v[112:115], v[0:3]\n"
      "v_mfma_f32_16x16x32_bf16 v[4:7], v[76:79], v[112:115], v[4:7]\n"
      "v_mfma_f32_16x16x32_bf16 v[8:11], v[72:75], v[116:119], v[8:11]\n"
      "v_mfma_f32_16x16x32_bf16 v[12:15], v[76:79], v[116:119], v[12:15]\n"
      "v_mfma_f32_16x16x32_bf16 v[16:19], v[72:75], v[120:123], v[16:19]\n"
      "v_mfma_f32_16x16x32_bf16 v[20:23], v[76:79], v[120:123], v[20:23]\n"
      "v_mfma_f32_16x16x32_bf16 v[24:27], v[72:75], v[124:127], v[24:27]\n"
      "v_mfma_f32_16x16x32_bf16 v[28:31], v[76:79], v[124:127], v[28:31]\n"
      "s_setprio 2\n"
      "s_mov_b32 s14, s69\n"
      "s_cmp_eq_u32 s46, 1\n"
      "s_cbranch_scc1 LF1DONE\n"
      "s_cmp_lg_u32 s56, 3\n"
      "s_cbranch_scc1 Lwt_7\n"
      "s_and_b32 s60, s52, 7\n"
      "s_mul_i32 s62, s60, 65536\n"
      "buffer_load_dword v55, v252, s[24:27], s62 offen\n"
      "s_add_u32 s62, s62, 65536\n"
      "Lwt_7:\n"
      "s_lshl_b32 s61, s56, 11\n"
      "s_add_u32 s61, s61, s75\n"
      "buffer_store_dwordx4 v[88:91], v247, s[32:35], s61 offen offset:0 sc1\n"
      "buffer_store_dwordx4 v[88:91], v247, s[32:35], s61 offen offset:1024 sc1\n"
      "s_getpc_b64 s[72:73]\n"
      "Lret_8p:\n"
      "s_add_u32 s72, s72, Lret_8-Lret_8p\n"
      "s_addc_u32 s73, s73, 0\n"
      "s_branch LMISH\n"
      "Lret_8:\n"
      "s_nop 1\n"
      "s_cmp_eq_u32 s69, s15\n"
      "s_cbranch_scc1 LONE\n"
      "s_mov_b32 s69, s15\n"
      "s_add_u32 s58, s100, 3\n"
      "s_and_b32 s58, s58, 15\n"
      "s_mul_i32 s58, s58, 114688\n"
      "s_add_u32 s67, s58, s69\n"
      "v_mov_b64 v[128:129], v[136:137]\n"
      "v_mov_b64 v[130:131], v[138:139]\n"
      "v_mov_b64 v[132:133], v[140:141]\n"
      "v_mov_b64 v[134:135], v[142:143]\n"
      "v_mov_b32 v211, v217\n"
      "v_mov_b32 v50, 0x7ffffff0\n"
      "v_mov_b32 v51, 0x7ffffff0\n"
      "s_mov_b32 s46, 1\n"
      "s_waitcnt vmcnt(4)\n"
      "s_branch LF1\n"
      "LMISH:\n"
      "s_mov_b32 s96, 0x3f3504f3\n"
      "s_mul_i32 s60, s52, 294912\n"
      "s_lshl_b32 s61, s14, 6\n"
      "s_add_u32 s60, s60, s61\n"
      "s_add_u32 s60, s60, 2097152\n"
      "v_exp_f32 v160, v0\n"
      "v_exp_f32 v161, v1\n"
      "v_exp_f32 v162, v2\n"
      "v_exp_f32 v163, v3\n"
      "v_exp_f32 v164, v4\n"
      "v_exp_f32 v165, v5\n"
      "v_exp_f32 v166, v6\n"
      "v_exp_f32 v167, v7\n"
      "v_pk_fma_f32 v[160:161], v[160:161], s[96:97], s[96:97] op_sel_hi:[1,0,0]\n"
      "v_pk_fma_f32 v[162:163], v[162:163], s[96:97], s[96:97] op_sel_hi:[1,0,0]\n"
      "v_pk_fma_f32 v[164:165], v[164:165], s[96:97], s[96:97] op_sel_hi:[1,0,0]\n"
      "v_pk_fma_f32 v[166:167], v[166:167], s[96:97], s[96:97] op_sel_hi:[1,0,0]\n"
      "v_pk_fma_f32 v[160:161], v[160:161], v[160:161], 0.5 op_sel_hi:[1,1,0]\n"
      "v_pk_fma_f32 v[162:163], v[162:163], v[162:163], 0.5 op_sel_hi:[1,1,0]\n"
      "v_pk_fma_f32 v[164:165], v[164:165], v[164:165], 0.5 op_sel_hi:[1,1,0]\n"
      "v_pk_fma_f32 v[166:167], v[166:167], v[166:167], 0.5 op_sel_hi:[1,1,0]\n"
      "v_rcp_f32 v160, v160\n"
      "v_rcp_f32 v161, v161\n"
      "v_rcp_f32 v162, v162\n"
      "v_rcp_f32 v163, v163\n"
      "v_rcp_f32 v164, v164\n"
      "v_rcp_f32 v165, v165\n"
      "v_rcp_f32 v166, v166\n"
      "v_rcp_f32 v167, v167\n"
      "v_pk_fma_f32 v[168:169], v[0:1], v[160:161], v[0:1] neg_lo:[1,0,0] neg_hi:[1,0,0]\n"
      "v_pk_fma_f32 v[170:171], v[2:3], v[162:163], v[2:3] neg_lo:[1,0,0] neg_hi:[1,0,0]\n"
      "v_pk_fma_f32 v[172:173], v[4:5], v[164:165], v[4:5] neg_lo:[1,0,0] neg_hi:[1,0,0]\n"
      "v_pk_fma_f32 v[174:175], v[6:7], v[166:167], v[6:7] neg_lo:[1,0,0] neg_hi:[1,0,0]\n"
      "v_cvt_pk_bf16_f32 v176, v168, v169\n"
      "v_cvt_pk_bf16_f32 v177, v170, v171\n"
      "v_cvt_pk_bf16_f32 v178, v172, v173\n"
      "v_cvt_pk_bf16_f32 v179, v174, v175\n"
      "buffer_store_dwordx4 v[176:179], v247, s[32:35], s60 offen offset:0 sc1\n"
      "v_exp_f32 v160, v8\n"
      "v_exp_f32 v161, v9\n"
      "v_exp_f32 v162, v10\n"
      "v_exp_f32 v163, v11\n"
      "v_exp_f32 v164, v12\n"
      "v_exp_f32 v165, v13\n"
      "v_exp_f32 v166, v14\n"
      "v_exp_f32 v167, v15\n"
      "v_pk_fma_f32 v[160:161], v[160:161], s[96:97], s[96:97] op_sel_hi:[1,0,0]\n"
      "v_pk_fma_f32 v[162:163], v[162:163], s[96:97], s[96:97] op_sel_hi:[1,0,0]\n"
      "v_pk_fma_f32 v[164:165], v[164:165], s[96:97], s[96:97] op_sel_hi:[1,0,0]\n"
      "v_pk_fma_f32 v[166:167], v[166:167], s[96:97], s[96:97] op_sel_hi:[1,0,0]\n"
      "v_pk_fma_f32 v[160:161], v[160:161], v[160:161], 0.5 op_sel_hi:[1,1,0]\n"
      "v_pk_fma_f32 v[162:163], v[162:163], v[162:163], 0.5 op_sel_hi:[1,1,0]\n"
      "v_pk_fma_f32 v[164:165], v[164:165], v[164:165], 0.5 op_sel_hi:[1,1,0]\n"
      "v_pk_fma_f32 v[166:167], v[166:167], v[166:167], 0.5 op_sel_hi:[1,1,0]\n"
      "v_rcp_f32 v160, v160\n"
      "v_rcp_f32 v161, v161\n"
      "v_rcp_f32 v162, v162\n"
      "v_rcp_f32 v163, v163\n"
      "v_rcp_f32 v164, v164\n"
      "v_rcp_f32 v165, v165\n"
      "v_rcp_f32 v166, v166\n"
      "v_rcp_f32 v167, v167\n"
      "v_pk_fma_f32 v[168:169], v[8:9], v[160:161], v[8:9] neg_lo:[1,0,0] neg_hi:[1,0,0]\n"
      "v_pk_fma_f32 v[170:171], v[10:11], v[162:163], v[10:11] neg_lo:[1,0,0] neg_hi:[1,0,0]\n"
      "v_pk_fma_f32 v[172:173], v[12:13], v[164:165], v[12:13] neg_lo:[1,0,0] neg_hi:[1,0,0]\n"
      "v_pk_fma_f32 v[174:175], v[14:15], v[166:167], v[14:15] neg_lo:[1,0,0] neg_hi:[1,0,0]\n"
      "v_cvt_pk_bf16_f32 v180, v168, v169\n"
      "v_cvt_pk_bf16_f32 v181, v170, v171\n"
      "v_cvt_pk_bf16_f32 v182, v172, v173\n"
      "v_cvt_pk_bf16_f32 v183, v174, v175\n"
      "buffer_store_dwordx4 v[180:183], v247, s[32:35], s60 offen offset:1024 sc1\n"
      "v_exp_f32 v160, v16\n"
      "v_exp_f32 v161, v17\n"
      "v_exp_f32 v162, v18\n"
      "v_exp_f32 v163, v19\n"
      "v_exp_f32 v164, v20\n"
      "v_exp_f32 v165, v21\n"
      "v_exp_f32 v166, v22\n"
      "v_exp_f32 v167, v23\n"
      "v_pk_fma_f32 v[160:161], v[160:161], s[96:97], s[96:97] op_sel_hi:[1,0,0]\n"
      "v_pk_fma_f32 v[162:163], v[162:163], s[96:97], s[96:97] op_sel_hi:[1,0,0]\n"
      "v_pk_fma_f32 v[164:165], v[164:165], s[96:97], s[96:97] op_sel_hi:[1,0,0]\n"
      "v_pk_fma_f32 v[166:167], v[166:167], s[96:97], s[96:97] op_sel_hi:[1,0,0]\n"
      "v_pk_fma_f32 v[160:161], v[160:161], v[160:161], 0.5 op_sel_hi:[1,1,0]\n"
      "v_pk_fma_f32 v[162:163], v[162:163], v[162:163], 0.5 op_sel_hi:[1,1,0]\n"
      "v_pk_fma_f32 v[164:165], v[164:165], v[164:165], 0.5 op_sel_hi:[1,1,0]\n"
      "v_pk_fma_f32 v[166:167], v[166:167], v[166:167], 0.5 op_sel_hi:[1,1,0]\n"
      "v_rcp_f32 v160, v160\n"
      "v_rcp_f32 v161, v161\n"
      "v_rcp_f32 v162, v162\n"
      "v_rcp_f32 v163, v163\n"
      "v_rcp_f32 v164, v164\n"
      "v_rcp_f32 v165, v165\n"
      "v_rcp_f32 v166, v166\n"
      "v_rcp_f32 v167, v167\n"
      "v_pk_fma_f32 v[168:169], v[16:17], v[160:161], v[16:17] neg_lo:[1,0,0] neg_hi:[1,0,0]\n"
      "v_pk_fma_f32 v[170:171], v[18:19], v[162:163], v[18:19] neg_lo:[1,0,0] neg_hi:[1,0,0]\n"
      "v_pk_fma_f32 v[172:173], v[20:21], v[164:165], v[20:21] neg_lo:[1,0,0] neg_hi:[1,0,0]\n"
      "v_pk_fma_f32 v[174:175], v[22:23], v[166:167], v[22:23] neg_lo:[1,0,0] neg_hi:[1,0,0]\n"
      "v_cvt_pk_bf16_f32 v184, v168, v169\n"
      "v_cvt_pk_bf16_f32 v185, v170, v171\n"
      "v_cvt_pk_bf16_f32 v186, v172, v173\n"
      "v_cvt_pk_bf16_f32 v187, v174, v175\n"
      "buffer_store_dwordx4 v[184:187], v247, s[32:35], s60 offen offset:2048 sc1\n"
      "v_exp_f32 v160, v24\n"
      "v_exp_f32 v161, v25\n"
      "v_exp_f32 v162, v26\n"
      "v_exp_f32 v163, v27\n"
      "v_exp_f32 v164, v28\n"
      "v_exp_f32 v165, v29\n"
      "v_exp_f32 v166, v30\n"
      "v_exp_f32 v167, v31\n"
      "v_pk_fma_f32 v[160:161], v[160:161], s[96:97], s[96:97] op_sel_hi:[1,0,0]\n"
      "v_pk_fma_f32 v[162:163], v[162:163], s[96:97], s[96:97] op_sel_hi:[1,0,0]\n"
      "v_pk_fma_f32 v[164:165], v[164:165], s[96:97], s[96:97] op_sel_hi:[1,0,0]\n"
      "v_pk_fma_f32 v[166:167], v[166:167], s[96:97], s[96:97] op_sel_hi:[1,0,0]\n"
      "v_pk_fma_f32 v[160:161], v[160:161], v[160:161], 0.5 op_sel_hi:[1,1,0]\n"
      "v_pk_fma_f32 v[162:163], v[162:163], v[162:163], 0.5 op_sel_hi:[1,1,0]\n"
      "v_pk_fma_f32 v[164:165], v[164:165], v[164:165], 0.5 op_sel_hi:[1,1,0]\n"
      "v_pk_fma_f32 v[166:167], v[166:167], v[166:167], 0.5 op_sel_hi:[1,1,0]\n"
      "v_rcp_f32 v160, v160\n"
      "v_rcp_f32 v161, v161\n"
      "v_rcp_f32 v162, v162\n"
      "v_rcp_f32 v163, v163\n"
      "v_rcp_f32 v164, v164\n"
      "v_rcp_f32 v165, v165\n"
      "v_rcp_f32 v166, v166\n"
      "v_rcp_f32 v167, v167\n"
      "v_pk_fma_f32 v[168:169], v[24:25], v[160:161], v[24:25] neg_lo:[1,0,0] neg_hi:[1,0,0]\n"
      "v_pk_fma_f32 v[170:171], v[26:27], v[162:163], v[26:27] neg_lo:[1,0,0] neg_hi:[1,0,0]\n"
      "v_pk_fma_f32 v[172:173], v[28:29], v[164:165], v[28:29] neg_lo:[1,0,0] neg_hi:[1,0,0]\n"
      "v_pk_fma_f32 v[174:175], v[30:31], v[166:167], v[30:31] neg_lo:[1,0,0] neg_hi:[1,0,0]\n"
      "v_cvt_pk_bf16_f32 v188, v168, v169\n"
      "v_cvt_pk_bf16_f32 v189, v170, v171\n"
      "v_cvt_pk_bf16_f32 v190, v172, v173\n"
      "v_cvt_pk_bf16_f32 v191, v174, v175\n"
      "buffer_store_dwordx4 v[188:191], v247, s[32:35], s60 offen offset:3072 sc1\n"
      "s_lshl_b32 s60, s52, 1\n"
      "s_add_u32 s60, s60, s46\n"
      "s_lshl_b32 s60, s60, 3\n"
      "s_add_u32 s60, s60, s53\n"
      "s_lshl_b32 s60, s60, 2\n"
      "s_add_u32 s60, s60, s56\n"
      "s_lshl_b32 s60, s60, 6\n"
      "s_add_u32 s60, s60, 65536\n"
      "v_mov_b32 v210, s60\n"
      "s_setpc_b64 s[72:73]\n"
      "LONE:\n"
      "s_waitcnt vmcnt(0)\n"
      "s_mov_b64 exec, 1\n"
      "buffer_store_dwordx2 v[208:209], v210, s[32:35], 0 offen sc1\n"
      "s_mov_b64 exec, -1\n"
      "v_mov_b32 v210, 0x7ffffff0\n"
      "s_sleep 16\n"
      "s_mul_i32 s69, s52, 294912\n"
      "s_add_u32 s69, s69, 2097152\n"
      "s_cmp_eq_u32 s56, 3\n"
      "s_cselect_b32 s68, 129024, s68\n"
      "s_and_b32 s45, s21, 15\n"
      "s_lshr_b32 s44, s21, 4\n"
      "s_mov_b32 s66, 0\n"
      "s_mov_b64 s[98:99], -1\n"
      "Lpf_9:\n"
      "s_mov_b64 exec, s[98:99]\n"
      "buffer_load_dwordx2 v[212:213], v217, s[32:35], 0 offen sc1\n"
      "s_mov_b64 exec, -1\n"
      "s_waitcnt vmcnt(0)\n"
      "Lpfc_10:\n"
      "v_and_b32 v214, 0xffff0000, v212\n"
      "v_xor_b32 v214, s70, v214\n"
      "v_xor_b32 v215, s71, v213\n"
      "v_or_b32 v214, v214, v215\n"
      "v_cmp_ne_u32 vcc, 0, v214\n"
      "s_cbranch_vccz Lpfd_11\n"
      "s_mov_b64 s[98:99], vcc\n"
      "s_sleep 3\n"
      "s_add_u32 s66, s66, 1\n"
      "s_cmp_lt_u32 s66, 131072\n"
      "s_cbranch_scc1 Lpf_9\n"
      "Lpfd_11:\n"
      "s_mov_b32 s12, s47\n"
      "s_bfe_u32 s47, s16, 0x80000\n"
      "s_lshl_b32 s67, s47, 15\n"
      "s_mov_b32 m0, s68\n"
      "s_nop 0\n"
      "buffer_load_dwordx4 v232, s[8:11], s67 offen offset:0 lds\n"
      "buffer_load_dwordx4 v233, s[8:11], s67 offen offset:1024 lds\n"
      "buffer_load_dwordx4 v234, s[8:11], s67 offen offset:2048 lds\n"
      "buffer_load_dwordx4 v235, s[8:11], s67 offen offset:3072 lds\n"
      "s_lshl_b32 s58, s47, 12\n"
      "s_add_u32 s58, s58, s69\n"
      "buffer_load_dwordx4 v[96:99], v247, s[32:35], s58 offen offset:0 sc1\n"
      "buffer_load_dwordx4 v[100:103], v247, s[32:35], s58 offen offset:1024 sc1\n"
      "buffer_load_dwordx4 v[104:107], v247, s[32:35], s58 offen offset:2048 sc1\n"
      "buffer_load_dwordx4 v[108:111], v247, s[32:35], s58 offen offset:3072 sc1\n"
      "s_mov_b32 s12, s47\n"
      "s_bfe_u32 s47, s16, 0x80008\n"
      "s_lshl_b32 s67, s47, 15\n"
      "s_add_u32 m0, s68, 4096\n"
      "s_nop 0\n"
      "buffer_load_dwordx4 v232, s[8:11], s67 offen offset:0 lds\n"
      "buffer_load_dwordx4 v233, s[8:11], s67 offen offset:1024 lds\n"
      "buffer_load_dwordx4 v234, s[8:11], s67 offen offset:2048 lds\n"
      "buffer_load_dwordx4 v235, s[8:11], s67 offen offset:3072 lds\n"
      "s_lshl_b32 s58, s47, 12\n"
      "s_add_u32 s58, s58, s69\n"
      "buffer_load_dwordx4 v[112:115], v247, s[32:35], s58 offen offset:0 sc1\n"
      "buffer_load_dwordx4 v[116:119], v247, s[32:35], s58 offen offset:1024 sc1\n"
      "buffer_load_dwordx4 v[120:123], v247, s[32:35], s58 offen offset:2048 sc1\n"
      "buffer_load_dwordx4 v[124:127], v247, s[32:35], s58 offen offset:3072 sc1\n"
      "s_mov_b32 s12, s47\n"
      "s_bfe_u32 s47, s16, 0x80010\n"
      "s_mov_b64 exec, 1\n"
      "buffer_load_dword v214, v247, s[28:31], 0 offen\n"
      "buffer_load_dword v214, v247, s[28:31], 0 offen\n"
      "buffer_load_dword v214, v247, s[28:31], 0 offen\n"
      "buffer_load_dword v214, v247, s[28:31], 0 offen\n"
      "s_mov_b64 exec, -1\n"
      "s_branch LF2\n"
      "LF1DONE:\n"
      "s_mul_i32 s69, s52, 294912\n"
      "s_add_u32 s69, s69, 2097152\n"
      "s_cmp_eq_u32 s56, 3\n"
      "s_cselect_b32 s68, 129024, s68\n"
      "s_and_b32 s45, s21, 15\n"
      "s_lshr_b32 s44, s21, 4\n"
      "s_mov_b32 s66, 0\n"
      "s_waitcnt vmcnt(0)\n"
      "s_branch Lpfc_13\n"
      "Lpf_12:\n"
      "s_mov_b64 exec, s[98:99]\n"
      "buffer_load_dwordx2 v[212:213], v217, s[32:35], 0 offen sc1\n"
      "s_mov_b64 exec, -1\n"
      "s_waitcnt vmcnt(0)\n"
      "Lpfc_13:\n"
      "v_and_b32 v214, 0xffff0000, v212\n"
      "v_xor_b32 v214, s70, v214\n"
      "v_xor_b32 v215, s71, v213\n"
      "v_or_b32 v214, v214, v215\n"
      "v_cmp_ne_u32 vcc, 0, v214\n"
      "s_cbranch_vccz Lpfd_14\n"
      "s_mov_b64 s[98:99], vcc\n"
      "s_sleep 3\n"
      "s_add_u32 s66, s66, 1\n"
      "s_cmp_lt_u32 s66, 131072\n"
      "s_cbranch_scc1 Lpf_12\n"
      "Lpfd_14:\n"
      "s_mov_b32 s12, s47\n"
      "s_bfe_u32 s47, s16, 0x80000\n"
      "s_lshl_b32 s67, s47, 15\n"
      "s_mov_b32 m0, s68\n"
      "s_nop 0\n"
      "buffer_load_dwordx4 v232, s[8:11], s67 offen offset:0 lds\n"
      "buffer_load_dwordx4 v233, s[8:11], s67 offen offset:1024 lds\n"
      "buffer_load_dwordx4 v234, s[8:11], s67 offen offset:2048 lds\n"
      "buffer_load_dwordx4 v235, s[8:11], s67 offen offset:3072 lds\n"
      "s_lshl_b32 s58, s47, 12\n"
      "s_add_u32 s58, s58, s69\n"
      "buffer_load_dwordx4 v[96:99], v247, s[32:35], s58 offen offset:0 sc1\n"
      "buffer_load_dwordx4 v[100:103], v247, s[32:35], s58 offen offset:1024 sc1\n"
      "buffer_load_dwordx4 v[104:107], v247, s[32:35], s58 offen offset:2048 sc1\n"
      "buffer_load_dwordx4 v[108:111], v247, s[32:35], s58 offen offset:3072 sc1\n"
      "s_mov_b32 s12, s47\n"
      "s_bfe_u32 s47, s16, 0x80008\n"
      "s_lshl_b32 s67, s47, 15\n"
      "s_add_u32 m0, s68, 4096\n"
      "s_nop 0\n"
      "buffer_load_dwordx4 v232, s[8:11], s67 offen offset:0 lds\n"
      "buffer_load_dwordx4 v233, s[8:11], s67 offen offset:1024 lds\n"
      "buffer_load_dwordx4 v234, s[8:11], s67 offen offset:2048 lds\n"
      "buffer_load_dwordx4 v235, s[8:11], s67 offen offset:3072 lds\n"
      "s_lshl_b32 s58, s47, 12\n"
      "s_add_u32 s58, s58, s69\n"
      "buffer_load_dwordx4 v[112:115], v247, s[32:35], s58 offen offset:0 sc1\n"
      "buffer_load_dwordx4 v[116:119], v247, s[32:35], s58 offen offset:1024 sc1\n"
      "buffer_load_dwordx4 v[120:123], v247, s[32:35], s58 offen offset:2048 sc1\n"
      "buffer_load_dwordx4 v[124:127], v247, s[32:35], s58 offen offset:3072 sc1\n"
      "s_mov_b32 s12, s47\n"
      "s_bfe_u32 s47, s16, 0x80010\n"
      "s_getpc_b64 s[72:73]\n"
      "Lret_15p:\n"
      "s_add_u32 s72, s72, Lret_15-Lret_15p\n"
      "s_addc_u32 s73, s73, 0\n"
      "s_branch LMISH\n"
      "Lret_15:\n"
      "s_nop 1\n"
      "LF2:\n"
      "s_setprio 0\n"
      "s_waitcnt vmcnt(16)\n"
      "ds_read_b64_tr_b16 v[64:65], v236 offset:0\n"
      "ds_read_b64_tr_b16 v[66:67], v236 offset:2048\n"
      "ds_read_b64_tr_b16 v[68:69], v237 offset:0\n"
      "ds_read_b64_tr_b16 v[70:71], v237 offset:2048\n"
      "ds_read_b64_tr_b16 v[72:73], v238 offset:0\n"
      "ds_read_b64_tr_b16 v[74:75], v238 offset:2048\n"
      "ds_read_b64_tr_b16 v[76:77], v239 offset:0\n"
      "ds_read_b64_tr_b16 v[78:79], v239 offset:2048\n"
      "s_waitcnt lgkmcnt(0)\n"
      "s_lshl_b32 s67, s47, 15\n"
      "s_mov_b32 m0, s68\n"
      "s_nop 0\n"
      "buffer_load_dwordx4 v232, s[8:11], s67 offen offset:0 lds\n"
      "buffer_load_dwordx4 v233, s[8:11], s67 offen offset:1024 lds\n"
      "buffer_load_dwordx4 v234, s[8:11], s67 offen offset:2048 lds\n"
      "buffer_load_dwordx4 v235, s[8:11], s67 offen offset:3072 lds\n"
      "s_mov_b32 s12, s47\n"
      "s_bfe_u32 s47, s16, 0x80018\n"
      "s_mov_b32 s16, s17\n"
      "s_mov_b32 s17, s18\n"
      "s_mov_b32 s18, s19\n"
      "s_mov_b32 s19, s20\n"
      "s_waitcnt vmcnt(16)\n"
      "v_mfma_f32_16x16x32_bf16 v[0:3], v[64:67], v[96:99], 0\n"
      "s_waitcnt vmcnt(12)\n"
      "ds_read_b64_tr_b16 v[80:81], v236 offset:4096\n"
      "ds_read_b64_tr_b16 v[82:83], v236 offset:6144\n"
      "v_mfma_f32_16x16x32_bf16 v[4:7], v[68:71], v[96:99], 0\n"
      "ds_read_b64_tr_b16 v[84:85], v237 offset:4096\n"
      "ds_read_b64_tr_b16 v[86:87], v237 offset:6144\n"
      "v_mfma_f32_16x16x32_bf16 v[8:11], v[72:75], v[96:99], 0\n"
      "ds_read_b64_tr_b16 v[88:89], v238 offset:4096\n"
      "ds_read_b64_tr_b16 v[90:91], v238 offset:6144\n"
      "v_mfma_f32_16x16x32_bf16 v[12:15], v[76:79], v[96:99], 0\n"
      "ds_read_b64_tr_b16 v[92:93], v239 offset:4096\n"
      "ds_read_b64_tr_b16 v[94:95], v239 offset:6144\n"
      "v_mfma_f32_16x16x32_bf16 v[16:19], v[64:67], v[100:103], 0\n"
      "s_lshl_b32 s58, s12, 12\n"
      "s_add_u32 s58, s58, s69\n"
      "buffer_load_dwordx4 v[128:131], v247, s[32:35], s58 offen offset:0 sc1\n"
      "v_mfma_f32_16x16x32_bf16 v[20:23], v[68:71], v[100:103], 0\n"
      "buffer_load_dwordx4 v[132:135], v247, s[32:35], s58 offen offset:1024 sc1\n"
      "v_mfma_f32_16x16x32_bf16 v[24:27], v[72:75], v[100:103], 0\n"
      "buffer_load_dwordx4 v[136:139], v247, s[32:35], s58 offen offset:2048 sc1\n"
      "v_mfma_f32_16x16x32_bf16 v[28:31], v[76:79], v[100:103], 0\n"
      "buffer_load_dwordx4 v[140:143], v247, s[32:35], s58 offen offset:3072 sc1\n"
      "v_mfma_f32_16x16x32_bf16 v[32:35], v[64:67], v[104:107], 0\n"
      "s_waitcnt lgkmcnt(0)\n"
      "s_lshl_b32 s67, s47, 15\n"
      "s_add_u32 m0, s68, 4096\n"
      "s_nop 0\n"
      "buffer_load_dwordx4 v232, s[8:11], s67 offen offset:0 lds\n"
      "v_mfma_f32_16x16x32_bf16 v[36:39], v[68:71], v[104:107], 0\n"
      "buffer_load_dwordx4 v233, s[8:11], s67 offen offset:1024 lds\n"
      "v_mfma_f32_16x16x32_bf16 v[40:43], v[72:75], v[104:107], 0\n"
      "buffer_load_dwordx4 v234, s[8:11], s67 offen offset:2048 lds\n"
      "v_mfma_f32_16x16x32_bf16 v[44:47], v[76:79], v[104:107], 0\n"
      "buffer_load_dwordx4 v235, s[8:11], s67 offen offset:3072 lds\n"
      "v_mfma_f32_16x16x32_bf16 v[48:51], v[64:67], v[108:111], 0\n"
      "v_mfma_f32_16x16x32_bf16 v[52:55], v[68:71], v[108:111], 0\n"
      "s_mov_b32 s12, s47\n"
      "v_mfma_f32_16x16x32_bf16 v[56:59], v[72:75], v[108:111], 0\n"
      "s_bfe_u32 s47, s16, 0x80000\n"
      "v_mfma_f32_16x16x32_bf16 v[60:63], v[76:79], v[108:111], 0\n"
      "s_waitcnt vmcnt(16)\n"
      "v_mfma_f32_16x16x32_bf16 v[0:3], v[80:83], v[112:115], v[0:3]\n"
      "s_waitcnt vmcnt(8)\n"
      "s_mov_b64 exec, 1\n"
      "buffer_store_dwordx2 v[208:209], v210, s[32:35], 0 offen sc1\n"
      "s_mov_b64 exec, -1\n"
      "ds_read_b64_tr_b16 v[64:65], v236 offset:0\n"
      "ds_read_b64_tr_b16 v[66:67], v236 offset:2048\n"
      "v_mfma_f32_16x16x32_bf16 v[4:7], v[84:87], v[112:115], v[4:7]\n"
      "ds_read_b64_tr_b16 v[68:69], v237 offset:0\n"
      "ds_read_b64_tr_b16 v[70:71], v237 offset:2048\n"
      "v_mfma_f32_16x16x32_bf16 v[8:11], v[88:91], v[112:115], v[8:11]\n"
      "ds_read_b64_tr_b16 v[72:73], v238 offset:0\n"
      "ds_read_b64_tr_b16 v[74:75], v238 offset:2048\n"
      "v_mfma_f32_16x16x32_bf16 v[12:15], v[92:95], v[112:115], v[12:15]\n"
      "ds_read_b64_tr_b16 v[76:77], v239 offset:0\n"
      "ds_read_b64_tr_b16 v[78:79], v239 offset:2048\n"
      "v_mfma_f32_16x16x32_bf16 v[16:19], v[80:83], v[116:119], v[16:19]\n"
      "s_lshl_b32 s58, s12, 12\n"
      "s_add_u32 s58, s58, s69\n"
      "buffer_load_dwordx4 v[144:147], v247, s[32:35], s58 offen offset:0 sc1\n"
      "v_mfma_f32_16x16x32_bf16 v[20:23], v[84:87], v[116:119], v[20:23]\n"
      "buffer_load_dwordx4 v[148:151], v247, s[32:35], s58 offen offset:1024 sc1\n"
      "v_mfma_f32_16x16x32_bf16 v[24:27], v[88:91], v[116:119], v[24:27]\n"
      "buffer_load_dwordx4 v[152:155], v247, s[32:35], s58 offen offset:2048 sc1\n"
      "v_mfma_f32_16x16x32_bf16 v[28:31], v[92:95], v[116:119], v[28:31]\n"
      "buffer_load_dwordx4 v[156:159], v247, s[32:35], s58 offen offset:3072 sc1\n"
      "v_mfma_f32_16x16x32_bf16 v[32:35], v[80:83], v[120:123], v[32:35]\n"
      "s_waitcnt lgkmcnt(0)\n"
      "s_lshl_b32 s67, s47, 15\n"
      "s_mov_b32 m0, s68\n"
      "s_nop 0\n"
      "buffer_load_dwordx4 v232, s[8:11], s67 offen offset:0 lds\n"
      "v_mfma_f32_16x16x32_bf16 v[36:39], v[84:87], v[120:123], v[36:39]\n"
      "buffer_load_dwordx4 v233, s[8:11], s67 offen offset:1024 lds\n"
      "v_mfma_f32_16x16x32_bf16 v[40:43], v[88:91], v[120:123], v[40:43]\n"
      "buffer_load_dwordx4 v234, s[8:11], s67 offen offset:2048 lds\n"
      "v_mfma_f32_16x16x32_bf16 v[44:47], v[92:95], v[120:123], v[44:47]\n"
      "buffer_load_dwordx4 v235, s[8:11], s67 offen offset:3072 lds\n"
      "v_mfma_f32_16x16x32_bf16 v[48:51], v[80:83], v[124:127], v[48:51]\n"
      "v_mfma_f32_16x16x32_bf16 v[52:55], v[84:87], v[124:127], v[52:55]\n"
      "s_mov_b32 s12, s47\n"
      "v_mfma_f32_16x16x32_bf16 v[56:59], v[88:91], v[124:127], v[56:59]\n"
      "s_bfe_u32 s47, s16, 0x80008\n"
      "v_mfma_f32_16x16x32_bf16 v[60:63], v[92:95], v[124:127], v[60:63]\n"
      "s_cmp_ge_u32 s76, 4\n"
      "s_cbranch_scc1 Lhk_16\n"
      "s_setprio 2\n"
      "s_getpc_b64 s[72:73]\n"
      "Lret_17p:\n"
      "s_add_u32 s72, s72, Lret_17-Lret_17p\n"
      "s_addc_u32 s73, s73, 0\n"
      "s_branch LTRY\n"
      "Lret_17:\n"
      "s_nop 1\n"
      "s_setprio 0\n"
      "Lhk_16:\n"
      "s_waitcnt vmcnt(13)\n"
      "v_mfma_f32_16x16x32_bf16 v[0:3], v[64:67], v[128:131], v[0:3]\n"
      "s_waitcnt vmcnt(9)\n"
      "ds_read_b64_tr_b16 v[80:81], v236 offset:4096\n"
      "ds_read_b64_tr_b16 v[82:83], v236 offset:6144\n"
      "v_mfma_f32_16x16x32_bf16 v[4:7], v[68:71], v[128:131], v[4:7]\n"
      "ds_read_b64_tr_b16 v[84:85], v237 offset:4096\n"
      "ds_read_b64_tr_b16 v[86:87], v237 offset:6144\n"
      "v_mfma_f32_16x16x32_bf16 v[8:11], v[72:75], v[128:131], v[8:11]\n"
      "ds_read_b64_tr_b16 v[88:89], v238 offset:4096\n"
      "ds_read_b64_tr_b16 v[90:91], v238 offset:6144\n"
      "v_mfma_f32_16x16x32_bf16 v[12:15], v[76:79], v[128:131], v[12:15]\n"
      "ds_read_b64_tr_b16 v[92:93], v239 offset:4096\n"
      "ds_read_b64_tr_b16 v[94:95], v239 offset:6144\n"
      "v_mfma_f32_16x16x32_bf16 v[16:19], v[64:67], v[132:135], v[16:19]\n"
      "s_lshl_b32 s58, s12, 12\n"
      "s_add_u32 s58, s58, s69\n"
      "buffer_load_dwordx4 v[96:99], v247, s[32:35], s58 offen offset:0 sc1\n"
      "v_mfma_f32_16x16x32_bf16 v[20:23], v[68:71], v[132:135], v[20:23]\n"
      "buffer_load_dwordx4 v[100:103], v247, s[32:35], s58 offen offset:1024 sc1\n"
      "v_mfma_f32_16x16x32_bf16 v[24:27], v[72:75], v[132:135], v[24:27]\n"
      "buffer_load_dwordx4 v[104:107], v247, s[32:35], s58 offen offset:2048 sc1\n"
      "v_mfma_f32_16x16x32_bf16 v[28:31], v[76:79], v[132:135], v[28:31]\n"
      "buffer_load_dwordx4 v[108:111], v247, s[32:35], s58 offen offset:3072 sc1\n"
      "v_mfma_f32_16x16x32_bf16 v[32:35], v[64:67], v[136:139], v[32:35]\n"
      "s_waitcnt lgkmcnt(0)\n"
      "s_lshl_b32 s67, s47, 15\n"
      "s_add_u32 m0, s68, 4096\n"
      "s_nop 0\n"
      "buffer_load_dwordx4 v232, s[8:11], s67 offen offset:0 lds\n"
      "v_mfma_f32_16x16x32_bf16 v[36:39], v[68:71], v[136:139], v[36:39]\n"
      "buffer_load_dwordx4 v233, s[8:11], s67 offen offset:1024 lds\n"
      "v_mfma_f32_16x16x32_bf16 v[40:43], v[72:75], v[136:139], v[40:43]\n"
      "buffer_load_dwordx4 v234, s[8:11], s67 offen offset:2048 lds\n"
      "v_mfma_f32_16x16x32_bf16 v[44:47], v[76:79], v[136:139], v[44:47]\n"
      "buffer_load_dwordx4 v235, s[8:11], s67 offen offset:3072 lds\n"
      "v_mfma_f32_16x16x32_bf16 v[48:51], v[64:67], v[140:143], v[48:51]\n"
      "v_mfma_f32_16x16x32_bf16 v[52:55], v[68:71], v[140:143], v[52:55]\n"
      "s_mov_b32 s12, s47\n"
      "v_mfma_f32_16x16x32_bf16 v[56:59], v[72:75], v[140:143], v[56:59]\n"
      "s_bfe_u32 s47, s16, 0x80010\n"
      "v_mfma_f32_16x16x32_bf16 v[60:63], v[76:79], v[140:143], v[60:63]\n"
      "s_waitcnt vmcnt(12)\n"
      "v_mfma_f32_16x16x32_bf16 v[0:3], v[80:83], v[144:147], v[0:3]\n"
      "s_waitcnt vmcnt(8)\n"
      "ds_read_b64_tr_b16 v[64:65], v236 offset:0\n"
      "ds_read_b64_tr_b16 v[66:67], v236 offset:2048\n"
      "v_mfma_f32_16x16x32_bf16 v[4:7], v[84:87], v[144:147], v[4:7]\n"
      "ds_read_b64_tr_b16 v[68:69], v237 offset:0\n"
      "ds_read_b64_tr_b16 v[70:71], v237 offset:2048\n"
      "v_mfma_f32_16x16x32_bf16 v[8:11], v[88:91], v[144:147], v[8:11]\n"
      "ds_read_b64_tr_b16 v[72:73], v238 offset:0\n"
      "ds_read_b64_tr_b16 v[74:75], v238 offset:2048\n"
      "v_mfma_f32_16x16x32_bf16 v[12:15], v[92:95], v[144:147], v[12:15]\n"
      "ds_read_b64_tr_b16 v[76:77], v239 offset:0\n"
      "ds_read_b64_tr_b16 v[78:79], v239 offset:2048\n"
      "v_mfma_f32_16x16x32_bf16 v[16:19], v[80:83], v[148:151], v[16:19]\n"
      "s_sub_u32 s45, s45, 2\n"
      "s_cmp_eq_u32 s45, 0\n"
      "s_cbranch_scc0 Lpp_18\n"
      "buffer_load_dwordx2 v[212:213], v216, s[32:35], 0 offen sc1\n"
      "Lpp_18:\n"
      "s_lshl_b32 s58, s12, 12\n"
      "s_add_u32 s58, s58, s69\n"
      "buffer_load_dwordx4 v[112:115], v247, s[32:35], s58 offen offset:0 sc1\n"
      "v_mfma_f32_16x16x32_bf16 v[20:23], v[84:87], v[148:151], v[20:23]\n"
      "buffer_load_dwordx4 v[116:119], v247, s[32:35], s58 offen offset:1024 sc1\n"
      "v_mfma_f32_16x16x32_bf16 v[24:27], v[88:91], v[148:151], v[24:27]\n"
      "buffer_load_dwordx4 v[120:123], v247, s[32:35], s58 offen offset:2048 sc1\n"
      "v_mfma_f32_16x16x32_bf16 v[28:31], v[92:95], v[148:151], v[28:31]\n"
      "buffer_load_dwordx4 v[124:127], v247, s[32:35], s58 offen offset:3072 sc1\n"
      "v_mfma_f32_16x16x32_bf16 v[32:35], v[80:83], v[152:155], v[32:35]\n"
      "s_waitcnt lgkmcnt(0)\n"
      "s_lshl_b32 s67, s47, 15\n"
      "s_mov_b32 m0, s68\n"
      "s_nop 0\n"
      "buffer_load_dwordx4 v232, s[8:11], s67 offen offset:0 lds\n"
      "v_mfma_f32_16x16x32_bf16 v[36:39], v[84:87], v[152:155], v[36:39]\n"
      "buffer_load_dwordx4 v233, s[8:11], s67 offen offset:1024 lds\n"
      "v_mfma_f32_16x16x32_bf16 v[40:43], v[88:91], v[152:155], v[40:43]\n"
      "buffer_load_dwordx4 v234, s[8:11], s67 offen offset:2048 lds\n"
      "v_mfma_f32_16x16x32_bf16 v[44:47], v[92:95], v[152:155], v[44:47]\n"
      "buffer_load_dwordx4 v235, s[8:11], s67 offen offset:3072 lds\n"
      "v_mfma_f32_16x16x32_bf16 v[48:51], v[80:83], v[156:159], v[48:51]\n"
      "v_mfma_f32_16x16x32_bf16 v[52:55], v[84:87], v[156:159], v[52:55]\n"
      "s_mov_b32 s12, s47\n"
      "s_bfe_u32 s47, s16, 0x80018\n"
      "v_mfma_f32_16x16x32_bf16 v[56:59], v[88:91], v[156:159], v[56:59]\n"
      "s_mov_b32 s16, s17\n"
      "s_mov_b32 s17, s18\n"
      "v_mfma_f32_16x16x32_bf16 v[60:63], v[92:95], v[156:159], v[60:63]\n"
      "s_mov_b32 s18, s19\n"
      "s_mov_b32 s19, s20\n"
      "s_cmp_eq_u32 s44, 0\n"
      "s_cbranch_scc1 LF2L\n"
      "LF2M:\n"
      "s_cmp_eq_u32 s45, 0\n"
      "s_cbranch_scc0 Lret_19\n"
      "s_getpc_b64 s[22:23]\n"
      "s_branch LF2POLL\n"
      "Lret_19:\n"
      "s_waitcnt vmcnt(12)\n"
      "v_mfma_f32_16x16x32_bf16 v[0:3], v[64:67], v[96:99], v[0:3]\n"
      "s_waitcnt vmcnt(8)\n"
      "ds_read_b64_tr_b16 v[80:81], v236 offset:4096\n"
      "ds_read_b64_tr_b16 v[82:83], v236 offset:6144\n"
      "v_mfma_f32_16x16x32_bf16 v[4:7], v[68:71], v[96:99], v[4:7]\n"
      "ds_read_b64_tr_b16 v[84:85], v237 offset:4096\n"
      "ds_read_b64_tr_b16 v[86:87], v237 offset:6144\n"
      "v_mfma_f32_16x16x32_bf16 v[8:11], v[72:75], v[96:99], v[8:11]\n"
      "ds_read_b64_tr_b16 v[88:89], v238 offset:4096\n"
      "ds_read_b64_tr_b16 v[90:91], v238 offset:6144\n"
      "v_mfma_f32_16x16x32_bf16 v[12:15], v[76:79], v[96:99], v[12:15]\n"
      "ds_read_b64_tr_b16 v[92:93], v239 offset:4096\n"
      "ds_read_b64_tr_b16 v[94:95], v239 offset:6144\n"
      "v_mfma_f32_16x16x32_bf16 v[16:19], v[64:67], v[100:103], v[16:19]\n"
      "s_lshl_b32 s58, s12, 12\n"
      "s_add_u32 s58, s58, s69\n"
      "buffer_load_dwordx4 v[128:131], v247, s[32:35], s58 offen offset:0 sc1\n"
      "v_mfma_f32_16x16x32_bf16 v[20:23], v[68:71], v[100:103], v[20:23]\n"
      "buffer_load_dwordx4 v[132:135], v247, s[32:35], s58 offen offset:1024 sc1\n"
      "v_mfma_f32_16x16x32_bf16 v[24:27], v[72:75], v[100:103], v[24:27]\n"
      "buffer_load_dwordx4 v[136:139], v247, s[32:35], s58 offen offset:2048 sc1\n"
      "v_mfma_f32_16x16x32_bf16 v[28:31], v[76:79], v[100:103], v[28:31]\n"
      "buffer_load_dwordx4 v[140:143], v247, s[32:35], s58 offen offset:3072 sc1\n"
      "v_mfma_f32_16x16x32_bf16 v[32:35], v[64:67], v[104:107], v[32:35]\n"
      "s_waitcnt lgkmcnt(0)\n"
      "s_lshl_b32 s67, s47, 15\n"
      "s_add_u32 m0, s68, 4096\n"
      "s_nop 0\n"
      "buffer_load_dwordx4 v232, s[8:11], s67 offen offset:0 lds\n"
      "v_mfma_f32_16x16x32_bf16 v[36:39], v[68:71], v[104:107], v[36:39]\n"
      "buffer_load_dwordx4 v233, s[8:11], s67 offen offset:1024 lds\n"
      "v_mfma_f32_16x16x32_bf16 v[40:43], v[72:75], v[104:107], v[40:43]\n"
      "buffer_load_dwordx4 v234, s[8:11], s67 offen offset:2048 lds\n"
      "v_mfma_f32_16x16x32_bf16 v[44:47], v[76:79], v[104:107], v[44:47]\n"
      "buffer_load_dwordx4 v235, s[8:11], s67 offen offset:3072 lds\n"
      "v_mfma_f32_16x16x32_bf16 v[48:51], v[64:67], v[108:111], v[48:51]\n"
      "v_mfma_f32_16x16x32_bf16 v[52:55], v[68:71], v[108:111], v[52:55]\n"
      "s_mov_b32 s12, s47\n"
      "v_mfma_f32_16x16x32_bf16 v[56:59], v[72:75], v[108:111], v[56:59]\n"
      "s_bfe_u32 s47, s16, 0x80000\n"
      "v_mfma_f32_16x16x32_bf16 v[60:63], v[76:79], v[108:111], v[60:63]\n"
      "s_cmp_ge_u32 s76, 4\n"
      "s_cbranch_scc1 Lhk_20\n"
      "s_setprio 2\n"
      "s_getpc_b64 s[72:73]\n"
      "Lret_21p:\n"
      "s_add_u32 s72, s72, Lret_21-Lret_21p\n"
      "s_addc_u32 s73, s73, 0\n"
      "s_branch LTRY\n"
      "Lret_21:\n"
      "s_nop 1\n"
      "s_setprio 0\n"
      "Lhk_20:\n"
      "s_waitcnt vmcnt(12)\n"
      "v_mfma_f32_16x16x32_bf16 v[0:3], v[80:83], v[112:115], v[0:3]\n"
      "s_waitcnt vmcnt(8)\n"
      "ds_read_b64_tr_b16 v[64:65], v236 offset:0\n"
      "ds_read_b64_tr_b16 v[66:67], v236 offset:2048\n"
      "v_mfma_f32_16x16x32_bf16 v[4:7], v[84:87], v[112:115], v[4:7]\n"
      "ds_read_b64_tr_b16 v[68:69], v237 offset:0\n"
      "ds_read_b64_tr_b16 v[70:71], v237 offset:2048\n"
      "v_mfma_f32_16x16x32_bf16 v[8:11], v[88:91], v[112:115], v[8:11]\n"
      "ds_read_b64_tr_b16 v[72:73], v238 offset:0\n"
      "ds_read_b64_tr_b16 v[74:75], v238 offset:2048\n"
      "v_mfma_f32_16x16x32_bf16 v[12:15], v[92:95], v[112:115], v[12:15]\n"
      "ds_read_b64_tr_b16 v[76:77], v239 offset:0\n"
      "ds_read_b64_tr_b16 v[78:79], v239 offset:2048\n"
      "v_mfma_f32_16x16x32_bf16 v[16:19], v[80:83], v[116:119], v[16:19]\n"
      "s_sub_u32 s45, s45, 2\n"
      "s_cmp_eq_u32 s45, 0\n"
      "s_cbranch_scc0 Lpp_22\n"
      "buffer_load_dwordx2 v[212:213], v216, s[32:35], 0 offen sc1\n"
      "Lpp_22:\n"
      "s_lshl_b32 s58, s12, 12\n"
      "s_add_u32 s58, s58, s69\n"
      "buffer_load_dwordx4 v[144:147], v247, s[32:35], s58 offen offset:0 sc1\n"
      "v_mfma_f32_16x16x32_bf16 v[20:23], v[84:87], v[116:119], v[20:23]\n"
      "buffer_load_dwordx4 v[148:151], v247, s[32:35], s58 offen offset:1024 sc1\n"
      "v_mfma_f32_16x16x32_bf16 v[24:27], v[88:91], v[116:119], v[24:27]\n"
      "buffer_load_dwordx4 v[152:155], v247, s[32:35], s58 offen offset:2048 sc1\n"
      "v_mfma_f32_16x16x32_bf16 v[28:31], v[92:95], v[116:119], v[28:31]\n"
      "buffer_load_dwordx4 v[156:159], v247, s[32:35], s58 offen offset:3072 sc1\n"
      "v_mfma_f32_16x16x32_bf16 v[32:35], v[80:83], v[120:123], v[32:35]\n"
      "s_waitcnt lgkmcnt(0)\n"
      "s_lshl_b32 s67, s47, 15\n"
      "s_mov_b32 m0, s68\n"
      "s_nop 0\n"
      "buffer_load_dwordx4 v232, s[8:11], s67 offen offset:0 lds\n"
      "v_mfma_f32_16x16x32_bf16 v[36:39], v[84:87], v[120:123], v[36:39]\n"
      "buffer_load_dwordx4 v233, s[8:11], s67 offen offset:1024 lds\n"
      "v_mfma_f32_16x16x32_bf16 v[40:43], v[88:91], v[120:123], v[40:43]\n"
      "buffer_load_dwordx4 v234, s[8:11], s67 offen offset:2048 lds\n"
      "v_mfma_f32_16x16x32_bf16 v[44:47], v[92:95], v[120:123], v[44:47]\n"
      "buffer_load_dwordx4 v235, s[8:11], s67 offen offset:3072 lds\n"
      "v_mfma_f32_16x16x32_bf16 v[48:51], v[80:83], v[124:127], v[48:51]\n"
      "v_mfma_f32_16x16x32_bf16 v[52:55], v[84:87], v[124:127], v[52:55]\n"
      "s_mov_b32 s12, s47\n"
      "v_mfma_f32_16x16x32_bf16 v[56:59], v[88:91], v[124:127], v[56:59]\n"
      "s_bfe_u32 s47, s16, 0x80008\n"
      "v_mfma_f32_16x16x32_bf16 v[60:63], v[92:95], v[124:127], v[60:63]\n"
      "s_cmp_eq_u32 s45, 0\n"
      "s_cbranch_scc0 Lret_23\n"
      "s_getpc_b64 s[22:23]\n"
      "s_branch LF2POLL\n"
      "Lret_23:\n"
      "s_waitcnt vmcnt(12)\n"
      "v_mfma_f32_16x16x32_bf16 v[0:3], v[64:67], v[128:131], v[0:3]\n"
      "s_waitcnt vmcnt(8)\n"
      "ds_read_b64_tr_b16 v[80:81], v236 offset:4096\n"
      "ds_read_b64_tr_b16 v[82:83], v236 offset:6144\n"
      "v_mfma_f32_16x16x32_bf16 v[4:7], v[68:71], v[128:131], v[4:7]\n"
      "ds_read_b64_tr_b16 v[84:85], v237 offset:4096\n"
      "ds_read_b64_tr_b16 v[86:87], v237 offset:6144\n"
      "v_mfma_f32_16x16x32_bf16 v[8:11], v[72:75], v[128:131], v[8:11]\n"
      "ds_read_b64_tr_b16 v[88:89], v238 offset:4096\n"
      "ds_read_b64_tr_b16 v[90:91], v238 offset:6144\n"
      "v_mfma_f32_16x16x32_bf16 v[12:15], v[76:79], v[128:131], v[12:15]\n"
      "ds_read_b64_tr_b16 v[92:93], v239 offset:4096\n"
      "ds_read_b64_tr_b16 v[94:95], v239 offset:6144\n"
      "v_mfma_f32_16x16x32_bf16 v[16:19], v[64:67], v[132:135], v[16:19]\n"
      "s_lshl_b32 s58, s12, 12\n"
      "s_add_u32 s58, s58, s69\n"
      "buffer_load_dwordx4 v[96:99], v247, s[32:35], s58 offen offset:0 sc1\n"
      "v_mfma_f32_16x16x32_bf16 v[20:23], v[68:71], v[132:135], v[20:23]\n"
      "buffer_load_dwordx4 v[100:103], v247, s[32:35], s58 offen offset:1024 sc1\n"
      "v_mfma_f32_16x16x32_bf16 v[24:27], v[72:75], v[132:135], v[24:27]\n"
      "buffer_load_dwordx4 v[104:107], v247, s[32:35], s58 offen offset:2048 sc1\n"
      "v_mfma_f32_16x16x32_bf16 v[28:31], v[76:79], v[132:135], v[28:31]\n"
      "buffer_load_dwordx4 v[108:111], v247, s[32:35], s58 offen offset:3072 sc1\n"
      "v_mfma_f32_16x16x32_bf16 v[32:35], v[64:67], v[136:139], v[32:35]\n"
      "s_waitcnt lgkmcnt(0)\n"
      "s_lshl_b32 s67, s47, 15\n"
      "s_add_u32 m0, s68, 4096\n"
      "s_nop 0\n"
      "buffer_load_dwordx4 v232, s[8:11], s67 offen offset:0 lds\n"
      "v_mfma_f32_16x16x32_bf16 v[36:39], v[68:71], v[136:139], v[36:39]\n"
      "buffer_load_dwordx4 v233, s[8:11], s67 offen offset:1024 lds\n"
      "v_mfma_f32_16x16x32_bf16 v[40:43], v[72:75], v[136:139], v[40:43]\n"
      "buffer_load_dwordx4 v234, s[8:11], s67 offen offset:2048 lds\n"
      "v_mfma_f32_16x16x32_bf16 v[44:47], v[76:79], v[136:139], v[44:47]\n"
      "buffer_load_dwordx4 v235, s[8:11], s67 offen offset:3072 lds\n"
      "v_mfma_f32_16x16x32_bf16 v[48:51], v[64:67], v[140:143], v[48:51]\n"
      "v_mfma_f32_16x16x32_bf16 v[52:55], v[68:71], v[140:143], v[52:55]\n"
      "s_mov_b32 s12, s47\n"
      "v_mfma_f32_16x16x32_bf16 v[56:59], v[72:75], v[140:143], v[56:59]\n"
      "s_bfe_u32 s47, s16, 0x80010\n"
      "v_mfma_f32_16x16x32_bf16 v[60:63], v[76:79], v[140:143], v[60:63]\n"
      "s_cmp_ge_u32 s76, 4\n"
      "s_cbranch_scc1 Lhk_24\n"
      "s_setprio 2\n"
      "s_getpc_b64 s[72:73]\n"
      "Lret_25p:\n"
      "s_add_u32 s72, s72, Lret_25-Lret_25p\n"
      "s_addc_u32 s73, s73, 0\n"
      "s_branch LTRY\n"
      "Lret_25:\n"
      "s_nop 1\n"
      "s_setprio 0\n"
      "Lhk_24:\n"
      "s_waitcnt vmcnt(12)\n"
      "v_mfma_f32_16x16x32_bf16 v[0:3], v[80:83], v[144:147], v[0:3]\n"
      "s_waitcnt vmcnt(8)\n"
      "ds_read_b64_tr_b16 v[64:65], v236 offset:0\n"
      "ds_read_b64_tr_b16 v[66:67], v236 offset:2048\n"
      "v_mfma_f32_16x16x32_bf16 v[4:7], v[84:87], v[144:147], v[4:7]\n"
      "ds_read_b64_tr_b16 v[68:69], v237 offset:0\n"
      "ds_read_b64_tr_b16 v[70:71], v237 offset:2048\n"
      "v_mfma_f32_16x16x32_bf16 v[8:11], v[88:91], v[144:147], v[8:11]\n"
      "ds_read_b64_tr_b16 v[72:73], v238 offset:0\n"
      "ds_read_b64_tr_b16 v[74:75], v238 offset:2048\n"
      "v_mfma_f32_16x16x32_bf16 v[12:15], v[92:95], v[144:147], v[12:15]\n"
      "ds_read_b64_tr_b16 v[76:77], v239 offset:0\n"
      "ds_read_b64_tr_b16 v[78:79], v239 offset:2048\n"
      "v_mfma_f32_16x16x32_bf16 v[16:19], v[80:83], v[148:151], v[16:19]\n"
      "s_sub_u32 s45, s45, 2\n"
      "s_cmp_eq_u32 s45, 0\n"
      "s_cbranch_scc0 Lpp_26\n"
      "buffer_load_dwordx2 v[212:213], v216, s[32:35], 0 offen sc1\n"
      "Lpp_26:\n"
      "s_lshl_b32 s58, s12, 12\n"
      "s_add_u32 s58, s58, s69\n"
      "buffer_load_dwordx4 v[112:115], v247, s[32:35], s58 offen offset:0 sc1\n"
      "v_mfma_f32_16x16x32_bf16 v[20:23], v[84:87], v[148:151], v[20:23]\n"
      "buffer_load_dwordx4 v[116:119], v247, s[32:35], s58 offen offset:1024 sc1\n"
      "v_mfma_f32_16x16x32_bf16 v[24:27], v[88:91], v[148:151], v[24:27]\n"
      "buffer_load_dwordx4 v[120:123], v247, s[32:35], s58 offen offset:2048 sc1\n"
      "v_mfma_f32_16x16x32_bf16 v[28:31], v[92:95], v[148:151], v[28:31]\n"
      "buffer_load_dwordx4 v[124:127], v247, s[32:35], s58 offen offset:3072 sc1\n"
      "v_mfma_f32_16x16x32_bf16 v[32:35], v[80:83], v[152:155], v[32:35]\n"
      "s_waitcnt lgkmcnt(0)\n"
      "s_lshl_b32 s67, s47, 15\n"
      "s_mov_b32 m0, s68\n"
      "s_nop 0\n"
      "buffer_load_dwordx4 v232, s[8:11], s67 offen offset:0 lds\n"
      "v_mfma_f32_16x16x32_bf16 v[36:39], v[84:87], v[152:155], v[36:39]\n"
      "buffer_load_dwordx4 v233, s[8:11], s67 offen offset:1024 lds\n"
      "v_mfma_f32_16x16x32_bf16 v[40:43], v[88:91], v[152:155], v[40:43]\n"
      "buffer_load_dwordx4 v234, s[8:11], s67 offen offset:2048 lds\n"
      "v_mfma_f32_16x16x32_bf16 v[44:47], v[92:95], v[152:155], v[44:47]\n"
      "buffer_load_dwordx4 v235, s[8:11], s67 offen offset:3072 lds\n"
      "v_mfma_f32_16x16x32_bf16 v[48:51], v[80:83], v[156:159], v[48:51]\n"
      "v_mfma_f32_16x16x32_bf16 v[52:55], v[84:87], v[156:159], v[52:55]\n"
      "s_mov_b32 s12, s47\n"
      "s_bfe_u32 s47, s16, 0x80018\n"
      "v_mfma_f32_16x16x32_bf16 v[56:59], v[88:91], v[156:159], v[56:59]\n"
      "s_mov_b32 s16, s17\n"
      "s_mov_b32 s17, s18\n"
      "v_mfma_f32_16x16x32_bf16 v[60:63], v[92:95], v[156:159], v[60:63]\n"
      "s_mov_b32 s18, s19\n"
      "s_mov_b32 s19, s20\n"
      "s_sub_u32 s44, s44, 1\n"
      "s_cmp_lg_u32 s44, 0\n"
      "s_cbranch_scc1 LF2M\n"
      "LF2L:\n"
      "s_cmp_eq_u32 s45, 0\n"
      "s_cbranch_scc0 Lret_27\n"
      "s_getpc_b64 s[22:23]\n"
      "s_branch LF2POLL\n"
      "Lret_27:\n"
      "s_waitcnt vmcnt(12)\n"
      "v_mfma_f32_16x16x32_bf16 v[0:3], v[64:67], v[96:99], v[0:3]\n"
      "s_waitcnt vmcnt(8)\n"
      "ds_read_b64_tr_b16 v[80:81], v236 offset:4096\n"
      "ds_read_b64_tr_b16 v[82:83], v236 offset:6144\n"
      "v_mfma_f32_16x16x32_bf16 v[4:7], v[68:71], v[96:99], v[4:7]\n"
      "ds_read_b64_tr_b16 v[84:85], v237 offset:4096\n"
      "ds_read_b64_tr_b16 v[86:87], v237 offset:6144\n"
      "v_mfma_f32_16x16x32_bf16 v[8:11], v[72:75], v[96:99], v[8:11]\n"
      "ds_read_b64_tr_b16 v[88:89], v238 offset:4096\n"
      "ds_read_b64_tr_b16 v[90:91], v238 offset:6144\n"
      "v_mfma_f32_16x16x32_bf16 v[12:15], v[76:79], v[96:99], v[12:15]\n"
      "ds_read_b64_tr_b16 v[92:93], v239 offset:4096\n"
      "ds_read_b64_tr_b16 v[94:95], v239 offset:6144\n"
      "v_mfma_f32_16x16x32_bf16 v[16:19], v[64:67], v[100:103], v[16:19]\n"
      "s_lshl_b32 s58, s12, 12\n"
      "s_add_u32 s58, s58, s69\n"
      "buffer_load_dwordx4 v[128:131], v247, s[32:35], s58 offen offset:0 sc1\n"
      "v_mfma_f32_16x16x32_bf16 v[20:23], v[68:71], v[100:103], v[20:23]\n"
      "buffer_load_dwordx4 v[132:135], v247, s[32:35], s58 offen offset:1024 sc1\n"
      "v_mfma_f32_16x16x32_bf16 v[24:27], v[72:75], v[100:103], v[24:27]\n"
      "buffer_load_dwordx4 v[136:139], v247, s[32:35], s58 offen offset:2048 sc1\n"
      "v_mfma_f32_16x16x32_bf16 v[28:31], v[76:79], v[100:103], v[28:31]\n"
      "buffer_load_dwordx4 v[140:143], v247, s[32:35], s58 offen offset:3072 sc1\n"
      "v_mfma_f32_16x16x32_bf16 v[32:35], v[64:67], v[104:107], v[32:35]\n"
      "s_waitcnt lgkmcnt(0)\n"
      "s_lshl_b32 s67, s47, 15\n"
      "s_add_u32 m0, s68, 4096\n"
      "s_nop 0\n"
      "buffer_load_dwordx4 v232, s[8:11], s67 offen offset:0 lds\n"
      "v_mfma_f32_16x16x32_bf16 v[36:39], v[68:71], v[104:107], v[36:39]\n"
      "buffer_load_dwordx4 v233, s[8:11], s67 offen offset:1024 lds\n"
      "v_mfma_f32_16x16x32_bf16 v[40:43], v[72:75], v[104:107], v[40:43]\n"
      "buffer_load_dwordx4 v234, s[8:11], s67 offen offset:2048 lds\n"
      "v_mfma_f32_16x16x32_bf16 v[44:47], v[76:79], v[104:107], v[44:47]\n"
      "buffer_load_dwordx4 v235, s[8:11], s67 offen offset:3072 lds\n"
      "v_mfma_f32_16x16x32_bf16 v[48:51], v[64:67], v[108:111], v[48:51]\n"
      "v_mfma_f32_16x16x32_bf16 v[52:55], v[68:71], v[108:111], v[52:55]\n"
      "s_mov_b32 s12, s47\n"
      "v_mfma_f32_16x16x32_bf16 v[56:59], v[72:75], v[108:111], v[56:59]\n"
      "s_bfe_u32 s47, s16, 0x80000\n"
      "v_mfma_f32_16x16x32_bf16 v[60:63], v[76:79], v[108:111], v[60:63]\n"
      "s_cmp_ge_u32 s76, 5\n"
      "s_cbranch_scc1 Lhk_28\n"
      "s_setprio 2\n"
      "s_getpc_b64 s[72:73]\n"
      "Lret_29p:\n"
      "s_add_u32 s72, s72, Lret_29-Lret_29p\n"
      "s_addc_u32 s73, s73, 0\n"
      "s_branch LTRY\n"
      "Lret_29:\n"
      "s_nop 1\n"
      "s_setprio 0\n"
      "Lhk_28:\n"
      "s_waitcnt vmcnt(12)\n"
      "v_mfma_f32_16x16x32_bf16 v[0:3], v[80:83], v[112:115], v[0:3]\n"
      "s_waitcnt vmcnt(8)\n"
      "ds_read_b64_tr_b16 v[64:65], v236 offset:0\n"
      "ds_read_b64_tr_b16 v[66:67], v236 offset:2048\n"
      "v_mfma_f32_16x16x32_bf16 v[4:7], v[84:87], v[112:115], v[4:7]\n"
      "ds_read_b64_tr_b16 v[68:69], v237 offset:0\n"
      "ds_read_b64_tr_b16 v[70:71], v237 offset:2048\n"
      "v_mfma_f32_16x16x32_bf16 v[8:11], v[88:91], v[112:115], v[8:11]\n"
      "ds_read_b64_tr_b16 v[72:73], v238 offset:0\n"
      "ds_read_b64_tr_b16 v[74:75], v238 offset:2048\n"
      "v_mfma_f32_16x16x32_bf16 v[12:15], v[92:95], v[112:115], v[12:15]\n"
      "ds_read_b64_tr_b16 v[76:77], v239 offset:0\n"
      "ds_read_b64_tr_b16 v[78:79], v239 offset:2048\n"
      "v_mfma_f32_16x16x32_bf16 v[16:19], v[80:83], v[116:119], v[16:19]\n"
      "s_sub_u32 s45, s45, 2\n"
      "s_cmp_eq_u32 s45, 0\n"
      "s_cbranch_scc0 Lpp_30\n"
      "buffer_load_dwordx2 v[212:213], v216, s[32:35], 0 offen sc1\n"
      "Lpp_30:\n"
      "s_lshl_b32 s58, s12, 12\n"
      "s_add_u32 s58, s58, s69\n"
      "buffer_load_dwordx4 v[144:147], v247, s[32:35], s58 offen offset:0 sc1\n"
      "v_mfma_f32_16x16x32_bf16 v[20:23], v[84:87], v[116:119], v[20:23]\n"
      "buffer_load_dwordx4 v[148:151], v247, s[32:35], s58 offen offset:1024 sc1\n"
      "v_mfma_f32_16x16x32_bf16 v[24:27], v[88:91], v[116:119], v[24:27]\n"
      "buffer_load_dwordx4 v[152:155], v247, s[32:35], s58 offen offset:2048 sc1\n"
      "v_mfma_f32_16x16x32_bf16 v[28:31], v[92:95], v[116:119], v[28:31]\n"
      "buffer_load_dwordx4 v[156:159], v247, s[32:35], s58 offen offset:3072 sc1\n"
      "v_mfma_f32_16x16x32_bf16 v[32:35], v[80:83], v[120:123], v[32:35]\n"
      "s_waitcnt lgkmcnt(0)\n"
      "s_lshl_b32 s67, s47, 15\n"
      "s_mov_b32 m0, s68\n"
      "s_nop 0\n"
      "buffer_load_dwordx4 v232, s[8:11], s67 offen offset:0 lds\n"
      "v_mfma_f32_16x16x32_bf16 v[36:39], v[84:87], v[120:123], v[36:39]\n"
      "buffer_load_dwordx4 v233, s[8:11], s67 offen offset:1024 lds\n"
      "v_mfma_f32_16x16x32_bf16 v[40:43], v[88:91], v[120:123], v[40:43]\n"
      "buffer_load_dwordx4 v234, s[8:11], s67 offen offset:2048 lds\n"
      "v_mfma_f32_16x16x32_bf16 v[44:47], v[92:95], v[120:123], v[44:47]\n"
      "buffer_load_dwordx4 v235, s[8:11], s67 offen offset:3072 lds\n"
      "v_mfma_f32_16x16x32_bf16 v[48:51], v[80:83], v[124:127], v[48:51]\n"
      "v_mfma_f32_16x16x32_bf16 v[52:55], v[84:87], v[124:127], v[52:55]\n"
      "s_mov_b32 s12, s47\n"
      "v_mfma_f32_16x16x32_bf16 v[56:59], v[88:91], v[124:127], v[56:59]\n"
      "s_bfe_u32 s47, s16, 0x80008\n"
      "v_mfma_f32_16x16x32_bf16 v[60:63], v[92:95], v[124:127], v[60:63]\n"
      "s_cmp_eq_u32 s45, 0\n"
      "s_cbranch_scc0 Lret_31\n"
      "s_getpc_b64 s[22:23]\n"
      "s_branch LF2POLL\n"
      "Lret_31:\n"
      "s_waitcnt vmcnt(12)\n"
      "v_mfma_f32_16x16x32_bf16 v[0:3], v[64:67], v[128:131], v[0:3]\n"
      "s_waitcnt vmcnt(8)\n"
      "ds_read_b64_tr_b16 v[80:81], v236 offset:4096\n"
      "ds_read_b64_tr_b16 v[82:83], v236 offset:6144\n"
      "v_mfma_f32_16x16x32_bf16 v[4:7], v[68:71], v[128:131], v[4:7]\n"
      "ds_read_b64_tr_b16 v[84:85], v237 offset:4096\n"
      "ds_read_b64_tr_b16 v[86:87], v237 offset:6144\n"
      "v_mfma_f32_16x16x32_bf16 v[8:11], v[72:75], v[128:131], v[8:11]\n"
      "ds_read_b64_tr_b16 v[88:89], v238 offset:4096\n"
      "ds_read_b64_tr_b16 v[90:91], v238 offset:6144\n"
      "v_mfma_f32_16x16x32_bf16 v[12:15], v[76:79], v[128:131], v[12:15]\n"
      "ds_read_b64_tr_b16 v[92:93], v239 offset:4096\n"
      "ds_read_b64_tr_b16 v[94:95], v239 offset:6144\n"
      "v_mfma_f32_16x16x32_bf16 v[16:19], v[64:67], v[132:135], v[16:19]\n"
      "s_lshl_b32 s58, s12, 12\n"
      "s_add_u32 s58, s58, s69\n"
      "buffer_load_dwordx4 v[96:99], v247, s[32:35], s58 offen offset:0 sc1\n"
      "v_mfma_f32_16x16x32_bf16 v[20:23], v[68:71], v[132:135], v[20:23]\n"
      "buffer_load_dwordx4 v[100:103], v247, s[32:35], s58 offen offset:1024 sc1\n"
      "v_mfma_f32_16x16x32_bf16 v[24:27], v[72:75], v[132:135], v[24:27]\n"
      "buffer_load_dwordx4 v[104:107], v247, s[32:35], s58 offen offset:2048 sc1\n"
      "v_mfma_f32_16x16x32_bf16 v[28:31], v[76:79], v[132:135], v[28:31]\n"
      "buffer_load_dwordx4 v[108:111], v247, s[32:35], s58 offen offset:3072 sc1\n"
      "v_mfma_f32_16x16x32_bf16 v[32:35], v[64:67], v[136:139], v[32:35]\n"
      "s_waitcnt lgkmcnt(0)\n"
      "s_lshl_b32 s67, s47, 15\n"
      "s_add_u32 m0, s68, 4096\n"
      "s_nop 0\n"
      "buffer_load_dwordx4 v232, s[8:11], s67 offen offset:0 lds\n"
      "v_mfma_f32_16x16x32_bf16 v[36:39], v[68:71], v[136:139], v[36:39]\n"
      "buffer_load_dwordx4 v233, s[8:11], s67 offen offset:1024 lds\n"
      "v_mfma_f32_16x16x32_bf16 v[40:43], v[72:75], v[136:139], v[40:43]\n"
      "buffer_load_dwordx4 v234, s[8:11], s67 offen offset:2048 lds\n"
      "v_mfma_f32_16x16x32_bf16 v[44:47], v[76:79], v[136:139], v[44:47]\n"
      "buffer_load_dwordx4 v235, s[8:11], s67 offen offset:3072 lds\n"
      "v_mfma_f32_16x16x32_bf16 v[48:51], v[64:67], v[140:143], v[48:51]\n"
      "v_mfma_f32_16x16x32_bf16 v[52:55], v[68:71], v[140:143], v[52:55]\n"
      "s_mov_b32 s12, s47\n"
      "v_mfma_f32_16x16x32_bf16 v[56:59], v[72:75], v[140:143], v[56:59]\n"
      "s_lshl_b32 s47, s56, 2\n"
      "v_mfma_f32_16x16x32_bf16 v[60:63], v[76:79], v[140:143], v[60:63]\n"
      "s_add_u32 s47, s47, 0\n"
      "s_cmp_ge_u32 s76, 5\n"
      "s_cbranch_scc1 Lhk_32\n"
      "s_setprio 2\n"
      "s_getpc_b64 s[72:73]\n"
      "Lret_33p:\n"
      "s_add_u32 s72, s72, Lret_33-Lret_33p\n"
      "s_addc_u32 s73, s73, 0\n"
      "s_branch LTRY\n"
      "Lret_33:\n"
      "s_nop 1\n"
      "s_setprio 0\n"
      "Lhk_32:\n"
      "s_waitcnt vmcnt(12)\n"
      "v_mfma_f32_16x16x32_bf16 v[0:3], v[80:83], v[144:147], v[0:3]\n"
      "s_waitcnt vmcnt(8)\n"
      "ds_read_b64_tr_b16 v[64:65], v236 offset:0\n"
      "ds_read_b64_tr_b16 v[66:67], v236 offset:2048\n"
      "v_mfma_f32_16x16x32_bf16 v[4:7], v[84:87], v[144:147], v[4:7]\n"
      "ds_read_b64_tr_b16 v[68:69], v237 offset:0\n"
      "ds_read_b64_tr_b16 v[70:71], v237 offset:2048\n"
      "v_mfma_f32_16x16x32_bf16 v[8:11], v[88:91], v[144:147], v[8:11]\n"
      "ds_read_b64_tr_b16 v[72:73], v238 offset:0\n"
      "ds_read_b64_tr_b16 v[74:75], v238 offset:2048\n"
      "v_mfma_f32_16x16x32_bf16 v[12:15], v[92:95], v[144:147], v[12:15]\n"
      "ds_read_b64_tr_b16 v[76:77], v239 offset:0\n"
      "ds_read_b64_tr_b16 v[78:79], v239 offset:2048\n"
      "v_mfma_f32_16x16x32_bf16 v[16:19], v[80:83], v[148:151], v[16:19]\n"
      "s_lshl_b32 s58, s12, 12\n"
      "s_add_u32 s58, s58, s69\n"
      "buffer_load_dwordx4 v[112:115], v247, s[32:35], s58 offen offset:0 sc1\n"
      "v_mfma_f32_16x16x32_bf16 v[20:23], v[84:87], v[148:151], v[20:23]\n"
      "buffer_load_dwordx4 v[116:119], v247, s[32:35], s58 offen offset:1024 sc1\n"
      "v_mfma_f32_16x16x32_bf16 v[24:27], v[88:91], v[148:151], v[24:27]\n"
      "buffer_load_dwordx4 v[120:123], v247, s[32:35], s58 offen offset:2048 sc1\n"
      "v_mfma_f32_16x16x32_bf16 v[28:31], v[92:95], v[148:151], v[28:31]\n"
      "buffer_load_dwordx4 v[124:127], v247, s[32:35], s58 offen offset:3072 sc1\n"
      "v_mfma_f32_16x16x32_bf16 v[32:35], v[80:83], v[152:155], v[32:35]\n"
      "s_waitcnt lgkmcnt(0)\n"
      "s_lshl_b32 s67, s47, 15\n"
      "s_mov_b32 m0, s68\n"
      "s_nop 0\n"
      "buffer_load_dwordx4 v232, s[24:27], s67 offen offset:0 lds\n"
      "v_mfma_f32_16x16x32_bf16 v[36:39], v[84:87], v[152:155], v[36:39]\n"
      "buffer_load_dwordx4 v233, s[24:27], s67 offen offset:1024 lds\n"
      "v_mfma_f32_16x16x32_bf16 v[40:43], v[88:91], v[152:155], v[40:43]\n"
      "buffer_load_dwordx4 v234, s[24:27], s67 offen offset:2048 lds\n"
      "v_mfma_f32_16x16x32_bf16 v[44:47], v[92:95], v[152:155], v[44:47]\n"
      "buffer_load_dwordx4 v235, s[24:27], s67 offen offset:3072 lds\n"
      "v_mfma_f32_16x16x32_bf16 v[48:51], v[80:83], v[156:159], v[48:51]\n"
      "v_mfma_f32_16x16x32_bf16 v[52:55], v[84:87], v[156:159], v[52:55]\n"
      "s_mov_b32 s12, s47\n"
      "s_lshl_b32 s47, s56, 2\n"
      "v_mfma_f32_16x16x32_bf16 v[56:59], v[88:91], v[156:159], v[56:59]\n"
      "s_add_u32 s47, s47, 2\n"
      "s_add_u32 s69, s69, 229376\n"
      "v_mfma_f32_16x16x32_bf16 v[60:63], v[92:95], v[156:159], v[60:63]\n"
      "s_waitcnt vmcnt(12)\n"
      "v_mfma_f32_16x16x32_bf16 v[0:3], v[64:67], v[96:99], v[0:3]\n"
      "s_waitcnt vmcnt(8)\n"
      "ds_read_b64_tr_b16 v[80:81], v236 offset:4096\n"
      "ds_read_b64_tr_b16 v[82:83], v236 offset:6144\n"
      "v_mfma_f32_16x16x32_bf16 v[4:7], v[68:71], v[96:99], v[4:7]\n"
      "ds_read_b64_tr_b16 v[84:85], v237 offset:4096\n"
      "ds_read_b64_tr_b16 v[86:87], v237 offset:6144\n"
      "v_mfma_f32_16x16x32_bf16 v[8:11], v[72:75], v[96:99], v[8:11]\n"
      "ds_read_b64_tr_b16 v[88:89], v238 offset:4096\n"
      "ds_read_b64_tr_b16 v[90:91], v238 offset:6144\n"
      "v_mfma_f32_16x16x32_bf16 v[12:15], v[76:79], v[96:99], v[12:15]\n"
      "ds_read_b64_tr_b16 v[92:93], v239 offset:4096\n"
      "ds_read_b64_tr_b16 v[94:95], v239 offset:6144\n"
      "v_mfma_f32_16x16x32_bf16 v[16:19], v[64:67], v[100:103], v[16:19]\n"
      "s_lshl_b32 s58, s12, 12\n"
      "s_add_u32 s58, s58, s69\n"
      "buffer_load_dwordx4 v[128:131], v247, s[32:35], s58 offen offset:0 sc1\n"
      "v_mfma_f32_16x16x32_bf16 v[20:23], v[68:71], v[100:103], v[20:23]\n"
      "buffer_load_dwordx4 v[132:135], v247, s[32:35], s58 offen offset:1024 sc1\n"
      "v_mfma_f32_16x16x32_bf16 v[24:27], v[72:75], v[100:103], v[24:27]\n"
      "buffer_load_dwordx4 v[136:139], v247, s[32:35], s58 offen offset:2048 sc1\n"
      "v_mfma_f32_16x16x32_bf16 v[28:31], v[76:79], v[100:103], v[28:31]\n"
      "buffer_load_dwordx4 v[140:143], v247, s[32:35], s58 offen offset:3072 sc1\n"
      "v_mfma_f32_16x16x32_bf16 v[32:35], v[64:67], v[104:107], v[32:35]\n"
      "s_waitcnt lgkmcnt(0)\n"
      "s_lshl_b32 s67, s47, 15\n"
      "s_add_u32 m0, s68, 4096\n"
      "s_nop 0\n"
      "buffer_load_dwordx4 v232, s[24:27], s67 offen offset:0 lds\n"
      "v_mfma_f32_16x16x32_bf16 v[36:39], v[68:71], v[104:107], v[36:39]\n"
      "buffer_load_dwordx4 v233, s[24:27], s67 offen offset:1024 lds\n"
      "v_mfma_f32_16x16x32_bf16 v[40:43], v[72:75], v[104:107], v[40:43]\n"
      "buffer_load_dwordx4 v234, s[24:27], s67 offen offset:2048 lds\n"
      "v_mfma_f32_16x16x32_bf16 v[44:47], v[76:79], v[104:107], v[44:47]\n"
      "buffer_load_dwordx4 v235, s[24:27], s67 offen offset:3072 lds\n"
      "v_mfma_f32_16x16x32_bf16 v[48:51], v[64:67], v[108:111], v[48:51]\n"
      "v_mfma_f32_16x16x32_bf16 v[52:55], v[68:71], v[108:111], v[52:55]\n"
      "s_mov_b32 s12, s47\n"
      "s_lshl_b32 s47, s56, 2\n"
      "v_mfma_f32_16x16x32_bf16 v[56:59], v[72:75], v[108:111], v[56:59]\n"
      "s_add_u32 s47, s47, 1\n"
      "s_lshl_b32 s44, s47, 12\n"
      "v_mfma_f32_16x16x32_bf16 v[60:63], v[76:79], v[108:111], v[60:63]\n"
      "s_add_u32 s44, s44, s69\n"
      "s_cmp_ge_u32 s76, 6\n"
      "s_cbranch_scc1 Lhk_34\n"
      "s_setprio 2\n"
      "s_getpc_b64 s[72:73]\n"
      "Lret_35p:\n"
      "s_add_u32 s72, s72, Lret_35-Lret_35p\n"
      "s_addc_u32 s73, s73, 0\n"
      "s_branch LTRY\n"
      "Lret_35:\n"
      "s_nop 1\n"
      "s_setprio 0\n"
      "Lhk_34:\n"
      "s_waitcnt vmcnt(12)\n"
      "v_mfma_f32_16x16x32_bf16 v[0:3], v[80:83], v[112:115], v[0:3]\n"
      "s_waitcnt vmcnt(8)\n"
      "ds_read_b64_tr_b16 v[64:65], v236 offset:0\n"
      "ds_read_b64_tr_b16 v[66:67], v236 offset:2048\n"
      "v_mfma_f32_16x16x32_bf16 v[4:7], v[84:87], v[112:115], v[4:7]\n"
      "ds_read_b64_tr_b16 v[68:69], v237 offset:0\n"
      "ds_read_b64_tr_b16 v[70:71], v237 offset:2048\n"
      "v_mfma_f32_16x16x32_bf16 v[8:11], v[88:91], v[112:115], v[8:11]\n"
      "ds_read_b64_tr_b16 v[72:73], v238 offset:0\n"
      "ds_read_b64_tr_b16 v[74:75], v238 offset:2048\n"
      "v_mfma_f32_16x16x32_bf16 v[12:15], v[92:95], v[112:115], v[12:15]\n"
      "ds_read_b64_tr_b16 v[76:77], v239 offset:0\n"
      "ds_read_b64_tr_b16 v[78:79], v239 offset:2048\n"
      "v_mfma_f32_16x16x32_bf16 v[16:19], v[80:83], v[116:119], v[16:19]\n"
      "s_lshl_b32 s58, s12, 12\n"
      "s_add_u32 s58, s58, s69\n"
      "buffer_load_dwordx4 v[144:147], v247, s[32:35], s58 offen offset:0 sc1\n"
      "v_mfma_f32_16x16x32_bf16 v[20:23], v[84:87], v[116:119], v[20:23]\n"
      "buffer_load_dwordx4 v[148:151], v247, s[32:35], s58 offen offset:1024 sc1\n"
      "v_mfma_f32_16x16x32_bf16 v[24:27], v[88:91], v[116:119], v[24:27]\n"
      "buffer_load_dwordx4 v[152:155], v247, s[32:35], s58 offen offset:2048 sc1\n"
      "v_mfma_f32_16x16x32_bf16 v[28:31], v[92:95], v[116:119], v[28:31]\n"
      "buffer_load_dwordx4 v[156:159], v247, s[32:35], s58 offen offset:3072 sc1\n"
      "v_mfma_f32_16x16x32_bf16 v[32:35], v[80:83], v[120:123], v[32:35]\n"
      "s_waitcnt lgkmcnt(0)\n"
      "s_lshl_b32 s67, s47, 15\n"
      "s_mov_b32 m0, s68\n"
      "s_nop 0\n"
      "buffer_load_dwordx4 v232, s[24:27], s67 offen offset:0 lds\n"
      "v_mfma_f32_16x16x32_bf16 v[36:39], v[84:87], v[120:123], v[36:39]\n"
      "buffer_load_dwordx4 v233, s[24:27], s67 offen offset:1024 lds\n"
      "v_mfma_f32_16x16x32_bf16 v[40:43], v[88:91], v[120:123], v[40:43]\n"
      "buffer_load_dwordx4 v234, s[24:27], s67 offen offset:2048 lds\n"
      "v_mfma_f32_16x16x32_bf16 v[44:47], v[92:95], v[120:123], v[44:47]\n"
      "buffer_load_dwordx4 v235, s[24:27], s67 offen offset:3072 lds\n"
      "v_mfma_f32_16x16x32_bf16 v[48:51], v[80:83], v[124:127], v[48:51]\n"
      "v_mfma_f32_16x16x32_bf16 v[52:55], v[84:87], v[124:127], v[52:55]\n"
      "s_mov_b32 s12, s47\n"
      "v_mfma_f32_16x16x32_bf16 v[56:59], v[88:91], v[124:127], v[56:59]\n"
      "s_lshl_b32 s47, s56, 2\n"
      "v_mfma_f32_16x16x32_bf16 v[60:63], v[92:95], v[124:127], v[60:63]\n"
      "s_add_u32 s47, s47, 3\n"
      "s_waitcnt vmcnt(12)\n"
      "s_setprio 2\n"
      "v_max3_u32 v206, v128, v129, v130\n"
      "v_max3_u32 v206, v206, v131, v132\n"
      "v_max3_u32 v206, v206, v133, v134\n"
      "v_max3_u32 v206, v206, v135, v136\n"
      "v_max3_u32 v206, v206, v137, v138\n"
      "v_max3_u32 v206, v206, v139, v140\n"
      "v_max3_u32 v206, v206, v141, v142\n"
      "v_max_u32 v206, v206, v143\n"
      "v_cmp_eq_u32 vcc, -1, v206\n"
      "s_cbranch_vccnz Lcs_36r\n"
      "Lcs_36:\n"
      "s_setprio 0\n"
      "v_mfma_f32_16x16x32_bf16 v[0:3], v[64:67], v[128:131], v[0:3]\n"
      "s_waitcnt vmcnt(8)\n"
      "ds_read_b64_tr_b16 v[80:81], v236 offset:4096\n"
      "ds_read_b64_tr_b16 v[82:83], v236 offset:6144\n"
      "v_mfma_f32_16x16x32_bf16 v[4:7], v[68:71], v[128:131], v[4:7]\n"
      "ds_read_b64_tr_b16 v[84:85], v237 offset:4096\n"
      "ds_read_b64_tr_b16 v[86:87], v237 offset:6144\n"
      "v_mfma_f32_16x16x32_bf16 v[8:11], v[72:75], v[128:131], v[8:11]\n"
      "ds_read_b64_tr_b16 v[88:89], v238 offset:4096\n"
      "ds_read_b64_tr_b16 v[90:91], v238 offset:6144\n"
      "v_mfma_f32_16x16x32_bf16 v[12:15], v[76:79], v[128:131], v[12:15]\n"
      "ds_read_b64_tr_b16 v[92:93], v239 offset:4096\n"
      "ds_read_b64_tr_b16 v[94:95], v239 offset:6144\n"
      "v_mfma_f32_16x16x32_bf16 v[16:19], v[64:67], v[132:135], v[16:19]\n"
      "s_mov_b32 s61, 0\n"
      "buffer_load_dwordx2 v[224:225], v240, s[28:31], s61 offen\n"
      "v_mfma_f32_16x16x32_bf16 v[20:23], v[68:71], v[132:135], v[20:23]\n"
      "s_mov_b32 s61, 16384\n"
      "buffer_load_dwordx2 v[226:227], v240, s[28:31], s61 offen\n"
      "v_mfma_f32_16x16x32_bf16 v[24:27], v[72:75], v[132:135], v[24:27]\n"
      "s_mov_b32 s61, 32768\n"
      "buffer_load_dwordx2 v[228:229], v240, s[28:31], s61 offen\n"
      "v_mfma_f32_16x16x32_bf16 v[28:31], v[76:79], v[132:135], v[28:31]\n"
      "s_mov_b32 s61, 49152\n"
      "buffer_load_dwordx2 v[230:231], v240, s[28:31], s61 offen\n"
      "v_mfma_f32_16x16x32_bf16 v[32:35], v[64:67], v[136:139], v[32:35]\n"
      "s_waitcnt lgkmcnt(0)\n"
      "s_lshl_b32 s67, s47, 15\n"
      "s_add_u32 m0, s68, 4096\n"
      "s_nop 0\n"
      "buffer_load_dwordx4 v232, s[24:27], s67 offen offset:0 lds\n"
      "v_mfma_f32_16x16x32_bf16 v[36:39], v[68:71], v[136:139], v[36:39]\n"
      "buffer_load_dwordx4 v233, s[24:27], s67 offen offset:1024 lds\n"
      "v_mfma_f32_16x16x32_bf16 v[40:43], v[72:75], v[136:139], v[40:43]\n"
      "buffer_load_dwordx4 v234, s[24:27], s67 offen offset:2048 lds\n"
      "v_mfma_f32_16x16x32_bf16 v[44:47], v[76:79], v[136:139], v[44:47]\n"
      "buffer_load_dwordx4 v235, s[24:27], s67 offen offset:3072 lds\n"
      "v_mfma_f32_16x16x32_bf16 v[48:51], v[64:67], v[140:143], v[48:51]\n"
      "v_mfma_f32_16x16x32_bf16 v[52:55], v[68:71], v[140:143], v[52:55]\n"
      "v_mfma_f32_16x16x32_bf16 v[56:59], v[72:75], v[140:143], v[56:59]\n"
      "v_mfma_f32_16x16x32_bf16 v[60:63], v[76:79], v[140:143], v[60:63]\n"
      "s_cmp_ge_u32 s76, 7\n"
      "s_cbranch_scc1 Lhk_37\n"
      "s_setprio 2\n"
      "s_getpc_b64 s[72:73]\n"
      "Lret_38p:\n"
      "s_add_u32 s72, s72, Lret_38-Lret_38p\n"
      "s_addc_u32 s73, s73, 0\n"
      "s_branch LTRY\n"
      "Lret_38:\n"
      "s_nop 1\n"
      "s_setprio 0\n"
      "Lhk_37:\n"
      "s_waitcnt vmcnt(12)\n"
      "s_setprio 2\n"
      "v_max3_u32 v206, v144, v145, v146\n"
      "v_max3_u32 v206, v206, v147, v148\n"
      "v_max3_u32 v206, v206, v149, v150\n"
      "v_max3_u32 v206, v206, v151, v152\n"
      "v_max3_u32 v206, v206, v153, v154\n"
      "v_max3_u32 v206, v206, v155, v156\n"
      "v_max3_u32 v206, v206, v157, v158\n"
      "v_max_u32 v206, v206, v159\n"
      "v_cmp_eq_u32 vcc, -1, v206\n"
      "s_cbranch_vccnz Lcs_39r\n"
      "Lcs_39:\n"
      "s_setprio 0\n"
      "v_mfma_f32_16x16x32_bf16 v[0:3], v[80:83], v[144:147], v[0:3]\n"
      "s_waitcnt vmcnt(8)\n"
      "ds_read_b64_tr_b16 v[64:65], v236 offset:0\n"
      "ds_read_b64_tr_b16 v[66:67], v236 offset:2048\n"
      "v_mfma_f32_16x16x32_bf16 v[4:7], v[84:87], v[144:147], v[4:7]\n"
      "ds_read_b64_tr_b16 v[68:69], v237 offset:0\n"
      "ds_read_b64_tr_b16 v[70:71], v237 offset:2048\n"
      "v_mfma_f32_16x16x32_bf16 v[8:11], v[88:91], v[144:147], v[8:11]\n"
      "ds_read_b64_tr_b16 v[72:73], v238 offset:0\n"
      "ds_read_b64_tr_b16 v[74:75], v238 offset:2048\n"
      "v_mfma_f32_16x16x32_bf16 v[12:15], v[92:95], v[144:147], v[12:15]\n"
      "ds_read_b64_tr_b16 v[76:77], v239 offset:0\n"
      "ds_read_b64_tr_b16 v[78:79], v239 offset:2048\n"
      "v_mfma_f32_16x16x32_bf16 v[16:19], v[80:83], v[148:151], v[16:19]\n"
      "s_add_u32 s60, s44, 0\n"
      "buffer_load_dwordx4 v[96:99], v247, s[32:35], s60 offen offset:0 sc1\n"
      "v_mfma_f32_16x16x32_bf16 v[20:23], v[84:87], v[148:151], v[20:23]\n"
      "s_add_u32 s60, s44, 8192\n"
      "buffer_load_dwordx4 v[100:103], v247, s[32:35], s60 offen offset:0 sc1\n"
      "v_mfma_f32_16x16x32_bf16 v[24:27], v[88:91], v[148:151], v[24:27]\n"
      "v_mfma_f32_16x16x32_bf16 v[28:31], v[92:95], v[148:151], v[28:31]\n"
      "v_mfma_f32_16x16x32_bf16 v[32:35], v[80:83], v[152:155], v[32:35]\n"
      "s_waitcnt lgkmcnt(0)\n"
      "v_mfma_f32_16x16x32_bf16 v[36:39], v[84:87], v[152:155], v[36:39]\n"
      "v_mfma_f32_16x16x32_bf16 v[40:43], v[88:91], v[152:155], v[40:43]\n"
      "v_mfma_f32_16x16x32_bf16 v[44:47], v[92:95], v[152:155], v[44:47]\n"
      "v_mfma_f32_16x16x32_bf16 v[48:51], v[80:83], v[156:159], v[48:51]\n"
      "v_mfma_f32_16x16x32_bf16 v[52:55], v[84:87], v[156:159], v[52:55]\n"
      "v_mfma_f32_16x16x32_bf16 v[56:59], v[88:91], v[156:159], v[56:59]\n"
      "v_mfma_f32_16x16x32_bf16 v[60:63], v[92:95], v[156:159], v[60:63]\n"
      "s_waitcnt vmcnt(2)\n"
      "ds_read_b64_tr_b16 v[80:81], v236 offset:4096\n"
      "ds_read_b64_tr_b16 v[82:83], v236 offset:6144\n"
      "ds_read_b64_tr_b16 v[84:85], v237 offset:4096\n"
      "ds_read_b64_tr_b16 v[86:87], v237 offset:6144\n"
      "ds_read_b64_tr_b16 v[88:89], v238 offset:4096\n"
      "ds_read_b64_tr_b16 v[90:91], v238 offset:6144\n"
      "ds_read_b64_tr_b16 v[92:93], v239 offset:4096\n"
      "ds_read_b64_tr_b16 v[94:95], v239 offset:6144\n"
      "s_setprio 2\n"
      "s_mov_b32 s96, 0x3f317218\n"
      "s_cmp_ge_u32 s76, 7\n"
      "s_cbranch_scc1 Lhk_40\n"
      "s_getpc_b64 s[72:73]\n"
      "Lret_41p:\n"
      "s_add_u32 s72, s72, Lret_41-Lret_41p\n"
      "s_addc_u32 s73, s73, 0\n"
      "s_branch LTRY\n"
      "Lret_41:\n"
      "s_nop 1\n"
      "Lhk_40:\n"
      "s_waitcnt lgkmcnt(0)\n"
      "s_mov_b32 s66, 0\n"
      "s_waitcnt vmcnt(0)\n"
      "s_branch Lcoc_43\n"
      "Lco_42:\n"
      "s_add_u32 s60, s44, 0\n"
      "buffer_load_dwordx4 v[96:99], v247, s[32:35], s60 offen offset:0 sc1\n"
      "s_add_u32 s60, s44, 8192\n"
      "buffer_load_dwordx4 v[100:103], v247, s[32:35], s60 offen offset:0 sc1\n"
      "s_waitcnt vmcnt(0)\n"
      "Lcoc_43:\n"
      "v_max3_u32 v206, v96, v97, v98\n"
      "v_max3_u32 v206, v206, v99, v100\n"
      "v_max3_u32 v206, v206, v101, v102\n"
      "v_max_u32 v206, v206, v103\n"
      "v_cmp_eq_u32 vcc, -1, v206\n"
      "s_cbranch_vccz Lcod_44\n"
      "s_cmp_ge_u32 s76, 7\n"
      "s_cbranch_scc1 Lhk_45\n"
      "s_getpc_b64 s[72:73]\n"
      "Lret_46p:\n"
      "s_add_u32 s72, s72, Lret_46-Lret_46p\n"
      "s_addc_u32 s73, s73, 0\n"
      "s_branch LTRY\n"
      "Lret_46:\n"
      "s_nop 1\n"
      "Lhk_45:\n"
      "s_sleep 8\n"
      "s_add_u32 s66, s66, 1\n"
      "s_cmp_lt_u32 s66, 131072\n"
      "s_cbranch_scc1 Lco_42\n"
      "Lcod_44:\n"
      "Ldr_47:\n"
      "s_cmp_gt_u32 s76, 5\n"
      "s_cbranch_scc1 Ldrd_48\n"
      "s_getpc_b64 s[72:73]\n"
      "Lret_49p:\n"
      "s_add_u32 s72, s72, Lret_49-Lret_49p\n"
      "s_addc_u32 s73, s73, 0\n"
      "s_branch LRED\n"
      "Lret_49:\n"
      "s_nop 1\n"
      "s_sleep 10\n"
      "s_branch Ldr_47\n"
      "Ldrd_48:\n"
      "v_mov_b32 v217, 162004\n"
      "s_mov_b32 s66, 0\n"
      "Lor_50:\n"
      "ds_read_b32 v216, v217\n"
      "s_waitcnt lgkmcnt(0)\n"
      "v_cmp_eq_u32 vcc, 0, v216\n"
      "s_cbranch_vccz Lord_51\n"
      "s_cmp_ge_u32 s76, 7\n"
      "s_cbranch_scc1 Lhk_53\n"
      "s_getpc_b64 s[72:73]\n"
      "Lret_54p:\n"
      "s_add_u32 s72, s72, Lret_54-Lret_54p\n"
      "s_addc_u32 s73, s73, 0\n"
      "s_branch LTRY\n"
      "Lret_54:\n"
      "s_nop 1\n"
      "Lhk_53:\n"
      "s_sleep 4\n"
      "s_add_u32 s66, s66, 1\n"
      "s_cmp_lt_u32 s66, 131072\n"
      "s_cbranch_scc1 Lor_50\n"
      "Lord_51:\n"
      "s_cmp_eq_u32 s66, 0\n"
      "s_cbranch_scc1 Lorg_52\n"
      "s_sleep 10\n"
      "Lorg_52:\n"
      "s_add_u32 s60, s44, 0\n"
      "buffer_load_dwordx4 v[104:107], v247, s[32:35], s60 offen offset:1024 sc1\n"
      "s_add_u32 s60, s44, 8192\n"
      "buffer_load_dwordx4 v[108:111], v247, s[32:35], s60 offen offset:1024 sc1\n"
      "s_cmp_ge_u32 s76, 7\n"
      "s_cbranch_scc1 Lhk_55\n"
      "s_getpc_b64 s[72:73]\n"
      "Lret_56p:\n"
      "s_add_u32 s72, s72, Lret_56-Lret_56p\n"
      "s_addc_u32 s73, s73, 0\n"
      "s_branch LTRY\n"
      "Lret_56:\n"
      "s_nop 1\n"
      "Lhk_55:\n"
      "s_setprio 0\n"
      "v_mfma_f32_16x16x32_bf16 v[0:3], v[64:67], v[96:99], v[0:3]\n"
      "v_mfma_f32_16x16x32_bf16 v[4:7], v[68:71], v[96:99], v[4:7]\n"
      "v_mfma_f32_16x16x32_bf16 v[8:11], v[72:75], v[96:99], v[8:11]\n"
      "v_mfma_f32_16x16x32_bf16 v[12:15], v[76:79], v[96:99], v[12:15]\n"
      "v_mfma_f32_16x16x32_bf16 v[0:3], v[80:83], v[100:103], v[0:3]\n"
      "v_mfma_f32_16x16x32_bf16 v[4:7], v[84:87], v[100:103], v[4:7]\n"
      "v_mfma_f32_16x16x32_bf16 v[8:11], v[88:91], v[100:103], v[8:11]\n"
      "v_mfma_f32_16x16x32_bf16 v[12:15], v[92:95], v[100:103], v[12:15]\n"
      "s_setprio 2\n"
      "s_nop 3\n"
      "ds_write_b128 v248, v[0:3] offset:0\n"
      "ds_write_b128 v249, v[4:7] offset:1024\n"
      "ds_write_b128 v250, v[8:11] offset:2048\n"
      "ds_write_b128 v251, v[12:15] offset:3072\n"
      "s_cmp_ge_u32 s76, 7\n"
      "s_cbranch_scc1 Lhk_57\n"
      "s_getpc_b64 s[72:73]\n"
      "Lret_58p:\n"
      "s_add_u32 s72, s72, Lret_58-Lret_58p\n"
      "s_addc_u32 s73, s73, 0\n"
      "s_branch LTRY\n"
      "Lret_58:\n"
      "s_nop 1\n"
      "Lhk_57:\n"
      "s_mov_b32 s66, 0\n"
      "s_waitcnt vmcnt(0)\n"
      "s_branch Lcoc_60\n"
      "Lco_59:\n"
      "s_add_u32 s60, s44, 0\n"
      "buffer_load_dwordx4 v[104:107], v247, s[32:35], s60 offen offset:1024 sc1\n"
      "s_add_u32 s60, s44, 8192\n"
      "buffer_load_dwordx4 v[108:111], v247, s[32:35], s60 offen offset:1024 sc1\n"
      "s_waitcnt vmcnt(0)\n"
      "Lcoc_60:\n"
      "v_max3_u32 v206, v104, v105, v106\n"
      "v_max3_u32 v206, v206, v107, v108\n"
      "v_max3_u32 v206, v206, v109, v110\n"
      "v_max_u32 v206, v206, v111\n"
      "v_cmp_eq_u32 vcc, -1, v206\n"
      "s_cbranch_vccz Lcod_61\n"
      "s_cmp_ge_u32 s76, 7\n"
      "s_cbranch_scc1 Lhk_62\n"
      "s_getpc_b64 s[72:73]\n"
      "Lret_63p:\n"
      "s_add_u32 s72, s72, Lret_63-Lret_63p\n"
      "s_addc_u32 s73, s73, 0\n"
      "s_branch LTRY\n"
      "Lret_63:\n"
      "s_nop 1\n"
      "Lhk_62:\n"
      "s_sleep 8\n"
      "s_add_u32 s66, s66, 1\n"
      "s_cmp_lt_u32 s66, 131072\n"
      "s_cbranch_scc1 Lco_59\n"
      "Lcod_61:\n"
      "Ldr_64:\n"
      "s_cmp_gt_u32 s76, 6\n"
      "s_cbranch_scc1 Ldrd_65\n"
      "s_getpc_b64 s[72:73]\n"
      "Lret_66p:\n"
      "s_add_u32 s72, s72, Lret_66-Lret_66p\n"
      "s_addc_u32 s73, s73, 0\n"
      "s_branch LRED\n"
      "Lret_66:\n"
      "s_nop 1\n"
      "s_sleep 10\n"
      "s_branch Ldr_64\n"
      "Ldrd_65:\n"
      "v_mov_b32 v217, 162008\n"
      "s_mov_b32 s66, 0\n"
      "Lor_67:\n"
      "ds_read_b32 v216, v217\n"
      "s_waitcnt lgkmcnt(0)\n"
      "v_cmp_eq_u32 vcc, 0, v216\n"
      "s_cbranch_vccz Lord_68\n"
      "s_sleep 4\n"
      "s_add_u32 s66, s66, 1\n"
      "s_cmp_lt_u32 s66, 131072\n"
      "s_cbranch_scc1 Lor_67\n"
      "Lord_68:\n"
      "s_cmp_eq_u32 s66, 0\n"
      "s_cbranch_scc1 Lorg_69\n"
      "s_sleep 10\n"
      "Lorg_69:\n"
      "s_add_u32 s60, s44, 0\n"
      "buffer_load_dwordx4 v[112:115], v247, s[32:35], s60 offen offset:2048 sc1\n"
      "s_add_u32 s60, s44, 8192\n"
      "buffer_load_dwordx4 v[116:119], v247, s[32:35], s60 offen offset:2048 sc1\n"
      "s_setprio 0\n"
      "v_mfma_f32_16x16x32_bf16 v[16:19], v[64:67], v[104:107], v[16:19]\n"
      "v_mfma_f32_16x16x32_bf16 v[20:23], v[68:71], v[104:107], v[20:23]\n"
      "v_mfma_f32_16x16x32_bf16 v[24:27], v[72:75], v[104:107], v[24:27]\n"
      "v_mfma_f32_16x16x32_bf16 v[28:31], v[76:79], v[104:107], v[28:31]\n"
      "v_mfma_f32_16x16x32_bf16 v[16:19], v[80:83], v[108:111], v[16:19]\n"
      "v_mfma_f32_16x16x32_bf16 v[20:23], v[84:87], v[108:111], v[20:23]\n"
      "v_mfma_f32_16x16x32_bf16 v[24:27], v[88:91], v[108:111], v[24:27]\n"
      "v_mfma_f32_16x16x32_bf16 v[28:31], v[92:95], v[108:111], v[28:31]\n"
      "s_setprio 2\n"
      "s_nop 3\n"
      "ds_write_b128 v248, v[16:19] offset:16384\n"
      "ds_write_b128 v249, v[20:23] offset:17408\n"
      "ds_write_b128 v250, v[24:27] offset:18432\n"
      "ds_write_b128 v251, v[28:31] offset:19456\n"
      "s_mov_b32 s66, 0\n"
      "s_waitcnt vmcnt(0)\n"
      "s_branch Lcoc_71\n"
      "Lco_70:\n"
      "s_add_u32 s60, s44, 0\n"
      "buffer_load_dwordx4 v[112:115], v247, s[32:35], s60 offen offset:2048 sc1\n"
      "s_add_u32 s60, s44, 8192\n"
      "buffer_load_dwordx4 v[116:119], v247, s[32:35], s60 offen offset:2048 sc1\n"
      "s_waitcnt vmcnt(0)\n"
      "Lcoc_71:\n"
      "v_max3_u32 v206, v112, v113, v114\n"
      "v_max3_u32 v206, v206, v115, v116\n"
      "v_max3_u32 v206, v206, v117, v118\n"
      "v_max_u32 v206, v206, v119\n"
      "v_cmp_eq_u32 vcc, -1, v206\n"
      "s_cbranch_vccz Lcod_72\n"
      "s_sleep 6\n"
      "s_add_u32 s66, s66, 1\n"
      "s_cmp_lt_u32 s66, 131072\n"
      "s_cbranch_scc1 Lco_70\n"
      "Lcod_72:\n"
      "s_setprio 0\n"
      "v_mfma_f32_16x16x32_bf16 v[32:35], v[64:67], v[112:115], v[32:35]\n"
      "v_mfma_f32_16x16x32_bf16 v[36:39], v[68:71], v[112:115], v[36:39]\n"
      "v_mfma_f32_16x16x32_bf16 v[40:43], v[72:75], v[112:115], v[40:43]\n"
      "v_mfma_f32_16x16x32_bf16 v[44:47], v[76:79], v[112:115], v[44:47]\n"
      "v_mfma_f32_16x16x32_bf16 v[32:35], v[80:83], v[116:119], v[32:35]\n"
      "v_mfma_f32_16x16x32_bf16 v[36:39], v[84:87], v[116:119], v[36:39]\n"
      "v_mfma_f32_16x16x32_bf16 v[40:43], v[88:91], v[116:119], v[40:43]\n"
      "v_mfma_f32_16x16x32_bf16 v[44:47], v[92:95], v[116:119], v[44:47]\n"
      "s_setprio 2\n"
      "s_nop 3\n"
      "ds_write_b128 v248, v[32:35] offset:32768\n"
      "ds_write_b128 v249, v[36:39] offset:33792\n"
      "ds_write_b128 v250, v[40:43] offset:34816\n"
      "ds_write_b128 v251, v[44:47] offset:35840\n"
      "s_waitcnt lgkmcnt(0)\n"
      "s_barrier\n"
      "ds_read_b128 v[0:3], v241 offset:0\n"
      "ds_read_b128 v[4:7], v241 offset:4096\n"
      "ds_read_b128 v[8:11], v241 offset:8192\n"
      "ds_read_b128 v[12:15], v241 offset:12288\n"
      "ds_read_b128 v[16:19], v241 offset:16384\n"
      "ds_read_b128 v[20:23], v241 offset:20480\n"
      "ds_read_b128 v[24:27], v241 offset:24576\n"
      "ds_read_b128 v[28:31], v241 offset:28672\n"
      "ds_read_b128 v[32:35], v241 offset:32768\n"
      "ds_read_b128 v[36:39], v241 offset:36864\n"
      "ds_read_b128 v[40:43], v241 offset:40960\n"
      "ds_read_b128 v[44:47], v241 offset:45056\n"
      "v_lshlrev_b32 v160, 16, v224\n"
      "v_and_b32 v161, s65, v224\n"
      "v_lshlrev_b32 v162, 16, v225\n"
      "v_and_b32 v163, s65, v225\n"
      "v_lshlrev_b32 v164, 16, v226\n"
      "v_and_b32 v165, s65, v226\n"
      "v_lshlrev_b32 v166, 16, v227\n"
      "v_and_b32 v167, s65, v227\n"
      "v_lshlrev_b32 v168, 16, v228\n"
      "v_and_b32 v169, s65, v228\n"
      "v_lshlrev_b32 v170, 16, v229\n"
      "v_and_b32 v171, s65, v229\n"
      "v_lshlrev_b32 v172, 16, v230\n"
      "v_and_b32 v173, s65, v230\n"
      "v_lshlrev_b32 v174, 16, v231\n"
      "v_and_b32 v175, s65, v231\n"
      "s_waitcnt lgkmcnt(10)\n"
      "v_pk_add_f32 v[0:1], v[0:1], v[4:5]\n"
      "v_pk_add_f32 v[2:3], v[2:3], v[6:7]\n"
      "s_waitcnt lgkmcnt(9)\n"
      "v_pk_add_f32 v[0:1], v[0:1], v[8:9]\n"
      "v_pk_add_f32 v[2:3], v[2:3], v[10:11]\n"
      "s_waitcnt lgkmcnt(8)\n"
      "v_pk_add_f32 v[0:1], v[0:1], v[12:13]\n"
      "v_pk_add_f32 v[2:3], v[2:3], v[14:15]\n"
      "v_pk_fma_f32 v[160:161], v[0:1], s[96:97], v[160:161] op_sel_hi:[1,0,1]\n"
      "v_pk_fma_f32 v[162:163], v[2:3], s[96:97], v[162:163] op_sel_hi:[1,0,1]\n"
      "v_cvt_pk_bf16_f32 v224, v160, v161\n"
      "v_cvt_pk_bf16_f32 v225, v162, v163\n"
      "s_waitcnt lgkmcnt(6)\n"
      "v_pk_add_f32 v[16:17], v[16:17], v[20:21]\n"
      "v_pk_add_f32 v[18:19], v[18:19], v[22:23]\n"
      "s_waitcnt lgkmcnt(5)\n"
      "v_pk_add_f32 v[16:17], v[16:17], v[24:25]\n"
      "v_pk_add_f32 v[18:19], v[18:19], v[26:27]\n"
      "s_waitcnt lgkmcnt(4)\n"
      "v_pk_add_f32 v[16:17], v[16:17], v[28:29]\n"
      "v_pk_add_f32 v[18:19], v[18:19], v[30:31]\n"
      "v_pk_fma_f32 v[164:165], v[16:17], s[96:97], v[164:165] op_sel_hi:[1,0,1]\n"
      "v_pk_fma_f32 v[166:167], v[18:19], s[96:97], v[166:167] op_sel_hi:[1,0,1]\n"
      "v_cvt_pk_bf16_f32 v226, v164, v165\n"
      "v_cvt_pk_bf16_f32 v227, v166, v167\n"
      "s_waitcnt lgkmcnt(2)\n"
      "v_pk_add_f32 v[32:33], v[32:33], v[36:37]\n"
      "v_pk_add_f32 v[34:35], v[34:35], v[38:39]\n"
      "s_waitcnt lgkmcnt(1)\n"
      "v_pk_add_f32 v[32:33], v[32:33], v[40:41]\n"
      "v_pk_add_f32 v[34:35], v[34:35], v[42:43]\n"
      "s_waitcnt lgkmcnt(0)\n"
      "v_pk_add_f32 v[32:33], v[32:33], v[44:45]\n"
      "v_pk_add_f32 v[34:35], v[34:35], v[46:47]\n"
      "v_pk_fma_f32 v[168:169], v[32:33], s[96:97], v[168:169] op_sel_hi:[1,0,1]\n"
      "v_pk_fma_f32 v[170:171], v[34:35], s[96:97], v[170:171] op_sel_hi:[1,0,1]\n"
      "v_cvt_pk_bf16_f32 v228, v168, v169\n"
      "v_cvt_pk_bf16_f32 v229, v170, v171\n"
      "s_sleep 3\n"
      "s_add_u32 s60, s44, 0\n"
      "buffer_load_dwordx4 v[120:123], v247, s[32:35], s60 offen offset:3072 sc1\n"
      "s_add_u32 s60, s44, 8192\n"
      "buffer_load_dwordx4 v[124:127], v247, s[32:35], s60 offen offset:3072 sc1\n"
      "s_mov_b32 s66, 0\n"
      "s_waitcnt vmcnt(0)\n"
      "s_branch Lcoc_74\n"
      "Lco_73:\n"
      "s_add_u32 s60, s44, 0\n"
      "buffer_load_dwordx4 v[120:123], v247, s[32:35], s60 offen offset:3072 sc1\n"
      "s_add_u32 s60, s44, 8192\n"
      "buffer_load_dwordx4 v[124:127], v247, s[32:35], s60 offen offset:3072 sc1\n"
      "s_waitcnt vmcnt(0)\n"
      "Lcoc_74:\n"
      "v_max3_u32 v206, v120, v121, v122\n"
      "v_max3_u32 v206, v206, v123, v124\n"
      "v_max3_u32 v206, v206, v125, v126\n"
      "v_max_u32 v206, v206, v127\n"
      "v_cmp_eq_u32 vcc, -1, v206\n"
      "s_cbranch_vccz Lcod_75\n"
      "s_sleep 1\n"
      "s_add_u32 s66, s66, 1\n"
      "s_cmp_lt_u32 s66, 131072\n"
      "s_cbranch_scc1 Lco_73\n"
      "Lcod_75:\n"
      "v_mfma_f32_16x16x32_bf16 v[48:51], v[64:67], v[120:123], v[48:51]\n"
      "v_mfma_f32_16x16x32_bf16 v[52:55], v[68:71], v[120:123], v[52:55]\n"
      "v_mfma_f32_16x16x32_bf16 v[56:59], v[72:75], v[120:123], v[56:59]\n"
      "v_mfma_f32_16x16x32_bf16 v[60:63], v[76:79], v[120:123], v[60:63]\n"
      "v_mfma_f32_16x16x32_bf16 v[48:51], v[80:83], v[124:127], v[48:51]\n"
      "v_mfma_f32_16x16x32_bf16 v[52:55], v[84:87], v[124:127], v[52:55]\n"
      "v_mfma_f32_16x16x32_bf16 v[56:59], v[88:91], v[124:127], v[56:59]\n"
      "v_mfma_f32_16x16x32_bf16 v[60:63], v[92:95], v[124:127], v[60:63]\n"
      "s_mov_b32 s61, 0\n"
      "buffer_store_dwordx2 v[224:225], v240, s[48:51], s61 offen sc1\n"
      "s_mov_b32 s61, 16384\n"
      "buffer_store_dwordx2 v[226:227], v240, s[48:51], s61 offen sc1\n"
      "s_mov_b32 s61, 32768\n"
      "buffer_store_dwordx2 v[228:229], v240, s[48:51], s61 offen sc1\n"
      "ds_write_b128 v248, v[48:51] offset:49152\n"
      "ds_write_b128 v249, v[52:55] offset:50176\n"
      "ds_write_b128 v250, v[56:59] offset:51200\n"
      "ds_write_b128 v251, v[60:63] offset:52224\n"
      "s_waitcnt lgkmcnt(0)\n"
      "s_barrier\n"
      "ds_read_b128 v[48:51], v241 offset:49152\n"
      "ds_read_b128 v[52:55], v241 offset:53248\n"
      "ds_read_b128 v[56:59], v241 offset:57344\n"
      "ds_read_b128 v[60:63], v241 offset:61440\n"
      "s_waitcnt lgkmcnt(2)\n"
      "v_pk_add_f32 v[48:49], v[48:49], v[52:53]\n"
      "v_pk_add_f32 v[50:51], v[50:51], v[54:55]\n"
      "s_waitcnt lgkmcnt(1)\n"
      "v_pk_add_f32 v[48:49], v[48:49], v[56:57]\n"
      "v_pk_add_f32 v[50:51], v[50:51], v[58:59]\n"
      "s_waitcnt lgkmcnt(0)\n"
      "v_pk_add_f32 v[48:49], v[48:49], v[60:61]\n"
      "v_pk_add_f32 v[50:51], v[50:51], v[62:63]\n"
      "v_pk_fma_f32 v[172:173], v[48:49], s[96:97], v[172:173] op_sel_hi:[1,0,1]\n"
      "v_pk_fma_f32 v[174:175], v[50:51], s[96:97], v[174:175] op_sel_hi:[1,0,1]\n"
      "v_cvt_pk_bf16_f32 v230, v172, v173\n"
      "v_cvt_pk_bf16_f32 v231, v174, v175\n"
      "s_mov_b32 s61, 49152\n"
      "buffer_store_dwordx2 v[230:231], v240, s[48:51], s61 offen sc1\n"
      "s_branch LEXIT\n"
      "Lcs_36r:\n"
      "s_mov_b32 s66, 0\n"
      "Lcs_36l:\n"
      "s_cmp_ge_u32 s76, 7\n"
      "s_cbranch_scc1 Lhk_76\n"
      "s_getpc_b64 s[72:73]\n"
      "Lret_77p:\n"
      "s_add_u32 s72, s72, Lret_77-Lret_77p\n"
      "s_addc_u32 s73, s73, 0\n"
      "s_branch LTRY\n"
      "Lret_77:\n"
      "s_nop 1\n"
      "Lhk_76:\n"
      "s_sleep 3\n"
      "s_sub_u32 s58, s44, 4096\n"
      "buffer_load_dwordx4 v[128:131], v247, s[32:35], s58 offen offset:0 sc1\n"
      "buffer_load_dwordx4 v[132:135], v247, s[32:35], s58 offen offset:1024 sc1\n"
      "buffer_load_dwordx4 v[136:139], v247, s[32:35], s58 offen offset:2048 sc1\n"
      "buffer_load_dwordx4 v[140:143], v247, s[32:35], s58 offen offset:3072 sc1\n"
      "s_waitcnt vmcnt(0)\n"
      "v_max3_u32 v206, v128, v129, v130\n"
      "v_max3_u32 v206, v206, v131, v132\n"
      "v_max3_u32 v206, v206, v133, v134\n"
      "v_max3_u32 v206, v206, v135, v136\n"
      "v_max3_u32 v206, v206, v137, v138\n"
      "v_max3_u32 v206, v206, v139, v140\n"
      "v_max3_u32 v206, v206, v141, v142\n"
      "v_max_u32 v206, v206, v143\n"
      "v_cmp_eq_u32 vcc, -1, v206\n"
      "s_cbranch_vccz Lcs_36\n"
      "s_add_u32 s66, s66, 1\n"
      "s_cmp_lt_u32 s66, 131072\n"
      "s_cbranch_scc1 Lcs_36l\n"
      "s_branch Lcs_36\n"
      "Lcs_39r:\n"
      "s_mov_b32 s66, 0\n"
      "Lcs_39l:\n"
      "s_cmp_ge_u32 s76, 7\n"
      "s_cbranch_scc1 Lhk_78\n"
      "s_getpc_b64 s[72:73]\n"
      "Lret_79p:\n"
      "s_add_u32 s72, s72, Lret_79-Lret_79p\n"
      "s_addc_u32 s73, s73, 0\n"
      "s_branch LTRY\n"
      "Lret_79:\n"
      "s_nop 1\n"
      "Lhk_78:\n"
      "s_sleep 3\n"
      "s_sub_u32 s58, s44, -4096\n"
      "buffer_load_dwordx4 v[144:147], v247, s[32:35], s58 offen offset:0 sc1\n"
      "buffer_load_dwordx4 v[148:151], v247, s[32:35], s58 offen offset:1024 sc1\n"
      "buffer_load_dwordx4 v[152:155], v247, s[32:35], s58 offen offset:2048 sc1\n"
      "buffer_load_dwordx4 v[156:159], v247, s[32:35], s58 offen offset:3072 sc1\n"
      "s_waitcnt vmcnt(0)\n"
      "v_max3_u32 v206, v144, v145, v146\n"
      "v_max3_u32 v206, v206, v147, v148\n"
      "v_max3_u32 v206, v206, v149, v150\n"
      "v_max3_u32 v206, v206, v151, v152\n"
      "v_max3_u32 v206, v206, v153, v154\n"
      "v_max3_u32 v206, v206, v155, v156\n"
      "v_max3_u32 v206, v206, v157, v158\n"
      "v_max_u32 v206, v206, v159\n"
      "v_cmp_eq_u32 vcc, -1, v206\n"
      "s_cbranch_vccz Lcs_39\n"
      "s_add_u32 s66, s66, 1\n"
      "s_cmp_lt_u32 s66, 131072\n"
      "s_cbranch_scc1 Lcs_39l\n"
      "s_branch Lcs_39\n"
      "LF2POLL:\n"
      "s_setprio 2\n"
      "s_add_u32 s22, s22, 4\n"
      "s_addc_u32 s23, s23, 0\n"
      "s_mov_b32 s66, 0\n"
      "s_waitcnt vmcnt(8)\n"
      "s_branch Lpfc_81\n"
      "Lpf_80:\n"
      "s_mov_b64 exec, s[98:99]\n"
      "buffer_load_dwordx2 v[212:213], v216, s[32:35], 0 offen sc1\n"
      "s_mov_b64 exec, -1\n"
      "s_waitcnt vmcnt(0)\n"
      "Lpfc_81:\n"
      "v_and_b32 v214, 0xffff0000, v212\n"
      "v_xor_b32 v214, s70, v214\n"
      "v_xor_b32 v215, s71, v213\n"
      "v_or_b32 v214, v214, v215\n"
      "v_cmp_ne_u32 vcc, 0, v214\n"
      "s_cbranch_vccz Lpfd_82\n"
      "s_mov_b64 s[98:99], vcc\n"
      "s_cmp_ge_u32 s76, 7\n"
      "s_cbranch_scc1 Lhk_83\n"
      "s_getpc_b64 s[72:73]\n"
      "Lret_84p:\n"
      "s_add_u32 s72, s72, Lret_84-Lret_84p\n"
      "s_addc_u32 s73, s73, 0\n"
      "s_branch LTRY\n"
      "Lret_84:\n"
      "s_nop 1\n"
      "Lhk_83:\n"
      "s_sleep 3\n"
      "s_add_u32 s66, s66, 1\n"
      "s_cmp_lt_u32 s66, 131072\n"
      "s_cbranch_scc1 Lpf_80\n"
      "Lpfd_82:\n"
      "s_setprio 0\n"
      "s_setpc_b64 s[22:23]\n"
      "LTRY:\n"
      "ds_read_b32 v200, v253 offset:256\n"
      "s_waitcnt lgkmcnt(0)\n"
      "v_cmp_eq_u32 vcc, 0, v200\n"
      "s_cbranch_vccz LRED2\n"
      "s_setpc_b64 s[72:73]\n"
      "LRED:\n"
      "s_mov_b32 s66, 0\n"
      "Lw4_85:\n"
      "ds_read_b32 v200, v253 offset:256\n"
      "s_waitcnt lgkmcnt(0)\n"
      "v_cmp_gt_u32 vcc, 1, v200\n"
      "s_cbranch_vccz Lw4d_86\n"
      "s_sleep 3\n"
      "s_add_u32 s66, s66, 1\n"
      "s_cmp_lt_u32 s66, 131072\n"
      "s_cbranch_scc1 Lw4_85\n"
      "Lw4d_86:\n"
      "LRED2:\n"
      "s_sub_u32 s61, s76, 6\n"
      "s_cselect_b32 s61, s76, s61\n"
      "s_mul_i32 s74, s61, 9216\n"
      "s_add_u32 s74, s74, 73728\n"
      "v_add_u32 v202, s74, v246\n"
      "v_add_u32 v204, s74, v245\n"
      "v_add_u32 v203, 64, v202\n"
      "v_add_u32 v205, 64, v204\n"
      "ds_read_b32 v200, v254 offset:80\n"
      "ds_read2st64_b32 v[192:193], v202 offset0:8 offset1:17\n"
      "ds_read2st64_b32 v[194:195], v202 offset0:26 offset1:35\n"
      "ds_read2st64_b32 v[196:197], v203 offset0:8 offset1:17\n"
      "ds_read2st64_b32 v[198:199], v203 offset0:26 offset1:35\n"
      "ds_read2st64_b32 v[160:161], v204 offset0:0 offset1:1\n"
      "ds_read2st64_b32 v[162:163], v204 offset0:2 offset1:3\n"
      "ds_read2st64_b32 v[164:165], v205 offset0:0 offset1:1\n"
      "ds_read2st64_b32 v[166:167], v205 offset0:2 offset1:3\n"
      "ds_read2st64_b32 v[168:169], v204 offset0:9 offset1:10\n"
      "ds_read2st64_b32 v[170:171], v204 offset0:11 offset1:12\n"
      "ds_read2st64_b32 v[172:173], v205 offset0:9 offset1:10\n"
      "ds_read2st64_b32 v[174:175], v205 offset0:11 offset1:12\n"
      "LRED3:\n"
      "s_waitcnt lgkmcnt(8)\n"
      "ds_read2st64_b32 v[176:177], v204 offset0:18 offset1:19\n"
      "ds_read2st64_b32 v[178:179], v204 offset0:20 offset1:21\n"
      "ds_read2st64_b32 v[180:181], v205 offset0:18 offset1:19\n"
      "ds_read2st64_b32 v[182:183], v205 offset0:20 offset1:21\n"
      "v_cmp_ne_u32 vcc, 0, v200\n"
      "s_waitcnt lgkmcnt(8)\n"
      "ds_read2st64_b32 v[184:185], v204 offset0:27 offset1:28\n"
      "ds_read2st64_b32 v[186:187], v204 offset0:29 offset1:30\n"
      "ds_read2st64_b32 v[188:189], v205 offset0:27 offset1:28\n"
      "ds_read2st64_b32 v[190:191], v205 offset0:29 offset1:30\n"
      "s_cbranch_vccnz LRED_G\n"
      "v_pk_add_f32 v[192:193], v[192:193], v[194:195]\n"
      "v_pk_add_f32 v[196:197], v[196:197], v[198:199]\n"
      "v_add_f32 v192, v192, v193\n"
      "v_add_f32 v193, v196, v197\n"
      "v_rcp_f32 v192, v192\n"
      "v_rcp_f32 v193, v193\n"
      "s_waitcnt lgkmcnt(8)\n"
      "v_pk_add_f32 v[160:161], v[160:161], v[168:169]\n"
      "v_pk_add_f32 v[162:163], v[162:163], v[170:171]\n"
      "v_pk_add_f32 v[164:165], v[164:165], v[172:173]\n"
      "v_pk_add_f32 v[166:167], v[166:167], v[174:175]\n"
      "s_waitcnt lgkmcnt(4)\n"
      "v_pk_add_f32 v[160:161], v[160:161], v[176:177]\n"
      "v_pk_add_f32 v[162:163], v[162:163], v[178:179]\n"
      "v_pk_add_f32 v[164:165], v[164:165], v[180:181]\n"
      "v_pk_add_f32 v[166:167], v[166:167], v[182:183]\n"
      "s_waitcnt lgkmcnt(0)\n"
      "v_pk_add_f32 v[160:161], v[160:161], v[184:185]\n"
      "v_pk_add_f32 v[162:163], v[162:163], v[186:187]\n"
      "v_pk_add_f32 v[164:165], v[164:165], v[188:189]\n"
      "v_pk_add_f32 v[166:167], v[166:167], v[190:191]\n"
      "LRED_F:\n"
      "v_pk_mul_f32 v[160:161], v[160:161], v[192:193] op_sel_hi:[1,0]\n"
      "v_pk_mul_f32 v[162:163], v[162:163], v[192:193] op_sel_hi:[1,0]\n"
      "v_pk_mul_f32 v[164:165], v[164:165], v[192:193] op_sel:[0,1] op_sel_hi:[1,1]\n"
      "v_pk_mul_f32 v[166:167], v[166:167], v[192:193] op_sel:[0,1] op_sel_hi:[1,1]\n"
      "v_cvt_pk_bf16_f32 v168, v160, v161\n"
      "v_cvt_pk_bf16_f32 v169, v162, v163\n"
      "v_cvt_pk_bf16_f32 v170, v164, v165\n"
      "v_cvt_pk_bf16_f32 v171, v166, v167\n"
      "v_min_u32 v168, -2, v168\n"
      "v_min_u32 v169, -2, v169\n"
      "v_min_u32 v170, -2, v170\n"
      "v_min_u32 v171, -2, v171\n"
      "s_lshl_b32 s61, s76, 10\n"
      "s_add_u32 s61, s61, s75\n"
      "buffer_store_dwordx4 v[168:171], v247, s[32:35], s61 offen sc1\n"
      "s_lshl_b32 s61, s76, 2\n"
      "s_add_u32 s74, s61, 161984\n"
      "v_mov_b32 v200, s74\n"
      "ds_write_b32 v200, v200\n"
      "s_or_b32 s61, s76, s77\n"
      "s_cbranch_scc0 LEPOCH\n"
      "LEPOCH2:\n"
      "s_lshl_b32 s61, s76, 2\n"
      "s_mov_b32 s76, 0x99999654\n"
      "s_bitcmp1_b32 s53, 2\n"
      "s_cselect_b32 s76, 0x99995432, s76\n"
      "s_lshr_b32 s76, s76, s61\n"
      "s_and_b32 s76, s76, 15\n"
      "v_lshl_add_u32 v253, s76, 4, v254\n"
      "s_setpc_b64 s[72:73]\n"
      "LEPOCH:\n"
      "v_mov_b32 v204, 0\n"
      "v_mov_b32 v202, s70\n"
      "v_mov_b32 v203, s71\n"
      "s_mov_b64 exec, 1\n"
      "buffer_store_dwordx2 v[202:203], v204, s[32:35], 0 offen sc1\n"
      "s_mov_b64 exec, -1\n"
      "s_branch LEPOCH2\n"
      "LRED_G:\n"
      "s_waitcnt lgkmcnt(0)\n"
      "v_add_u32 v242, 128, v202\n"
      "v_add_u32 v243, 128, v203\n"
      "ds_read2st64_b32 v[200:201], v242 offset0:8 offset1:17\n"
      "ds_read2st64_b32 v[202:203], v242 offset0:26 offset1:35\n"
      "ds_read2st64_b32 v[204:205], v243 offset0:8 offset1:17\n"
      "ds_read2st64_b32 v[206:207], v243 offset0:26 offset1:35\n"
      "s_waitcnt lgkmcnt(0)\n"
      "v_max3_f32 v243, v200, v201, v202\n"
      "v_max_f32 v243, v243, v203\n"
      "v_sub_f32 v200, v200, v243\n"
      "v_sub_f32 v201, v201, v243\n"
      "v_sub_f32 v202, v202, v243\n"
      "v_sub_f32 v203, v203, v243\n"
      "v_max3_f32 v243, v204, v205, v206\n"
      "v_max_f32 v243, v243, v207\n"
      "v_sub_f32 v204, v204, v243\n"
      "v_sub_f32 v205, v205, v243\n"
      "v_sub_f32 v206, v206, v243\n"
      "v_sub_f32 v207, v207, v243\n"
      "v_exp_f32 v200, v200\n"
      "v_exp_f32 v201, v201\n"
      "v_exp_f32 v202, v202\n"
      "v_exp_f32 v203, v203\n"
      "v_exp_f32 v204, v204\n"
      "v_exp_f32 v205, v205\n"
      "v_exp_f32 v206, v206\n"
      "v_exp_f32 v207, v207\n"
      "v_mul_f32 v192, v192, v200\n"
      "v_fmac_f32 v192, v193, v201\n"
      "v_fmac_f32 v192, v194, v202\n"
      "v_fmac_f32 v192, v195, v203\n"
      "v_mul_f32 v196, v196, v204\n"
      "v_fmac_f32 v196, v197, v205\n"
      "v_fmac_f32 v196, v198, v206\n"
      "v_fmac_f32 v196, v199, v207\n"
      "v_rcp_f32 v192, v192\n"
      "v_rcp_f32 v193, v196\n"
      "v_mul_f32 v160, v160, v200\n"
      "v_mul_f32 v161, v161, v200\n"
      "v_mul_f32 v162, v162, v200\n"
      "v_mul_f32 v163, v163, v200\n"
      "v_mul_f32 v164, v164, v204\n"
      "v_mul_f32 v165, v165, v204\n"
      "v_mul_f32 v166, v166, v204\n"
      "v_mul_f32 v167, v167, v204\n"
      "v_fmac_f32 v160, v168, v201\n"
      "v_fmac_f32 v161, v169, v201\n"
      "v_fmac_f32 v162, v170, v201\n"
      "v_fmac_f32 v163, v171, v201\n"
      "v_fmac_f32 v164, v172, v205\n"
      "v_fmac_f32 v165, v173, v205\n"
      "v_fmac_f32 v166, v174, v205\n"
      "v_fmac_f32 v167, v175, v205\n"
      "v_fmac_f32 v160, v176, v202\n"
      "v_fmac_f32 v161, v177, v202\n"
      "v_fmac_f32 v162, v178, v202\n"
      "v_fmac_f32 v163, v179, v202\n"
      "v_fmac_f32 v164, v180, v206\n"
      "v_fmac_f32 v165, v181, v206\n"
      "v_fmac_f32 v166, v182, v206\n"
      "v_fmac_f32 v167, v183, v206\n"
      "v_fmac_f32 v160, v184, v203\n"
      "v_fmac_f32 v161, v185, v203\n"
      "v_fmac_f32 v162, v186, v203\n"
      "v_fmac_f32 v163, v187, v203\n"
      "v_fmac_f32 v164, v188, v207\n"
      "v_fmac_f32 v165, v189, v207\n"
      "v_fmac_f32 v166, v190, v207\n"
      "v_fmac_f32 v167, v191, v207\n"
      "s_branch LRED_F\n"
      "LATT:\n"
      "s_setprio 2\n"
      "s_barrier\n"
      "s_mov_b32 s32, s26\n"
      "s_and_b32 s33, s27, 0xffff\n"
      "s_mov_b32 s34, 12582912\n"
      "s_mov_b32 s35, 0x20000\n"
      "s_lshl_b32 s58, s56, 2\n"
      "v_xor_b32 v192, s58, v244\n"
      "v_lshlrev_b32 v192, 4, v192\n"
      "s_lshl_b32 s59, s56, 12\n"
      "s_add_u32 s58, s55, 0\n"
      "s_and_b32 s58, s58, 3\n"
      "s_lshl_b32 s58, s58, 14\n"
      "s_or_b32 s58, s58, s59\n"
      "v_or_b32 v56, s58, v192\n"
      "v_xor_b32 v57, 1040, v56\n"
      "v_xor_b32 v58, 2080, v56\n"
      "v_xor_b32 v59, 3120, v56\n"
      "s_add_u32 s58, s55, 1\n"
      "s_and_b32 s58, s58, 3\n"
      "s_lshl_b32 s58, s58, 14\n"
      "s_or_b32 s58, s58, s59\n"
      "v_or_b32 v60, s58, v192\n"
      "v_xor_b32 v61, 1040, v60\n"
      "v_xor_b32 v62, 2080, v60\n"
      "v_xor_b32 v63, 3120, v60\n"
      "s_add_u32 s58, s55, 2\n"
      "s_and_b32 s58, s58, 3\n"
      "s_lshl_b32 s58, s58, 14\n"
      "s_or_b32 s58, s58, s59\n"
      "v_or_b32 v240, s58, v192\n"
      "v_xor_b32 v241, 1040, v240\n"
      "v_xor_b32 v242, 2080, v240\n"
      "v_xor_b32 v243, 3120, v240\n"
      "v_mov_b32 v192, s64\n"
      "s_mov_b32 vcc_lo, 0x10001\n"
      "s_mov_b32 vcc_hi, 0x10001\n"
      "v_cndmask_b32 v208, 0, v192, vcc\n"
      "v_cndmask_b32 v209, 0, v192, vcc\n"
      "v_mov_b64 v[210:211], v[208:209]\n"
      "s_mov_b32 vcc_lo, 0x100010\n"
      "s_mov_b32 vcc_hi, 0x100010\n"
      "v_cndmask_b32 v212, 0, v192, vcc\n"
      "v_cndmask_b32 v213, 0, v192, vcc\n"
      "v_mov_b64 v[214:215], v[212:213]\n"
      "s_mov_b32 vcc_lo, 0x1000100\n"
      "s_mov_b32 vcc_hi, 0x1000100\n"
      "v_cndmask_b32 v216, 0, v192, vcc\n"
      "v_cndmask_b32 v217, 0, v192, vcc\n"
      "v_mov_b64 v[218:219], v[216:217]\n"
      "s_mov_b32 vcc_lo, 0x10001000\n"
      "s_mov_b32 vcc_hi, 0x10001000\n"
      "v_cndmask_b32 v220, 0, v192, vcc\n"
      "v_cndmask_b32 v221, 0, v192, vcc\n"
      "v_mov_b64 v[222:223], v[220:221]\n"
      "s_lshl_b32 s59, s54, 2\n"
      "s_add_u32 s59, s59, 161792\n"
      "v_mov_b32 v248, s59\n"
      "s_mov_b32 s96, 0x3e317218\n"
      "v_mov_b32 v252, 1\n"
      "v_mov_b32 v251, 161792\n"
      "s_waitcnt vmcnt(3)\n"
      "v_dot2_f32_bf16 v228, v0, v0, 0\n"
      "s_add_u32 s75, s75, 16384\n"
      "s_and_b32 s75, s75, 0xffff\n"
      "buffer_load_dwordx4 v[16:19], v54, s[28:31], s75 offen offset:0\n"
      "buffer_load_dwordx4 v[20:23], v54, s[28:31], s75 offen offset:1024\n"
      "buffer_load_dwordx4 v[24:27], v54, s[28:31], s75 offen offset:2048\n"
      "buffer_load_dwordx4 v[28:31], v54, s[28:31], s75 offen offset:3072\n"
      "v_dot2_f32_bf16 v228, v1, v1, v228\n"
      "v_dot2_f32_bf16 v228, v2, v2, v228\n"
      "v_dot2_f32_bf16 v228, v3, v3, v228\n"
      "s_waitcnt vmcnt(6)\n"
      "v_dot2_f32_bf16 v229, v4, v4, 0\n"
      "v_dot2_f32_bf16 v229, v5, v5, v229\n"
      "v_dot2_f32_bf16 v229, v6, v6, v229\n"
      "v_dot2_f32_bf16 v229, v7, v7, v229\n"
      "s_waitcnt vmcnt(5)\n"
      "v_dot2_f32_bf16 v230, v8, v8, 0\n"
      "v_dot2_f32_bf16 v230, v9, v9, v230\n"
      "v_dot2_f32_bf16 v230, v10, v10, v230\n"
      "v_dot2_f32_bf16 v230, v11, v11, v230\n"
      "s_waitcnt vmcnt(4)\n"
      "v_dot2_f32_bf16 v231, v12, v12, 0\n"
      "ds_write_b32 v248, v252 offset:160\n"
      "v_dot2_f32_bf16 v231, v13, v13, v231\n"
      "v_dot2_f32_bf16 v231, v14, v14, v231\n"
      "v_dot2_f32_bf16 v231, v15, v15, v231\n"
      "v_mfma_f32_16x16x32_bf16 v[224:227], v[208:211], v[0:3], 0\n"
      "v_permlane32_swap_b32 v228, v230\n"
      "v_mfma_f32_16x16x32_bf16 v[224:227], v[212:215], v[4:7], v[224:227]\n"
      "v_permlane32_swap_b32 v229, v231\n"
      "v_mfma_f32_16x16x32_bf16 v[224:227], v[216:219], v[8:11], v[224:227]\n"
      "v_add_f32 v228, v228, v230\n"
      "v_add_f32 v229, v229, v231\n"
      "v_mfma_f32_16x16x32_bf16 v[224:227], v[220:223], v[12:15], v[224:227]\n"
      "v_lshlrev_b32 v192, 16, v0\n"
      "v_permlane16_swap_b32 v228, v229\n"
      "v_add_f32 v228, v228, v229\n"
      "v_and_b32 v193, s65, v0\n"
      "v_lshlrev_b32 v194, 16, v1\n"
      "v_add_f32_dpp v228, v228, v228 row_ror:8 row_mask:0xf bank_mask:0xf\n"
      "v_and_b32 v195, s65, v1\n"
      "v_lshlrev_b32 v196, 16, v2\n"
      "v_add_f32_dpp v224, v224, v224 row_ror:8 row_mask:0xf bank_mask:0xf\n"
      "v_add_f32_dpp v228, v228, v228 row_ror:4 row_mask:0xf bank_mask:0xf\n"
      "v_and_b32 v197, s65, v2\n"
      "v_add_f32_dpp v224, v224, v224 row_ror:4 row_mask:0xf bank_mask:0xf\n"
      "v_add_f32_dpp v228, v228, v228 row_ror:2 row_mask:0xf bank_mask:0xf\n"
      "v_lshlrev_b32 v198, 16, v3\n"
      "v_add_f32_dpp v224, v224, v224 row_ror:2 row_mask:0xf bank_mask:0xf\n"
      "v_add_f32_dpp v228, v228, v228 row_ror:1 row_mask:0xf bank_mask:0xf\n"
      "v_and_b32 v199, s65, v3\n"
      "v_add_f32_dpp v224, v224, v224 row_ror:1 row_mask:0xf bank_mask:0xf\n"
      "v_mul_f32 v224, 0x3b000000, v224\n"
      "v_mul_f32 v228, 0x3b000000, v228\n"
      "v_fma_f32 v228, -v224, v224, v228\n"
      "v_max_f32 v228, 0, v228\n"
      "v_fmaak_f32 v228, v49, v228, 0x36a1369a\n"
      "v_rsq_f32 v228, v228\n"
      "v_lshlrev_b32 v200, 16, v4\n"
      "v_mul_f32 v224, -v224, v228\n"
      "v_readlane_b32 s58, v228, 0\n"
      "v_readlane_b32 s59, v224, 0\n"
      "v_readlane_b32 s60, v228, 16\n"
      "v_readlane_b32 s61, v224, 16\n"
      "v_readlane_b32 s62, v228, 32\n"
      "v_readlane_b32 s63, v224, 32\n"
      "v_readlane_b32 s72, v228, 48\n"
      "v_readlane_b32 s73, v224, 48\n"
      "v_and_b32 v201, s65, v4\n"
      "v_lshlrev_b32 v202, 16, v5\n"
      "v_and_b32 v203, s65, v5\n"
      "v_lshlrev_b32 v204, 16, v6\n"
      "v_and_b32 v205, s65, v6\n"
      "v_lshlrev_b32 v206, 16, v7\n"
      "v_and_b32 v207, s65, v7\n"
      "v_pk_fma_f32 v[192:193], v[192:193], s[58:59], s[58:59] op_sel:[0,0,1] op_sel_hi:[1,0,1]\n"
      "v_pk_fma_f32 v[194:195], v[194:195], s[58:59], s[58:59] op_sel:[0,0,1] op_sel_hi:[1,0,1]\n"
      "v_pk_fma_f32 v[196:197], v[196:197], s[58:59], s[58:59] op_sel:[0,0,1] op_sel_hi:[1,0,1]\n"
      "v_pk_fma_f32 v[198:199], v[198:199], s[58:59], s[58:59] op_sel:[0,0,1] op_sel_hi:[1,0,1]\n"
      "v_cvt_pk_bf16_f32 v0, v192, v193\n"
      "v_cvt_pk_bf16_f32 v1, v194, v195\n"
      "v_cvt_pk_bf16_f32 v2, v196, v197\n"
      "v_cvt_pk_bf16_f32 v3, v198, v199\n"
      "ds_write_b128 v56, v[0:3]\n"
      "v_pk_fma_f32 v[200:201], v[200:201], s[60:61], s[60:61] op_sel:[0,0,1] op_sel_hi:[1,0,1]\n"
      "v_pk_fma_f32 v[202:203], v[202:203], s[60:61], s[60:61] op_sel:[0,0,1] op_sel_hi:[1,0,1]\n"
      "v_pk_fma_f32 v[204:205], v[204:205], s[60:61], s[60:61] op_sel:[0,0,1] op_sel_hi:[1,0,1]\n"
      "v_pk_fma_f32 v[206:207], v[206:207], s[60:61], s[60:61] op_sel:[0,0,1] op_sel_hi:[1,0,1]\n"
      "v_cvt_pk_bf16_f32 v4, v200, v201\n"
      "v_cvt_pk_bf16_f32 v5, v202, v203\n"
      "v_cvt_pk_bf16_f32 v6, v204, v205\n"
      "v_cvt_pk_bf16_f32 v7, v206, v207\n"
      "ds_write_b128 v57, v[4:7]\n"
      "v_lshlrev_b32 v192, 16, v8\n"
      "v_and_b32 v193, s65, v8\n"
      "v_lshlrev_b32 v194, 16, v9\n"
      "v_and_b32 v195, s65, v9\n"
      "v_lshlrev_b32 v196, 16, v10\n"
      "v_and_b32 v197, s65, v10\n"
      "v_lshlrev_b32 v198, 16, v11\n"
      "v_and_b32 v199, s65, v11\n"
      "v_pk_fma_f32 v[192:193], v[192:193], s[62:63], s[62:63] op_sel:[0,0,1] op_sel_hi:[1,0,1]\n"
      "v_pk_fma_f32 v[194:195], v[194:195], s[62:63], s[62:63] op_sel:[0,0,1] op_sel_hi:[1,0,1]\n"
      "v_pk_fma_f32 v[196:197], v[196:197], s[62:63], s[62:63] op_sel:[0,0,1] op_sel_hi:[1,0,1]\n"
      "v_pk_fma_f32 v[198:199], v[198:199], s[62:63], s[62:63] op_sel:[0,0,1] op_sel_hi:[1,0,1]\n"
      "v_cvt_pk_bf16_f32 v8, v192, v193\n"
      "v_cvt_pk_bf16_f32 v9, v194, v195\n"
      "v_cvt_pk_bf16_f32 v10, v196, v197\n"
      "v_cvt_pk_bf16_f32 v11, v198, v199\n"
      "ds_write_b128 v58, v[8:11]\n"
      "v_lshlrev_b32 v200, 16, v12\n"
      "v_and_b32 v201, s65, v12\n"
      "v_lshlrev_b32 v202, 16, v13\n"
      "v_and_b32 v203, s65, v13\n"
      "v_lshlrev_b32 v204, 16, v14\n"
      "v_and_b32 v205, s65, v14\n"
      "v_lshlrev_b32 v206, 16, v15\n"
      "v_and_b32 v207, s65, v15\n"
      "v_pk_fma_f32 v[200:201], v[200:201], s[72:73], s[72:73] op_sel:[0,0,1] op_sel_hi:[1,0,1]\n"
      "v_pk_fma_f32 v[202:203], v[202:203], s[72:73], s[72:73] op_sel:[0,0,1] op_sel_hi:[1,0,1]\n"
      "v_pk_fma_f32 v[204:205], v[204:205], s[72:73], s[72:73] op_sel:[0,0,1] op_sel_hi:[1,0,1]\n"
      "v_pk_fma_f32 v[206:207], v[206:207], s[72:73], s[72:73] op_sel:[0,0,1] op_sel_hi:[1,0,1]\n"
      "v_cvt_pk_bf16_f32 v12, v200, v201\n"
      "v_cvt_pk_bf16_f32 v13, v202, v203\n"
      "v_cvt_pk_bf16_f32 v14, v204, v205\n"
      "v_cvt_pk_bf16_f32 v15, v206, v207\n"
      "ds_write_b128 v59, v[12:15]\n"
      "ds_write_b32 v248, v252 offset:112\n"
      "s_add_u32 s75, s75, 16384\n"
      "s_and_b32 s75, s75, 0xffff\n"
      "buffer_load_dwordx4 v[32:35], v54, s[28:31], s75 offen offset:0\n"
      "buffer_load_dwordx4 v[36:39], v54, s[28:31], s75 offen offset:1024\n"
      "buffer_load_dwordx4 v[40:43], v54, s[28:31], s75 offen offset:2048\n"
      "buffer_load_dwordx4 v[44:47], v54, s[28:31], s75 offen offset:3072\n"
      "v_and_b32 v192, 15, v244\n"
      "v_cmp_eq_u32 vcc, 0, v192\n"
      "v_mov_b32 v193, s64\n"
      "s_nop 0\n"
      "v_cndmask_b32 v68, 0, v193, vcc\n"
      "v_mov_b32 v69, v68\n"
      "v_mov_b64 v[70:71], v[68:69]\n"
      "v_mov_b64 v[76:77], v[68:69]\n"
      "v_mov_b64 v[78:79], v[68:69]\n"
      "v_mov_b64 v[84:85], v[68:69]\n"
      "v_mov_b64 v[86:87], v[68:69]\n"
      "v_mov_b64 v[92:93], v[68:69]\n"
      "v_mov_b64 v[94:95], v[68:69]\n"
      "v_mov_b64 v[100:101], v[68:69]\n"
      "v_mov_b64 v[102:103], v[68:69]\n"
      "v_mov_b64 v[108:109], v[68:69]\n"
      "v_mov_b64 v[110:111], v[68:69]\n"
      "v_mov_b64 v[116:117], v[68:69]\n"
      "v_mov_b64 v[118:119], v[68:69]\n"
      "v_mov_b64 v[124:125], v[68:69]\n"
      "v_mov_b64 v[126:127], v[68:69]\n"
      "v_mov_b64 v[132:133], v[68:69]\n"
      "v_mov_b64 v[134:135], v[68:69]\n"
      "v_mov_b64 v[140:141], v[68:69]\n"
      "v_mov_b64 v[142:143], v[68:69]\n"
      "v_mov_b64 v[148:149], v[68:69]\n"
      "v_mov_b64 v[150:151], v[68:69]\n"
      "v_mov_b64 v[156:157], v[68:69]\n"
      "v_mov_b64 v[158:159], v[68:69]\n"
      "v_mov_b64 v[164:165], v[68:69]\n"
      "v_mov_b64 v[166:167], v[68:69]\n"
      "v_mov_b64 v[172:173], v[68:69]\n"
      "v_mov_b64 v[174:175], v[68:69]\n"
      "v_mov_b64 v[180:181], v[68:69]\n"
      "v_mov_b64 v[182:183], v[68:69]\n"
      "v_mov_b64 v[188:189], v[68:69]\n"
      "v_mov_b64 v[190:191], v[68:69]\n"
      "v_lshrrev_b32 v192, 5, v244\n"
      "v_bfe_u32 v193, v244, 2, 2\n"
      "v_lshl_add_u32 v194, v192, 3, v193\n"
      "v_and_b32 v193, 2, v193\n"
      "v_bfe_u32 v195, v244, 4, 1\n"
      "v_or_b32 v193, v193, v195\n"
      "s_and_b32 s60, s54, 2\n"
      "v_xor_b32 v193, s60, v193\n"
      "v_lshlrev_b32 v193, 5, v193\n"
      "v_lshl_or_b32 v194, v194, 7, v193\n"
      "v_and_b32 v193, 3, v244\n"
      "v_lshl_or_b32 v194, v193, 3, v194\n"
      "v_add_u32 v232, 65536, v194\n"
      "v_and_b32 v193, 15, v244\n"
      "v_xor_b32 v193, v193, v192\n"
      "v_lshlrev_b32 v193, 4, v193\n"
      "v_and_b32 v194, 31, v244\n"
      "v_lshl_or_b32 v193, v194, 10, v193\n"
      "s_and_b32 s59, s54, 1\n"
      "s_lshl_b32 s59, s59, 15\n"
      "v_add_u32 v236, s59, v193\n"
      "v_xor_b32 v237, 32, v236\n"
      "v_xor_b32 v238, 64, v236\n"
      "v_xor_b32 v239, 96, v236\n"
      "s_lshl_b32 s59, s54, 6\n"
      "v_add_u32 v234, s59, v244\n"
      "v_mul_u32_u24 v234, 64, v234\n"
      "v_add_u32 v234, 4096, v234\n"
      "v_lshlrev_b32 v192, 3, v192\n"
      "v_and_b32 v193, 15, v244\n"
      "v_lshl_or_b32 v192, v193, 4, v192\n"
      "v_lshl_or_b32 v192, v195, 10, v192\n"
      "s_and_b32 s59, s54, 1\n"
      "s_lshl_b32 s59, s59, 11\n"
      "s_lshr_b32 s60, s54, 1\n"
      "s_lshl_b32 s60, s60, 12\n"
      "s_add_u32 s59, s59, s60\n"
      "s_add_u32 s59, s59, 65536\n"
      "v_add_u32 v235, s59, v192\n"
      "v_mov_b32 v250, v247\n"
      "v_mov_b32 v249, 0xdddd\n"
      "s_waitcnt vmcnt(7)\n"
      "v_dot2_f32_bf16 v228, v16, v16, 0\n"
      "v_dot2_f32_bf16 v228, v17, v17, v228\n"
      "v_dot2_f32_bf16 v228, v18, v18, v228\n"
      "v_dot2_f32_bf16 v228, v19, v19, v228\n"
      "s_waitcnt vmcnt(6)\n"
      "v_dot2_f32_bf16 v229, v20, v20, 0\n"
      "v_dot2_f32_bf16 v229, v21, v21, v229\n"
      "v_dot2_f32_bf16 v229, v22, v22, v229\n"
      "v_dot2_f32_bf16 v229, v23, v23, v229\n"
      "s_waitcnt vmcnt(5)\n"
      "v_dot2_f32_bf16 v230, v24, v24, 0\n"
      "v_dot2_f32_bf16 v230, v25, v25, v230\n"
      "v_dot2_f32_bf16 v230, v26, v26, v230\n"
      "v_dot2_f32_bf16 v230, v27, v27, v230\n"
      "s_waitcnt vmcnt(4)\n"
      "v_dot2_f32_bf16 v231, v28, v28, 0\n"
      "v_dot2_f32_bf16 v231, v29, v29, v231\n"
      "v_dot2_f32_bf16 v231, v30, v30, v231\n"
      "v_dot2_f32_bf16 v231, v31, v31, v231\n"
      "v_mfma_f32_16x16x32_bf16 v[224:227], v[208:211], v[16:19], 0\n"
      "v_permlane32_swap_b32 v228, v230\n"
      "v_mfma_f32_16x16x32_bf16 v[224:227], v[212:215], v[20:23], v[224:227]\n"
      "v_permlane32_swap_b32 v229, v231\n"
      "v_mfma_f32_16x16x32_bf16 v[224:227], v[216:219], v[24:27], v[224:227]\n"
      "v_add_f32 v228, v228, v230\n"
      "v_add_f32 v229, v229, v231\n"
      "v_mfma_f32_16x16x32_bf16 v[224:227], v[220:223], v[28:31], v[224:227]\n"
      "v_lshlrev_b32 v192, 16, v16\n"
      "v_permlane16_swap_b32 v228, v229\n"
      "v_add_f32 v228, v228, v229\n"
      "v_and_b32 v193, s65, v16\n"
      "v_lshlrev_b32 v194, 16, v17\n"
      "v_add_f32_dpp v228, v228, v228 row_ror:8 row_mask:0xf bank_mask:0xf\n"
      "v_and_b32 v195, s65, v17\n"
      "v_lshlrev_b32 v196, 16, v18\n"
      "v_add_f32_dpp v224, v224, v224 row_ror:8 row_mask:0xf bank_mask:0xf\n"
      "v_add_f32_dpp v228, v228, v228 row_ror:4 row_mask:0xf bank_mask:0xf\n"
      "v_and_b32 v197, s65, v18\n"
      "v_add_f32_dpp v224, v224, v224 row_ror:4 row_mask:0xf bank_mask:0xf\n"
      "v_add_f32_dpp v228, v228, v228 row_ror:2 row_mask:0xf bank_mask:0xf\n"
      "v_lshlrev_b32 v198, 16, v19\n"
      "v_add_f32_dpp v224, v224, v224 row_ror:2 row_mask:0xf bank_mask:0xf\n"
      "v_add_f32_dpp v228, v228, v228 row_ror:1 row_mask:0xf bank_mask:0xf\n"
      "v_and_b32 v199, s65, v19\n"
      "v_add_f32_dpp v224, v224, v224 row_ror:1 row_mask:0xf bank_mask:0xf\n"
      "v_mul_f32 v224, 0x3b000000, v224\n"
      "v_mul_f32 v228, 0x3b000000, v228\n"
      "v_fma_f32 v228, -v224, v224, v228\n"
      "v_max_f32 v228, 0, v228\n"
      "v_fmaak_f32 v228, v49, v228, 0x36a1369a\n"
      "v_rsq_f32 v228, v228\n"
      "v_lshlrev_b32 v200, 16, v20\n"
      "v_mul_f32 v224, -v224, v228\n"
      "v_readlane_b32 s58, v228, 0\n"
      "v_readlane_b32 s59, v224, 0\n"
      "v_readlane_b32 s60, v228, 16\n"
      "v_readlane_b32 s61, v224, 16\n"
      "v_readlane_b32 s62, v228, 32\n"
      "v_readlane_b32 s63, v224, 32\n"
      "v_readlane_b32 s72, v228, 48\n"
      "v_readlane_b32 s73, v224, 48\n"
      "v_and_b32 v201, s65, v20\n"
      "v_lshlrev_b32 v202, 16, v21\n"
      "v_and_b32 v203, s65, v21\n"
      "v_lshlrev_b32 v204, 16, v22\n"
      "v_and_b32 v205, s65, v22\n"
      "v_lshlrev_b32 v206, 16, v23\n"
      "v_and_b32 v207, s65, v23\n"
      "v_pk_fma_f32 v[192:193], v[192:193], s[58:59], s[58:59] op_sel:[0,0,1] op_sel_hi:[1,0,1]\n"
      "v_pk_fma_f32 v[194:195], v[194:195], s[58:59], s[58:59] op_sel:[0,0,1] op_sel_hi:[1,0,1]\n"
      "v_pk_fma_f32 v[196:197], v[196:197], s[58:59], s[58:59] op_sel:[0,0,1] op_sel_hi:[1,0,1]\n"
      "v_pk_fma_f32 v[198:199], v[198:199], s[58:59], s[58:59] op_sel:[0,0,1] op_sel_hi:[1,0,1]\n"
      "v_cvt_pk_bf16_f32 v16, v192, v193\n"
      "v_cvt_pk_bf16_f32 v17, v194, v195\n"
      "v_cvt_pk_bf16_f32 v18, v196, v197\n"
      "v_cvt_pk_bf16_f32 v19, v198, v199\n"
      "ds_write_b128 v60, v[16:19]\n"
      "v_pk_fma_f32 v[200:201], v[200:201], s[60:61], s[60:61] op_sel:[0,0,1] op_sel_hi:[1,0,1]\n"
      "v_pk_fma_f32 v[202:203], v[202:203], s[60:61], s[60:61] op_sel:[0,0,1] op_sel_hi:[1,0,1]\n"
      "v_pk_fma_f32 v[204:205], v[204:205], s[60:61], s[60:61] op_sel:[0,0,1] op_sel_hi:[1,0,1]\n"
      "v_pk_fma_f32 v[206:207], v[206:207], s[60:61], s[60:61] op_sel:[0,0,1] op_sel_hi:[1,0,1]\n"
      "v_cvt_pk_bf16_f32 v20, v200, v201\n"
      "v_cvt_pk_bf16_f32 v21, v202, v203\n"
      "v_cvt_pk_bf16_f32 v22, v204, v205\n"
      "v_cvt_pk_bf16_f32 v23, v206, v207\n"
      "ds_write_b128 v61, v[20:23]\n"
      "v_lshlrev_b32 v192, 16, v24\n"
      "v_and_b32 v193, s65, v24\n"
      "v_lshlrev_b32 v194, 16, v25\n"
      "v_and_b32 v195, s65, v25\n"
      "v_lshlrev_b32 v196, 16, v26\n"
      "v_and_b32 v197, s65, v26\n"
      "v_lshlrev_b32 v198, 16, v27\n"
      "v_and_b32 v199, s65, v27\n"
      "v_pk_fma_f32 v[192:193], v[192:193], s[62:63], s[62:63] op_sel:[0,0,1] op_sel_hi:[1,0,1]\n"
      "v_pk_fma_f32 v[194:195], v[194:195], s[62:63], s[62:63] op_sel:[0,0,1] op_sel_hi:[1,0,1]\n"
      "v_pk_fma_f32 v[196:197], v[196:197], s[62:63], s[62:63] op_sel:[0,0,1] op_sel_hi:[1,0,1]\n"
      "v_pk_fma_f32 v[198:199], v[198:199], s[62:63], s[62:63] op_sel:[0,0,1] op_sel_hi:[1,0,1]\n"
      "v_cvt_pk_bf16_f32 v24, v192, v193\n"
      "v_cvt_pk_bf16_f32 v25, v194, v195\n"
      "v_cvt_pk_bf16_f32 v26, v196, v197\n"
      "v_cvt_pk_bf16_f32 v27, v198, v199\n"
      "ds_write_b128 v62, v[24:27]\n"
      "v_lshlrev_b32 v200, 16, v28\n"
      "v_and_b32 v201, s65, v28\n"
      "v_lshlrev_b32 v202, 16, v29\n"
      "v_and_b32 v203, s65, v29\n"
      "v_lshlrev_b32 v204, 16, v30\n"
      "v_and_b32 v205, s65, v30\n"
      "v_lshlrev_b32 v206, 16, v31\n"
      "v_and_b32 v207, s65, v31\n"
      "v_pk_fma_f32 v[200:201], v[200:201], s[72:73], s[72:73] op_sel:[0,0,1] op_sel_hi:[1,0,1]\n"
      "v_pk_fma_f32 v[202:203], v[202:203], s[72:73], s[72:73] op_sel:[0,0,1] op_sel_hi:[1,0,1]\n"
      "v_pk_fma_f32 v[204:205], v[204:205], s[72:73], s[72:73] op_sel:[0,0,1] op_sel_hi:[1,0,1]\n"
      "v_pk_fma_f32 v[206:207], v[206:207], s[72:73], s[72:73] op_sel:[0,0,1] op_sel_hi:[1,0,1]\n"
      "v_cvt_pk_bf16_f32 v28, v200, v201\n"
      "v_cvt_pk_bf16_f32 v29, v202, v203\n"
      "v_cvt_pk_bf16_f32 v30, v204, v205\n"
      "v_cvt_pk_bf16_f32 v31, v206, v207\n"
      "ds_write_b128 v63, v[28:31]\n"
      "s_waitcnt vmcnt(3)\n"
      "v_dot2_f32_bf16 v228, v32, v32, 0\n"
      "v_dot2_f32_bf16 v228, v33, v33, v228\n"
      "v_dot2_f32_bf16 v228, v34, v34, v228\n"
      "v_dot2_f32_bf16 v228, v35, v35, v228\n"
      "s_waitcnt vmcnt(2)\n"
      "v_dot2_f32_bf16 v229, v36, v36, 0\n"
      "v_dot2_f32_bf16 v229, v37, v37, v229\n"
      "v_dot2_f32_bf16 v229, v38, v38, v229\n"
      "v_dot2_f32_bf16 v229, v39, v39, v229\n"
      "s_waitcnt vmcnt(1)\n"
      "v_dot2_f32_bf16 v230, v40, v40, 0\n"
      "v_dot2_f32_bf16 v230, v41, v41, v230\n"
      "v_dot2_f32_bf16 v230, v42, v42, v230\n"
      "v_dot2_f32_bf16 v230, v43, v43, v230\n"
      "s_waitcnt vmcnt(0)\n"
      "v_dot2_f32_bf16 v231, v44, v44, 0\n"
      "v_dot2_f32_bf16 v231, v45, v45, v231\n"
      "v_dot2_f32_bf16 v231, v46, v46, v231\n"
      "v_dot2_f32_bf16 v231, v47, v47, v231\n"
      "v_mfma_f32_16x16x32_bf16 v[224:227], v[208:211], v[32:35], 0\n"
      "v_permlane32_swap_b32 v228, v230\n"
      "v_mfma_f32_16x16x32_bf16 v[224:227], v[212:215], v[36:39], v[224:227]\n"
      "v_permlane32_swap_b32 v229, v231\n"
      "v_mfma_f32_16x16x32_bf16 v[224:227], v[216:219], v[40:43], v[224:227]\n"
      "v_add_f32 v228, v228, v230\n"
      "v_add_f32 v229, v229, v231\n"
      "v_mfma_f32_16x16x32_bf16 v[224:227], v[220:223], v[44:47], v[224:227]\n"
      "v_lshlrev_b32 v192, 16, v32\n"
      "v_permlane16_swap_b32 v228, v229\n"
      "v_add_f32 v228, v228, v229\n"
      "v_and_b32 v193, s65, v32\n"
      "v_lshlrev_b32 v194, 16, v33\n"
      "v_add_f32_dpp v228, v228, v228 row_ror:8 row_mask:0xf bank_mask:0xf\n"
      "v_and_b32 v195, s65, v33\n"
      "v_lshlrev_b32 v196, 16, v34\n"
      "v_add_f32_dpp v224, v224, v224 row_ror:8 row_mask:0xf bank_mask:0xf\n"
      "v_add_f32_dpp v228, v228, v228 row_ror:4 row_mask:0xf bank_mask:0xf\n"
      "v_and_b32 v197, s65, v34\n"
      "v_add_f32_dpp v224, v224, v224 row_ror:4 row_mask:0xf bank_mask:0xf\n"
      "v_add_f32_dpp v228, v228, v228 row_ror:2 row_mask:0xf bank_mask:0xf\n"
      "v_lshlrev_b32 v198, 16, v35\n"
      "v_add_f32_dpp v224, v224, v224 row_ror:2 row_mask:0xf bank_mask:0xf\n"
      "v_add_f32_dpp v228, v228, v228 row_ror:1 row_mask:0xf bank_mask:0xf\n"
      "v_and_b32 v199, s65, v35\n"
      "v_add_f32_dpp v224, v224, v224 row_ror:1 row_mask:0xf bank_mask:0xf\n"
      "v_mul_f32 v224, 0x3b000000, v224\n"
      "v_mul_f32 v228, 0x3b000000, v228\n"
      "v_fma_f32 v228, -v224, v224, v228\n"
      "v_max_f32 v228, 0, v228\n"
      "v_fmaak_f32 v228, v49, v228, 0x36a1369a\n"
      "v_rsq_f32 v228, v228\n"
      "v_lshlrev_b32 v200, 16, v36\n"
      "v_mul_f32 v224, -v224, v228\n"
      "v_readlane_b32 s58, v228, 0\n"
      "v_readlane_b32 s59, v224, 0\n"
      "v_readlane_b32 s60, v228, 16\n"
      "v_readlane_b32 s61, v224, 16\n"
      "v_readlane_b32 s62, v228, 32\n"
      "v_readlane_b32 s63, v224, 32\n"
      "v_readlane_b32 s72, v228, 48\n"
      "v_readlane_b32 s73, v224, 48\n"
      "v_and_b32 v201, s65, v36\n"
      "v_lshlrev_b32 v202, 16, v37\n"
      "v_and_b32 v203, s65, v37\n"
      "v_lshlrev_b32 v204, 16, v38\n"
      "v_and_b32 v205, s65, v38\n"
      "v_lshlrev_b32 v206, 16, v39\n"
      "v_and_b32 v207, s65, v39\n"
      "v_pk_fma_f32 v[192:193], v[192:193], s[58:59], s[58:59] op_sel:[0,0,1] op_sel_hi:[1,0,1]\n"
      "v_pk_fma_f32 v[194:195], v[194:195], s[58:59], s[58:59] op_sel:[0,0,1] op_sel_hi:[1,0,1]\n"
      "v_pk_fma_f32 v[196:197], v[196:197], s[58:59], s[58:59] op_sel:[0,0,1] op_sel_hi:[1,0,1]\n"
      "v_pk_fma_f32 v[198:199], v[198:199], s[58:59], s[58:59] op_sel:[0,0,1] op_sel_hi:[1,0,1]\n"
      "v_cvt_pk_bf16_f32 v32, v192, v193\n"
      "v_cvt_pk_bf16_f32 v33, v194, v195\n"
      "v_cvt_pk_bf16_f32 v34, v196, v197\n"
      "v_cvt_pk_bf16_f32 v35, v198, v199\n"
      "ds_write_b128 v240, v[32:35]\n"
      "v_pk_fma_f32 v[200:201], v[200:201], s[60:61], s[60:61] op_sel:[0,0,1] op_sel_hi:[1,0,1]\n"
      "v_pk_fma_f32 v[202:203], v[202:203], s[60:61], s[60:61] op_sel:[0,0,1] op_sel_hi:[1,0,1]\n"
      "v_pk_fma_f32 v[204:205], v[204:205], s[60:61], s[60:61] op_sel:[0,0,1] op_sel_hi:[1,0,1]\n"
      "v_pk_fma_f32 v[206:207], v[206:207], s[60:61], s[60:61] op_sel:[0,0,1] op_sel_hi:[1,0,1]\n"
      "v_cvt_pk_bf16_f32 v36, v200, v201\n"
      "v_cvt_pk_bf16_f32 v37, v202, v203\n"
      "v_cvt_pk_bf16_f32 v38, v204, v205\n"
      "v_cvt_pk_bf16_f32 v39, v206, v207\n"
      "ds_write_b128 v241, v[36:39]\n"
      "v_lshlrev_b32 v192, 16, v40\n"
      "v_and_b32 v193, s65, v40\n"
      "v_lshlrev_b32 v194, 16, v41\n"
      "v_and_b32 v195, s65, v41\n"
      "v_lshlrev_b32 v196, 16, v42\n"
      "v_and_b32 v197, s65, v42\n"
      "v_lshlrev_b32 v198, 16, v43\n"
      "v_and_b32 v199, s65, v43\n"
      "v_pk_fma_f32 v[192:193], v[192:193], s[62:63], s[62:63] op_sel:[0,0,1] op_sel_hi:[1,0,1]\n"
      "v_pk_fma_f32 v[194:195], v[194:195], s[62:63], s[62:63] op_sel:[0,0,1] op_sel_hi:[1,0,1]\n"
      "v_pk_fma_f32 v[196:197], v[196:197], s[62:63], s[62:63] op_sel:[0,0,1] op_sel_hi:[1,0,1]\n"
      "v_pk_fma_f32 v[198:199], v[198:199], s[62:63], s[62:63] op_sel:[0,0,1] op_sel_hi:[1,0,1]\n"
      "v_cvt_pk_bf16_f32 v40, v192, v193\n"
      "v_cvt_pk_bf16_f32 v41, v194, v195\n"
      "v_cvt_pk_bf16_f32 v42, v196, v197\n"
      "v_cvt_pk_bf16_f32 v43, v198, v199\n"
      "ds_write_b128 v242, v[40:43]\n"
      "v_lshlrev_b32 v200, 16, v44\n"
      "v_and_b32 v201, s65, v44\n"
      "v_lshlrev_b32 v202, 16, v45\n"
      "v_and_b32 v203, s65, v45\n"
      "v_lshlrev_b32 v204, 16, v46\n"
      "v_and_b32 v205, s65, v46\n"
      "v_lshlrev_b32 v206, 16, v47\n"
      "v_and_b32 v207, s65, v47\n"
      "v_pk_fma_f32 v[200:201], v[200:201], s[72:73], s[72:73] op_sel:[0,0,1] op_sel_hi:[1,0,1]\n"
      "v_pk_fma_f32 v[202:203], v[202:203], s[72:73], s[72:73] op_sel:[0,0,1] op_sel_hi:[1,0,1]\n"
      "v_pk_fma_f32 v[204:205], v[204:205], s[72:73], s[72:73] op_sel:[0,0,1] op_sel_hi:[1,0,1]\n"
      "v_pk_fma_f32 v[206:207], v[206:207], s[72:73], s[72:73] op_sel:[0,0,1] op_sel_hi:[1,0,1]\n"
      "v_cvt_pk_bf16_f32 v44, v200, v201\n"
      "v_cvt_pk_bf16_f32 v45, v202, v203\n"
      "v_cvt_pk_bf16_f32 v46, v204, v205\n"
      "v_cvt_pk_bf16_f32 v47, v206, v207\n"
      "ds_write_b128 v243, v[44:47]\n"
      "s_setprio 0\n"
      "v_add_u32 v247, 65536, v250\n"
      "s_lshl_b32 s69, s54, 15\n"
      "s_add_u32 s69, s69, 1048576\n"
      "s_lshl_b32 s67, s77, 8\n"
      "s_and_b32 s67, s67, 0x7800\n"
      "s_or_b32 s69, s67, s69\n"
      "v_xor_b32 v240, 128, v236\n"
      "v_xor_b32 v241, 160, v236\n"
      "v_xor_b32 v242, 192, v236\n"
      "v_xor_b32 v243, 224, v236\n"
      "v_and_b32 v246, 15, v244\n"
      "v_lshlrev_b32 v246, 2, v246\n"
      "v_bfe_u32 v194, v244, 4, 1\n"
      "v_lshl_or_b32 v245, v194, 7, v246\n"
      "v_lshrrev_b32 v194, 5, v244\n"
      "v_lshl_or_b32 v245, v194, 10, v245\n"
      "v_and_b32 v254, 3, v244\n"
      "v_lshlrev_b32 v254, 2, v254\n"
      "v_add_u32 v254, 161792, v254\n"
      "s_mov_b32 s74, 82944\n"
      "s_mov_b32 s76, 7\n"
      "s_mul_i32 s75, s52, 294912\n"
      "s_lshl_b32 s61, s53, 13\n"
      "s_add_u32 s75, s75, s61\n"
      "s_add_u32 s75, s75, 2326528\n"
      "s_lshl_b32 s60, s54, 8\n"
      "s_add_u32 s45, s75, s60\n"
      "s_add_u32 s45, s45, 7168\n"
      "s_lshr_b32 s60, s54, 1\n"
      "s_lshl_b32 s60, s60, 10\n"
      "s_and_b32 s61, s54, 1\n"
      "s_lshl_b32 s61, s61, 7\n"
      "s_or_b32 s44, s60, s61\n"
      "s_getpc_b64 s[72:73]\n"
      "LR7p:\n"
      "s_add_u32 s72, s72, Lret_r7-LR7p\n"
      "s_addc_u32 s73, s73, 0\n"
      "s_mul_i32 s59, s54, 2304\n"
      "s_add_u32 m0, s59, 57344\n"
      "s_waitcnt vmcnt(0) lgkmcnt(0)\n"
      "s_barrier\n"
      "s_setprio 1\n"
      "ds_read_b64 v[228:229], v251 offset:512\n"
      "ds_read_b64_tr_b16 v[32:33], v232 offset:0\n"
      "ds_read_b64_tr_b16 v[34:35], v232 offset:512\n"
      "ds_read_b128 v[40:43], v236 offset:0\n"
      "ds_read_b64_tr_b16 v[36:37], v232 offset:2048\n"
      "ds_read_b64_tr_b16 v[38:39], v232 offset:2560\n"
      "ds_read_b128 v[44:47], v237 offset:0\n"
      "ds_read_b64_tr_b16 v[48:49], v232 offset:4096\n"
      "ds_read_b64_tr_b16 v[50:51], v232 offset:4608\n"
      "ds_read_b128 v[56:59], v238 offset:0\n"
      "ds_read_b64_tr_b16 v[52:53], v232 offset:6144\n"
      "ds_read_b64_tr_b16 v[54:55], v232 offset:6656\n"
      "ds_read_b128 v[60:63], v239 offset:0\n"
      "s_waitcnt lgkmcnt(6)\n"
      "v_mfma_f32_32x32x16_bf16 v[192:207], v[32:35], v[40:43], 0\n"
      "ds_read_b64_tr_b16 v[208:209], v232 offset:8192\n"
      "ds_read_b64_tr_b16 v[210:211], v232 offset:8704\n"
      "ds_read_b128 v[216:219], v240 offset:0\n"
      "v_mfma_f32_32x32x16_bf16 v[192:207], v[36:39], v[44:47], v[192:207]\n"
      "ds_read_b64_tr_b16 v[212:213], v232 offset:10240\n"
      "ds_read_b64_tr_b16 v[214:215], v232 offset:10752\n"
      "ds_read_b128 v[220:223], v241 offset:0\n"
      "buffer_load_dwordx2 v[224:225], v234, s[32:35], 0 offen sc1\n"
      "s_waitcnt lgkmcnt(6)\n"
      "v_mfma_f32_32x32x16_bf16 v[192:207], v[48:51], v[56:59], v[192:207]\n"
      "ds_read_b64_tr_b16 v[32:33], v232 offset:12288\n"
      "ds_read_b64_tr_b16 v[34:35], v232 offset:12800\n"
      "ds_read_b128 v[40:43], v242 offset:0\n"
      "v_mfma_f32_32x32x16_bf16 v[192:207], v[52:55], v[60:63], v[192:207]\n"
      "ds_read_b64_tr_b16 v[36:37], v232 offset:14336\n"
      "ds_read_b64_tr_b16 v[38:39], v232 offset:14848\n"
      "ds_read_b128 v[44:47], v243 offset:0\n"
      "ds_write_b32 v248, v252 offset:16\n"
      "v_readfirstlane_b32 s70, v228\n"
      "v_readfirstlane_b32 s71, v229\n"
      "s_waitcnt lgkmcnt(7)\n"
      "v_mfma_f32_32x32x16_bf16 v[192:207], v[208:211], v[216:219], v[192:207]\n"
      "ds_read_b64_tr_b16 v[48:49], v232 offset:16384\n"
      "ds_read_b64_tr_b16 v[50:51], v232 offset:16896\n"
      "ds_read_b128 v[56:59], v236 offset:256\n"
      "v_mfma_f32_32x32x16_bf16 v[192:207], v[212:215], v[220:223], v[192:207]\n"
      "ds_read_b64_tr_b16 v[52:53], v232 offset:18432\n"
      "ds_read_b64_tr_b16 v[54:55], v232 offset:18944\n"
      "ds_read_b128 v[60:63], v237 offset:256\n"
      "s_waitcnt lgkmcnt(7)\n"
      "v_mfma_f32_32x32x16_bf16 v[192:207], v[32:35], v[40:43], v[192:207]\n"
      "ds_read_b64_tr_b16 v[208:209], v232 offset:20480\n"
      "ds_read_b64_tr_b16 v[210:211], v232 offset:20992\n"
      "ds_read_b128 v[216:219], v238 offset:256\n"
      "v_mfma_f32_32x32x16_bf16 v[192:207], v[36:39], v[44:47], v[192:207]\n"
      "ds_read_b64_tr_b16 v[212:213], v232 offset:22528\n"
      "ds_read_b64_tr_b16 v[214:215], v232 offset:23040\n"
      "ds_read_b128 v[220:223], v239 offset:256\n"
      "s_waitcnt lgkmcnt(6)\n"
      "v_mfma_f32_32x32x16_bf16 v[192:207], v[48:51], v[56:59], v[192:207]\n"
      "ds_read_b64_tr_b16 v[32:33], v232 offset:24576\n"
      "ds_read_b64_tr_b16 v[34:35], v232 offset:25088\n"
      "ds_read_b128 v[40:43], v240 offset:256\n"
      "v_mfma_f32_32x32x16_bf16 v[192:207], v[52:55], v[60:63], v[192:207]\n"
      "ds_read_b64_tr_b16 v[36:37], v232 offset:26624\n"
      "ds_read_b64_tr_b16 v[38:39], v232 offset:27136\n"
      "ds_read_b128 v[44:47], v241 offset:256\n"
      "s_waitcnt lgkmcnt(6)\n"
      "v_mfma_f32_32x32x16_bf16 v[192:207], v[208:211], v[216:219], v[192:207]\n"
      "ds_read_b64_tr_b16 v[48:49], v232 offset:28672\n"
      "ds_read_b64_tr_b16 v[50:51], v232 offset:29184\n"
      "ds_read_b128 v[56:59], v242 offset:256\n"
      "v_mfma_f32_32x32x16_bf16 v[192:207], v[212:215], v[220:223], v[192:207]\n"
      "ds_read_b64_tr_b16 v[52:53], v232 offset:30720\n"
      "ds_read_b64_tr_b16 v[54:55], v232 offset:31232\n"
      "ds_read_b128 v[60:63], v243 offset:256\n"
      "s_waitcnt lgkmcnt(6)\n"
      "v_mfma_f32_32x32x16_bf16 v[192:207], v[32:35], v[40:43], v[192:207]\n"
      "ds_read_b64_tr_b16 v[208:209], v232 offset:32768\n"
      "ds_read_b64_tr_b16 v[210:211], v232 offset:33280\n"
      "ds_read_b128 v[216:219], v236 offset:512\n"
      "v_mfma_f32_32x32x16_bf16 v[192:207], v[36:39], v[44:47], v[192:207]\n"
      "ds_read_b64_tr_b16 v[212:213], v232 offset:34816\n"
      "ds_read_b64_tr_b16 v[214:215], v232 offset:35328\n"
      "ds_read_b128 v[220:223], v237 offset:512\n"
      "s_waitcnt lgkmcnt(6)\n"
      "v_mfma_f32_32x32x16_bf16 v[192:207], v[48:51], v[56:59], v[192:207]\n"
      "ds_read_b64_tr_b16 v[32:33], v232 offset:36864\n"
      "ds_read_b64_tr_b16 v[34:35], v232 offset:37376\n"
      "ds_read_b128 v[40:43], v238 offset:512\n"
      "v_mfma_f32_32x32x16_bf16 v[192:207], v[52:55], v[60:63], v[192:207]\n"
      "ds_read_b64_tr_b16 v[36:37], v232 offset:38912\n"
      "ds_read_b64_tr_b16 v[38:39], v232 offset:39424\n"
      "ds_read_b128 v[44:47], v239 offset:512\n"
      "s_waitcnt lgkmcnt(6)\n"
      "v_mfma_f32_32x32x16_bf16 v[192:207], v[208:211], v[216:219], v[192:207]\n"
      "ds_read_b64_tr_b16 v[48:49], v232 offset:40960\n"
      "ds_read_b64_tr_b16 v[50:51], v232 offset:41472\n"
      "ds_read_b128 v[56:59], v240 offset:512\n"
      "v_mfma_f32_32x32x16_bf16 v[192:207], v[212:215], v[220:223], v[192:207]\n"
      "ds_read_b64_tr_b16 v[52:53], v232 offset:43008\n"
      "ds_read_b64_tr_b16 v[54:55], v232 offset:43520\n"
      "ds_read_b128 v[60:63], v241 offset:512\n"
      "s_waitcnt lgkmcnt(6)\n"
      "v_mfma_f32_32x32x16_bf16 v[192:207], v[32:35], v[40:43], v[192:207]\n"
      "ds_read_b64_tr_b16 v[208:209], v232 offset:45056\n"
      "ds_read_b64_tr_b16 v[210:211], v232 offset:45568\n"
      "ds_read_b128 v[216:219], v242 offset:512\n"
      "v_mfma_f32_32x32x16_bf16 v[192:207], v[36:39], v[44:47], v[192:207]\n"
      "ds_read_b64_tr_b16 v[212:213], v232 offset:47104\n"
      "ds_read_b64_tr_b16 v[214:215], v232 offset:47616\n"
      "ds_read_b128 v[220:223], v243 offset:512\n"
      "s_waitcnt lgkmcnt(6)\n"
      "s_mov_b32 s66, 0\n"
      "s_waitcnt vmcnt(0)\n"
      "s_branch Lpfc_88\n"
      "Lpf_87:\n"
      "s_mov_b64 exec, s[98:99]\n"
      "buffer_load_dwordx2 v[224:225], v234, s[32:35], 0 offen sc1\n"
      "s_mov_b64 exec, -1\n"
      "s_waitcnt vmcnt(0)\n"
      "Lpfc_88:\n"
      "v_and_b32 v226, 0xffff0000, v224\n"
      "v_xor_b32 v226, s70, v226\n"
      "v_xor_b32 v227, s71, v225\n"
      "v_or_b32 v226, v226, v227\n"
      "v_cmp_ne_u32 vcc, 0, v226\n"
      "s_cbranch_vccz Lpfd_89\n"
      "s_mov_b64 s[98:99], vcc\n"
      "s_sleep 2\n"
      "s_add_u32 s66, s66, 1\n"
      "s_cmp_lt_u32 s66, 131072\n"
      "s_cbranch_scc1 Lpf_87\n"
      "Lpfd_89:\n"
      "s_xor_b32 s67, s69, 0\n"
      "buffer_load_dwordx4 v[0:3], v250, s[32:35], s67 offen sc1\n"
      "buffer_load_dwordx4 v[64:67], v250, s[32:35], s67 offen offset:1024 sc1\n"
      "v_lshlrev_b32 v253, 16, v224\n"
      "v_mfma_f32_32x32x16_bf16 v[192:207], v[48:51], v[56:59], v[192:207]\n"
      "ds_read_b64_tr_b16 v[32:33], v232 offset:49152\n"
      "ds_read_b64_tr_b16 v[34:35], v232 offset:49664\n"
      "ds_read_b128 v[40:43], v236 offset:768\n"
      "v_mfma_f32_32x32x16_bf16 v[192:207], v[52:55], v[60:63], v[192:207]\n"
      "ds_read_b64_tr_b16 v[36:37], v232 offset:51200\n"
      "ds_read_b64_tr_b16 v[38:39], v232 offset:51712\n"
      "ds_read_b128 v[44:47], v237 offset:768\n"
      "s_xor_b32 s67, s69, 2048\n"
      "buffer_load_dwordx4 v[4:7], v250, s[32:35], s67 offen sc1\n"
      "s_waitcnt lgkmcnt(6)\n"
      "v_mfma_f32_32x32x16_bf16 v[192:207], v[208:211], v[216:219], v[192:207]\n"
      "ds_read_b64_tr_b16 v[48:49], v232 offset:53248\n"
      "ds_read_b64_tr_b16 v[50:51], v232 offset:53760\n"
      "ds_read_b128 v[56:59], v238 offset:768\n"
      "v_mfma_f32_32x32x16_bf16 v[192:207], v[212:215], v[220:223], v[192:207]\n"
      "ds_read_b64_tr_b16 v[52:53], v232 offset:55296\n"
      "ds_read_b64_tr_b16 v[54:55], v232 offset:55808\n"
      "ds_read_b128 v[60:63], v239 offset:768\n"
      "buffer_load_dwordx4 v[72:75], v250, s[32:35], s67 offen offset:1024 sc1\n"
      "s_waitcnt lgkmcnt(6)\n"
      "v_mfma_f32_32x32x16_bf16 v[192:207], v[32:35], v[40:43], v[192:207]\n"
      "ds_read_b64_tr_b16 v[208:209], v232 offset:57344\n"
      "ds_read_b64_tr_b16 v[210:211], v232 offset:57856\n"
      "ds_read_b128 v[216:219], v240 offset:768\n"
      "v_mfma_f32_32x32x16_bf16 v[192:207], v[36:39], v[44:47], v[192:207]\n"
      "ds_read_b64_tr_b16 v[212:213], v232 offset:59392\n"
      "ds_read_b64_tr_b16 v[214:215], v232 offset:59904\n"
      "ds_read_b128 v[220:223], v241 offset:768\n"
      "s_xor_b32 s67, s69, 4096\n"
      "buffer_load_dwordx4 v[8:11], v250, s[32:35], s67 offen sc1\n"
      "s_waitcnt lgkmcnt(6)\n"
      "v_mfma_f32_32x32x16_bf16 v[192:207], v[48:51], v[56:59], v[192:207]\n"
      "ds_read_b64_tr_b16 v[32:33], v232 offset:61440\n"
      "ds_read_b64_tr_b16 v[34:35], v232 offset:61952\n"
      "ds_read_b128 v[40:43], v242 offset:768\n"
      "v_mfma_f32_32x32x16_bf16 v[192:207], v[52:55], v[60:63], v[192:207]\n"
      "ds_read_b64_tr_b16 v[36:37], v232 offset:63488\n"
      "ds_read_b64_tr_b16 v[38:39], v232 offset:64000\n"
      "ds_read_b128 v[44:47], v243 offset:768\n"
      "buffer_load_dwordx4 v[80:83], v250, s[32:35], s67 offen offset:1024 sc1\n"
      "s_waitcnt lgkmcnt(6)\n"
      "v_mfma_f32_32x32x16_bf16 v[192:207], v[208:211], v[216:219], v[192:207]\n"
      "ds_read_b32 v228, v254 offset:16\n"
      "v_mfma_f32_32x32x16_bf16 v[192:207], v[212:215], v[220:223], v[192:207]\n"
      "s_xor_b32 s67, s69, 6144\n"
      "buffer_load_dwordx4 v[12:15], v250, s[32:35], s67 offen sc1\n"
      "s_waitcnt lgkmcnt(1)\n"
      "v_mfma_f32_32x32x16_bf16 v[192:207], v[32:35], v[40:43], v[192:207]\n"
      "v_mfma_f32_32x32x16_bf16 v[192:207], v[36:39], v[44:47], v[192:207]\n"
      "buffer_load_dwordx4 v[88:91], v250, s[32:35], s67 offen offset:1024 sc1\n"
      "s_waitcnt lgkmcnt(0)\n"
      "v_cmp_eq_u32 vcc, 0, v228\n"
      "s_cbranch_vccnz LQA\n"
      "LQA2:\n"
      "s_nop 7\n"
      "v_pk_mul_f32 v[192:193], v[192:193], s[96:97] op_sel_hi:[1,0]\n"
      "v_pk_mul_f32 v[194:195], v[194:195], s[96:97] op_sel_hi:[1,0]\n"
      "v_pk_mul_f32 v[196:197], v[196:197], s[96:97] op_sel_hi:[1,0]\n"
      "v_pk_mul_f32 v[198:199], v[198:199], s[96:97] op_sel_hi:[1,0]\n"
      "v_pk_mul_f32 v[200:201], v[200:201], s[96:97] op_sel_hi:[1,0]\n"
      "v_pk_mul_f32 v[202:203], v[202:203], s[96:97] op_sel_hi:[1,0]\n"
      "v_pk_mul_f32 v[204:205], v[204:205], s[96:97] op_sel_hi:[1,0]\n"
      "v_pk_mul_f32 v[206:207], v[206:207], s[96:97] op_sel_hi:[1,0]\n"
      "v_cvt_pk_bf16_f32 v208, v192, v193\n"
      "v_cvt_pk_bf16_f32 v209, v194, v195\n"
      "v_cvt_pk_bf16_f32 v210, v196, v197\n"
      "v_cvt_pk_bf16_f32 v211, v198, v199\n"
      "v_cvt_pk_bf16_f32 v212, v200, v201\n"
      "v_cvt_pk_bf16_f32 v213, v202, v203\n"
      "v_cvt_pk_bf16_f32 v214, v204, v205\n"
      "v_cvt_pk_bf16_f32 v215, v206, v207\n"
      "ds_write_b64 v235, v[208:209] offset:0\n"
      "ds_write_b64 v235, v[210:211] offset:512\n"
      "ds_write_b64 v235, v[212:213] offset:256\n"
      "ds_write_b64 v235, v[214:215] offset:768\n"
      "v_dot2_f32_bf16 v216, v208, v208, 0\n"
      "v_dot2_f32_bf16 v217, v212, v212, 0\n"
      "v_dot2_f32_bf16 v216, v209, v209, v216\n"
      "v_dot2_f32_bf16 v217, v213, v213, v217\n"
      "v_dot2_f32_bf16 v216, v210, v210, v216\n"
      "v_dot2_f32_bf16 v217, v214, v214, v217\n"
      "v_dot2_f32_bf16 v216, v211, v211, v216\n"
      "v_dot2_f32_bf16 v217, v215, v215, v217\n"
      "v_mov_b64 v[238:239], 0\n"
      "v_mov_b64 v[236:237], 0\n"
      "s_nop 0\n"
      "v_permlane32_swap_b32 v216, v217\n"
      "v_add_f32 v216, v216, v217\n"
      "v_mov_b64 v[234:235], 0\n"
      "v_mov_b64 v[232:233], 0\n"
      "v_max_u32_dpp v216, v216, v216 row_ror:8 row_mask:0xf bank_mask:0xf\n"
      "v_mov_b64 v[230:231], 0\n"
      "v_mov_b64 v[228:229], 0\n"
      "v_max_u32_dpp v216, v216, v216 row_ror:4 row_mask:0xf bank_mask:0xf\n"
      "s_mov_b32 exec_lo, 0x000f000f\n"
      "s_mov_b32 exec_hi, 0x000f000f\n"
      "ds_max_u32 v251, v216 offset:100\n"
      "s_mov_b64 exec, -1\n"
      "s_xor_b32 s67, s69, 8192\n"
      "buffer_load_dwordx4 v[16:19], v250, s[32:35], s67 offen sc1\n"
      "buffer_load_dwordx4 v[96:99], v250, s[32:35], s67 offen offset:1024 sc1\n"
      "s_xor_b32 s67, s69, 10240\n"
      "buffer_load_dwordx4 v[20:23], v250, s[32:35], s67 offen sc1\n"
      "buffer_load_dwordx4 v[104:107], v250, s[32:35], s67 offen offset:1024 sc1\n"
      "s_waitcnt lgkmcnt(0)\n"
      "s_barrier\n"
      "ds_read_b32 v192, v251 offset:100\n"
      "ds_read_b128 v[240:243], v247\n"
      "v_mov_b64 v[226:227], 0\n"
      "v_mov_b64 v[224:225], 0\n"
      "s_mov_b32 vcc_lo, 0xf0f0f0f0\n"
      "s_mov_b32 vcc_hi, 0xf0f0f0f0\n"
      "v_mov_b32 v193, 0x13610000\n"
      "s_waitcnt lgkmcnt(1)\n"
      "v_cndmask_b32 v192, v192, v193, vcc\n"
      "v_mul_f32 v192, v192, v253\n"
      "v_cmp_nge_f32 vcc, 0x45610000, v192\n"
      "v_and_b32 v251, -9, v254\n"
      "s_cbranch_vccnz LSLOW\n"
      "s_waitcnt lgkmcnt(0)\n"
      "s_waitcnt vmcnt(10)\n"
      "v_mfma_f32_32x32x16_bf16 v[192:207], v[0:3], v[240:243], -4.0\n"
      "v_permlane16_swap_b32 v64, v68\n"
      "v_permlane16_swap_b32 v65, v69\n"
      "v_permlane16_swap_b32 v66, v70\n"
      "v_permlane16_swap_b32 v67, v71\n"
      "s_nop 7\n"
      "v_exp_f32 v192, v192\n"
      "v_exp_f32 v193, v193\n"
      "v_exp_f32 v194, v194\n"
      "v_exp_f32 v195, v195\n"
      "v_exp_f32 v196, v196\n"
      "s_waitcnt vmcnt(8)\n"
      "v_mfma_f32_32x32x16_bf16 v[208:223], v[4:7], v[240:243], -4.0\n"
      "v_permlane16_swap_b32 v72, v76\n"
      "v_permlane16_swap_b32 v73, v77\n"
      "v_permlane16_swap_b32 v74, v78\n"
      "v_permlane16_swap_b32 v75, v79\n"
      "s_xor_b32 s67, s69, 12288\n"
      "buffer_load_dwordx4 v[24:27], v250, s[32:35], s67 offen sc1\n"
      "buffer_load_dwordx4 v[112:115], v250, s[32:35], s67 offen offset:1024 sc1\n"
      "v_exp_f32 v197, v197\n"
      "v_exp_f32 v198, v198\n"
      "v_exp_f32 v199, v199\n"
      "v_exp_f32 v200, v200\n"
      "v_exp_f32 v201, v201\n"
      "v_smfmac_f32_32x32x32_bf16 v[224:239], v[64:67], v[192:199], v249\n"
      "v_exp_f32 v202, v202\n"
      "v_exp_f32 v203, v203\n"
      "v_exp_f32 v204, v204\n"
      "v_exp_f32 v205, v205\n"
      "v_exp_f32 v206, v206\n"
      "v_exp_f32 v207, v207\n"
      "v_exp_f32 v208, v208\n"
      "v_exp_f32 v209, v209\n"
      "v_smfmac_f32_32x32x32_bf16 v[224:239], v[68:71], v[200:207], v249\n"
      "v_exp_f32 v210, v210\n"
      "v_exp_f32 v211, v211\n"
      "v_exp_f32 v212, v212\n"
      "s_waitcnt vmcnt(8)\n"
      "v_mfma_f32_32x32x16_bf16 v[192:207], v[8:11], v[240:243], -4.0\n"
      "v_permlane16_swap_b32 v80, v84\n"
      "v_permlane16_swap_b32 v81, v85\n"
      "v_permlane16_swap_b32 v82, v86\n"
      "v_permlane16_swap_b32 v83, v87\n"
      "s_xor_b32 s67, s69, 14336\n"
      "buffer_load_dwordx4 v[28:31], v250, s[32:35], s67 offen sc1\n"
      "buffer_load_dwordx4 v[120:123], v250, s[32:35], s67 offen offset:1024 sc1\n"
      "v_exp_f32 v213, v213\n"
      "v_exp_f32 v214, v214\n"
      "v_exp_f32 v215, v215\n"
      "v_exp_f32 v216, v216\n"
      "v_exp_f32 v217, v217\n"
      "v_smfmac_f32_32x32x32_bf16 v[224:239], v[72:75], v[208:215], v249\n"
      "v_exp_f32 v218, v218\n"
      "v_exp_f32 v219, v219\n"
      "v_exp_f32 v220, v220\n"
      "v_exp_f32 v221, v221\n"
      "v_exp_f32 v222, v222\n"
      "v_exp_f32 v223, v223\n"
      "v_exp_f32 v192, v192\n"
      "v_exp_f32 v193, v193\n"
      "v_smfmac_f32_32x32x32_bf16 v[224:239], v[76:79], v[216:223], v249\n"
      "v_exp_f32 v194, v194\n"
      "v_exp_f32 v195, v195\n"
      "v_exp_f32 v196, v196\n"
      "s_waitcnt vmcnt(8)\n"
      "v_mfma_f32_32x32x16_bf16 v[208:223], v[12:15], v[240:243], -4.0\n"
      "v_permlane16_swap_b32 v88, v92\n"
      "v_permlane16_swap_b32 v89, v93\n"
      "v_permlane16_swap_b32 v90, v94\n"
      "v_permlane16_swap_b32 v91, v95\n"
      "s_xor_b32 s67, s69, 16384\n"
      "buffer_load_dwordx4 v[32:35], v250, s[32:35], s67 offen sc1\n"
      "buffer_load_dwordx4 v[128:131], v250, s[32:35], s67 offen offset:1024 sc1\n"
      "v_exp_f32 v197, v197\n"
      "v_exp_f32 v198, v198\n"
      "v_exp_f32 v199, v199\n"
      "v_exp_f32 v200, v200\n"
      "v_exp_f32 v201, v201\n"
      "v_smfmac_f32_32x32x32_bf16 v[224:239], v[80:83], v[192:199], v249\n"
      "v_exp_f32 v202, v202\n"
      "v_exp_f32 v203, v203\n"
      "v_exp_f32 v204, v204\n"
      "v_exp_f32 v205, v205\n"
      "v_exp_f32 v206, v206\n"
      "v_exp_f32 v207, v207\n"
      "v_exp_f32 v208, v208\n"
      "v_exp_f32 v209, v209\n"
      "v_smfmac_f32_32x32x32_bf16 v[224:239], v[84:87], v[200:207], v249\n"
      "v_exp_f32 v210, v210\n"
      "v_exp_f32 v211, v211\n"
      "v_exp_f32 v212, v212\n"
      "s_waitcnt vmcnt(8)\n"
      "v_mfma_f32_32x32x16_bf16 v[192:207], v[16:19], v[240:243], -4.0\n"
      "v_permlane16_swap_b32 v96, v100\n"
      "v_permlane16_swap_b32 v97, v101\n"
      "v_permlane16_swap_b32 v98, v102\n"
      "v_permlane16_swap_b32 v99, v103\n"
      "s_xor_b32 s67, s69, 18432\n"
      "buffer_load_dwordx4 v[36:39], v250, s[32:35], s67 offen sc1\n"
      "buffer_load_dwordx4 v[136:139], v250, s[32:35], s67 offen offset:1024 sc1\n"
      "v_exp_f32 v213, v213\n"
      "v_exp_f32 v214, v214\n"
      "v_exp_f32 v215, v215\n"
      "v_exp_f32 v216, v216\n"
      "v_exp_f32 v217, v217\n"
      "v_smfmac_f32_32x32x32_bf16 v[224:239], v[88:91], v[208:215], v249\n"
      "v_exp_f32 v218, v218\n"
      "v_exp_f32 v219, v219\n"
      "v_exp_f32 v220, v220\n"
      "v_exp_f32 v221, v221\n"
      "v_exp_f32 v222, v222\n"
      "v_exp_f32 v223, v223\n"
      "v_exp_f32 v192, v192\n"
      "v_exp_f32 v193, v193\n"
      "v_smfmac_f32_32x32x32_bf16 v[224:239], v[92:95], v[216:223], v249\n"
      "v_exp_f32 v194, v194\n"
      "v_exp_f32 v195, v195\n"
      "v_exp_f32 v196, v196\n"
      "s_waitcnt vmcnt(8)\n"
      "v_mfma_f32_32x32x16_bf16 v[208:223], v[20:23], v[240:243], -4.0\n"
      "v_permlane16_swap_b32 v104, v108\n"
      "v_permlane16_swap_b32 v105, v109\n"
      "v_permlane16_swap_b32 v106, v110\n"
      "v_permlane16_swap_b32 v107, v111\n"
      "s_xor_b32 s67, s69, 20480\n"
      "buffer_load_dwordx4 v[40:43], v250, s[32:35], s67 offen sc1\n"
      "buffer_load_dwordx4 v[144:147], v250, s[32:35], s67 offen offset:1024 sc1\n"
      "v_exp_f32 v197, v197\n"
      "v_exp_f32 v198, v198\n"
      "v_exp_f32 v199, v199\n"
      "v_exp_f32 v200, v200\n"
      "v_exp_f32 v201, v201\n"
      "v_smfmac_f32_32x32x32_bf16 v[224:239], v[96:99], v[192:199], v249\n"
      "v_exp_f32 v202, v202\n"
      "v_exp_f32 v203, v203\n"
      "v_exp_f32 v204, v204\n"
      "v_exp_f32 v205, v205\n"
      "v_exp_f32 v206, v206\n"
      "v_exp_f32 v207, v207\n"
      "v_exp_f32 v208, v208\n"
      "v_exp_f32 v209, v209\n"
      "v_smfmac_f32_32x32x32_bf16 v[224:239], v[100:103], v[200:207], v249\n"
      "v_exp_f32 v210, v210\n"
      "v_exp_f32 v211, v211\n"
      "v_exp_f32 v212, v212\n"
      "s_waitcnt vmcnt(8)\n"
      "v_mfma_f32_32x32x16_bf16 v[192:207], v[24:27], v[240:243], -4.0\n"
      "v_permlane16_swap_b32 v112, v116\n"
      "v_permlane16_swap_b32 v113, v117\n"
      "v_permlane16_swap_b32 v114, v118\n"
      "v_permlane16_swap_b32 v115, v119\n"
      "s_xor_b32 s67, s69, 22528\n"
      "buffer_load_dwordx4 v[44:47], v250, s[32:35], s67 offen sc1\n"
      "buffer_load_dwordx4 v[152:155], v250, s[32:35], s67 offen offset:1024 sc1\n"
      "v_exp_f32 v213, v213\n"
      "v_exp_f32 v214, v214\n"
      "v_exp_f32 v215, v215\n"
      "v_exp_f32 v216, v216\n"
      "v_exp_f32 v217, v217\n"
      "v_smfmac_f32_32x32x32_bf16 v[224:239], v[104:107], v[208:215], v249\n"
      "v_exp_f32 v218, v218\n"
      "v_exp_f32 v219, v219\n"
      "v_exp_f32 v220, v220\n"
      "v_exp_f32 v221, v221\n"
      "v_exp_f32 v222, v222\n"
      "v_exp_f32 v223, v223\n"
      "v_exp_f32 v192, v192\n"
      "v_exp_f32 v193, v193\n"
      "v_smfmac_f32_32x32x32_bf16 v[224:239], v[108:111], v[216:223], v249\n"
      "v_exp_f32 v194, v194\n"
      "v_exp_f32 v195, v195\n"
      "v_exp_f32 v196, v196\n"
      "s_waitcnt vmcnt(8)\n"
      "v_mfma_f32_32x32x16_bf16 v[208:223], v[28:31], v[240:243], -4.0\n"
      "v_permlane16_swap_b32 v120, v124\n"
      "v_permlane16_swap_b32 v121, v125\n"
      "v_permlane16_swap_b32 v122, v126\n"
      "v_permlane16_swap_b32 v123, v127\n"
      "s_xor_b32 s67, s69, 24576\n"
      "buffer_load_dwordx4 v[48:51], v250, s[32:35], s67 offen sc1\n"
      "buffer_load_dwordx4 v[160:163], v250, s[32:35], s67 offen offset:1024 sc1\n"
      "v_exp_f32 v197, v197\n"
      "v_exp_f32 v198, v198\n"
      "v_exp_f32 v199, v199\n"
      "v_exp_f32 v200, v200\n"
      "v_exp_f32 v201, v201\n"
      "v_smfmac_f32_32x32x32_bf16 v[224:239], v[112:115], v[192:199], v249\n"
      "v_exp_f32 v202, v202\n"
      "v_exp_f32 v203, v203\n"
      "v_exp_f32 v204, v204\n"
      "v_exp_f32 v205, v205\n"
      "v_exp_f32 v206, v206\n"
      "v_exp_f32 v207, v207\n"
      "v_exp_f32 v208, v208\n"
      "v_exp_f32 v209, v209\n"
      "v_smfmac_f32_32x32x32_bf16 v[224:239], v[116:119], v[200:207], v249\n"
      "v_exp_f32 v210, v210\n"
      "v_exp_f32 v211, v211\n"
      "v_exp_f32 v212, v212\n"
      "s_waitcnt vmcnt(8)\n"
      "v_mfma_f32_32x32x16_bf16 v[192:207], v[32:35], v[240:243], -4.0\n"
      "v_permlane16_swap_b32 v128, v132\n"
      "v_permlane16_swap_b32 v129, v133\n"
      "v_permlane16_swap_b32 v130, v134\n"
      "v_permlane16_swap_b32 v131, v135\n"
      "s_xor_b32 s67, s69, 26624\n"
      "buffer_load_dwordx4 v[52:55], v250, s[32:35], s67 offen sc1\n"
      "buffer_load_dwordx4 v[168:171], v250, s[32:35], s67 offen offset:1024 sc1\n"
      "v_exp_f32 v213, v213\n"
      "v_exp_f32 v214, v214\n"
      "v_exp_f32 v215, v215\n"
      "v_exp_f32 v216, v216\n"
      "v_exp_f32 v217, v217\n"
      "v_smfmac_f32_32x32x32_bf16 v[224:239], v[120:123], v[208:215], v249\n"
      "v_exp_f32 v218, v218\n"
      "v_exp_f32 v219, v219\n"
      "v_exp_f32 v220, v220\n"
      "v_exp_f32 v221, v221\n"
      "v_exp_f32 v222, v222\n"
      "v_exp_f32 v223, v223\n"
      "v_exp_f32 v192, v192\n"
      "v_exp_f32 v193, v193\n"
      "v_smfmac_f32_32x32x32_bf16 v[224:239], v[124:127], v[216:223], v249\n"
      "v_exp_f32 v194, v194\n"
      "v_exp_f32 v195, v195\n"
      "v_exp_f32 v196, v196\n"
      "s_waitcnt vmcnt(8)\n"
      "v_mfma_f32_32x32x16_bf16 v[208:223], v[36:39], v[240:243], -4.0\n"
      "v_permlane16_swap_b32 v136, v140\n"
      "v_permlane16_swap_b32 v137, v141\n"
      "v_permlane16_swap_b32 v138, v142\n"
      "v_permlane16_swap_b32 v139, v143\n"
      "s_xor_b32 s67, s69, 28672\n"
      "buffer_load_dwordx4 v[56:59], v250, s[32:35], s67 offen sc1\n"
      "buffer_load_dwordx4 v[176:179], v250, s[32:35], s67 offen offset:1024 sc1\n"
      "v_exp_f32 v197, v197\n"
      "v_exp_f32 v198, v198\n"
      "v_exp_f32 v199, v199\n"
      "v_exp_f32 v200, v200\n"
      "v_exp_f32 v201, v201\n"
      "v_smfmac_f32_32x32x32_bf16 v[224:239], v[128:131], v[192:199], v249\n"
      "v_exp_f32 v202, v202\n"
      "v_exp_f32 v203, v203\n"
      "v_exp_f32 v204, v204\n"
      "v_exp_f32 v205, v205\n"
      "v_exp_f32 v206, v206\n"
      "v_exp_f32 v207, v207\n"
      "v_exp_f32 v208, v208\n"
      "v_exp_f32 v209, v209\n"
      "v_smfmac_f32_32x32x32_bf16 v[224:239], v[132:135], v[200:207], v249\n"
      "v_exp_f32 v210, v210\n"
      "v_exp_f32 v211, v211\n"
      "v_exp_f32 v212, v212\n"
      "s_waitcnt vmcnt(8)\n"
      "v_mfma_f32_32x32x16_bf16 v[192:207], v[40:43], v[240:243], -4.0\n"
      "v_permlane16_swap_b32 v144, v148\n"
      "v_permlane16_swap_b32 v145, v149\n"
      "v_permlane16_swap_b32 v146, v150\n"
      "v_permlane16_swap_b32 v147, v151\n"
      "s_xor_b32 s67, s69, 30720\n"
      "buffer_load_dwordx4 v[60:63], v250, s[32:35], s67 offen sc1\n"
      "buffer_load_dwordx4 v[184:187], v250, s[32:35], s67 offen offset:1024 sc1\n"
      "v_exp_f32 v213, v213\n"
      "v_exp_f32 v214, v214\n"
      "v_exp_f32 v215, v215\n"
      "v_exp_f32 v216, v216\n"
      "v_exp_f32 v217, v217\n"
      "v_smfmac_f32_32x32x32_bf16 v[224:239], v[136:139], v[208:215], v249\n"
      "v_exp_f32 v218, v218\n"
      "v_exp_f32 v219, v219\n"
      "v_exp_f32 v220, v220\n"
      "v_exp_f32 v221, v221\n"
      "v_exp_f32 v222, v222\n"
      "v_exp_f32 v223, v223\n"
      "v_exp_f32 v192, v192\n"
      "v_exp_f32 v193, v193\n"
      "v_smfmac_f32_32x32x32_bf16 v[224:239], v[140:143], v[216:223], v249\n"
      "v_exp_f32 v194, v194\n"
      "v_exp_f32 v195, v195\n"
      "v_exp_f32 v196, v196\n"
      "s_waitcnt vmcnt(8)\n"
      "v_mfma_f32_32x32x16_bf16 v[208:223], v[44:47], v[240:243], -4.0\n"
      "v_permlane16_swap_b32 v152, v156\n"
      "v_permlane16_swap_b32 v153, v157\n"
      "v_permlane16_swap_b32 v154, v158\n"
      "v_permlane16_swap_b32 v155, v159\n"
      "v_exp_f32 v197, v197\n"
      "v_exp_f32 v198, v198\n"
      "v_exp_f32 v199, v199\n"
      "v_exp_f32 v200, v200\n"
      "v_exp_f32 v201, v201\n"
      "v_smfmac_f32_32x32x32_bf16 v[224:239], v[144:147], v[192:199], v249\n"
      "v_exp_f32 v202, v202\n"
      "v_exp_f32 v203, v203\n"
      "v_exp_f32 v204, v204\n"
      "v_exp_f32 v205, v205\n"
      "v_exp_f32 v206, v206\n"
      "v_exp_f32 v207, v207\n"
      "v_exp_f32 v208, v208\n"
      "v_exp_f32 v209, v209\n"
      "v_smfmac_f32_32x32x32_bf16 v[224:239], v[148:151], v[200:207], v249\n"
      "v_exp_f32 v210, v210\n"
      "v_exp_f32 v211, v211\n"
      "v_exp_f32 v212, v212\n"
      "s_waitcnt vmcnt(6)\n"
      "v_mfma_f32_32x32x16_bf16 v[192:207], v[48:51], v[240:243], -4.0\n"
      "v_permlane16_swap_b32 v160, v164\n"
      "v_permlane16_swap_b32 v161, v165\n"
      "v_permlane16_swap_b32 v162, v166\n"
      "v_permlane16_swap_b32 v163, v167\n"
      "v_exp_f32 v213, v213\n"
      "v_exp_f32 v214, v214\n"
      "v_exp_f32 v215, v215\n"
      "v_exp_f32 v216, v216\n"
      "v_exp_f32 v217, v217\n"
      "v_smfmac_f32_32x32x32_bf16 v[224:239], v[152:155], v[208:215], v249\n"
      "v_exp_f32 v218, v218\n"
      "v_exp_f32 v219, v219\n"
      "v_exp_f32 v220, v220\n"
      "v_exp_f32 v221, v221\n"
      "v_exp_f32 v222, v222\n"
      "v_exp_f32 v223, v223\n"
      "v_exp_f32 v192, v192\n"
      "v_exp_f32 v193, v193\n"
      "v_smfmac_f32_32x32x32_bf16 v[224:239], v[156:159], v[216:223], v249\n"
      "v_exp_f32 v194, v194\n"
      "v_exp_f32 v195, v195\n"
      "v_exp_f32 v196, v196\n"
      "s_waitcnt vmcnt(4)\n"
      "v_mfma_f32_32x32x16_bf16 v[208:223], v[52:55], v[240:243], -4.0\n"
      "v_permlane16_swap_b32 v168, v172\n"
      "v_permlane16_swap_b32 v169, v173\n"
      "v_permlane16_swap_b32 v170, v174\n"
      "v_permlane16_swap_b32 v171, v175\n"
      "v_exp_f32 v197, v197\n"
      "v_exp_f32 v198, v198\n"
      "v_exp_f32 v199, v199\n"
      "v_exp_f32 v200, v200\n"
      "v_exp_f32 v201, v201\n"
      "v_smfmac_f32_32x32x32_bf16 v[224:239], v[160:163], v[192:199], v249\n"
      "v_exp_f32 v202, v202\n"
      "v_exp_f32 v203, v203\n"
      "v_exp_f32 v204, v204\n"
      "v_exp_f32 v205, v205\n"
      "v_exp_f32 v206, v206\n"
      "v_exp_f32 v207, v207\n"
      "v_exp_f32 v208, v208\n"
      "v_exp_f32 v209, v209\n"
      "v_smfmac_f32_32x32x32_bf16 v[224:239], v[164:167], v[200:207], v249\n"
      "v_exp_f32 v210, v210\n"
      "v_exp_f32 v211, v211\n"
      "v_exp_f32 v212, v212\n"
      "s_waitcnt vmcnt(2)\n"
      "v_mfma_f32_32x32x16_bf16 v[192:207], v[56:59], v[240:243], -4.0\n"
      "v_permlane16_swap_b32 v176, v180\n"
      "v_permlane16_swap_b32 v177, v181\n"
      "v_permlane16_swap_b32 v178, v182\n"
      "v_permlane16_swap_b32 v179, v183\n"
      "v_exp_f32 v213, v213\n"
      "v_exp_f32 v214, v214\n"
      "v_exp_f32 v215, v215\n"
      "v_exp_f32 v216, v216\n"
      "v_exp_f32 v217, v217\n"
      "v_smfmac_f32_32x32x32_bf16 v[224:239], v[168:171], v[208:215], v249\n"
      "v_exp_f32 v218, v218\n"
      "v_exp_f32 v219, v219\n"
      "v_exp_f32 v220, v220\n"
      "v_exp_f32 v221, v221\n"
      "v_exp_f32 v222, v222\n"
      "v_exp_f32 v223, v223\n"
      "v_exp_f32 v192, v192\n"
      "v_exp_f32 v193, v193\n"
      "v_smfmac_f32_32x32x32_bf16 v[224:239], v[172:175], v[216:223], v249\n"
      "v_exp_f32 v194, v194\n"
      "v_exp_f32 v195, v195\n"
      "v_exp_f32 v196, v196\n"
      "s_waitcnt vmcnt(0)\n"
      "v_mfma_f32_32x32x16_bf16 v[208:223], v[60:63], v[240:243], -4.0\n"
      "v_permlane16_swap_b32 v184, v188\n"
      "v_permlane16_swap_b32 v185, v189\n"
      "v_permlane16_swap_b32 v186, v190\n"
      "v_permlane16_swap_b32 v187, v191\n"
      "v_exp_f32 v197, v197\n"
      "ds_read_b128 v[240:243], v247 offset:1024\n"
      "v_exp_f32 v198, v198\n"
      "v_exp_f32 v199, v199\n"
      "v_exp_f32 v200, v200\n"
      "v_exp_f32 v201, v201\n"
      "v_smfmac_f32_32x32x32_bf16 v[224:239], v[176:179], v[192:199], v249\n"
      "v_exp_f32 v202, v202\n"
      "v_exp_f32 v203, v203\n"
      "v_exp_f32 v204, v204\n"
      "v_exp_f32 v205, v205\n"
      "v_exp_f32 v206, v206\n"
      "v_exp_f32 v207, v207\n"
      "v_exp_f32 v208, v208\n"
      "v_exp_f32 v209, v209\n"
      "v_smfmac_f32_32x32x32_bf16 v[224:239], v[180:183], v[200:207], v249\n"
      "v_exp_f32 v210, v210\n"
      "v_exp_f32 v211, v211\n"
      "v_exp_f32 v212, v212\n"
      "s_waitcnt lgkmcnt(0)\n"
      "v_mfma_f32_32x32x16_bf16 v[192:207], v[0:3], v[240:243], -4.0\n"
      "v_exp_f32 v213, v213\n"
      "v_exp_f32 v214, v214\n"
      "v_exp_f32 v215, v215\n"
      "v_exp_f32 v216, v216\n"
      "v_exp_f32 v217, v217\n"
      "v_smfmac_f32_32x32x32_bf16 v[224:239], v[184:187], v[208:215], v249\n"
      "v_exp_f32 v218, v218\n"
      "v_exp_f32 v219, v219\n"
      "v_exp_f32 v220, v220\n"
      "v_exp_f32 v221, v221\n"
      "v_exp_f32 v222, v222\n"
      "v_exp_f32 v223, v223\n"
      "v_exp_f32 v192, v192\n"
      "v_exp_f32 v193, v193\n"
      "v_smfmac_f32_32x32x32_bf16 v[224:239], v[188:191], v[216:223], v249\n"
      "v_exp_f32 v194, v194\n"
      "v_exp_f32 v195, v195\n"
      "v_exp_f32 v196, v196\n"
      "v_mfma_f32_32x32x16_bf16 v[208:223], v[4:7], v[240:243], -4.0\n"
      "v_exp_f32 v197, v197\n"
      "v_exp_f32 v198, v198\n"
      "v_exp_f32 v199, v199\n"
      "v_exp_f32 v200, v200\n"
      "v_exp_f32 v201, v201\n"
      "v_exp_f32 v202, v202\n"
      "v_exp_f32 v203, v203\n"
      "v_exp_f32 v204, v204\n"
      "ds_write_addtid_b32 v224 offset:16384\n"
      "ds_write_addtid_b32 v225 offset:16640\n"
      "ds_write_addtid_b32 v226 offset:16896\n"
      "ds_write_addtid_b32 v227 offset:17152\n"
      "ds_write_addtid_b32 v228 offset:17408\n"
      "ds_write_addtid_b32 v229 offset:17664\n"
      "ds_write_addtid_b32 v230 offset:17920\n"
      "ds_write_addtid_b32 v231 offset:18176\n"
      "ds_write_addtid_b32 v232 offset:18432\n"
      "ds_write_b32 v248, v252 offset:256\n"
      "v_mov_b64 v[224:225], 0\n"
      "v_mov_b64 v[226:227], 0\n"
      "v_mov_b64 v[228:229], 0\n"
      "v_mov_b64 v[230:231], 0\n"
      "v_mov_b64 v[232:233], 0\n"
      "v_exp_f32 v205, v205\n"
      "v_exp_f32 v206, v206\n"
      "v_smfmac_f32_32x32x32_bf16 v[224:239], v[64:67], v[192:199], v249\n"
      "v_exp_f32 v207, v207\n"
      "v_exp_f32 v208, v208\n"
      "v_exp_f32 v209, v209\n"
      "v_smfmac_f32_32x32x32_bf16 v[224:239], v[68:71], v[200:207], v249\n"
      "v_exp_f32 v210, v210\n"
      "v_exp_f32 v211, v211\n"
      "v_exp_f32 v212, v212\n"
      "v_mfma_f32_32x32x16_bf16 v[192:207], v[8:11], v[240:243], -4.0\n"
      "v_exp_f32 v213, v213\n"
      "v_exp_f32 v214, v214\n"
      "v_exp_f32 v215, v215\n"
      "v_exp_f32 v216, v216\n"
      "v_exp_f32 v217, v217\n"
      "v_smfmac_f32_32x32x32_bf16 v[224:239], v[72:75], v[208:215], v249\n"
      "v_exp_f32 v218, v218\n"
      "v_exp_f32 v219, v219\n"
      "v_exp_f32 v220, v220\n"
      "v_exp_f32 v221, v221\n"
      "v_exp_f32 v222, v222\n"
      "v_exp_f32 v223, v223\n"
      "v_exp_f32 v192, v192\n"
      "v_exp_f32 v193, v193\n"
      "v_smfmac_f32_32x32x32_bf16 v[224:239], v[76:79], v[216:223], v249\n"
      "v_exp_f32 v194, v194\n"
      "v_exp_f32 v195, v195\n"
      "v_exp_f32 v196, v196\n"
      "v_mfma_f32_32x32x16_bf16 v[208:223], v[12:15], v[240:243], -4.0\n"
      "v_exp_f32 v197, v197\n"
      "v_exp_f32 v198, v198\n"
      "v_exp_f32 v199, v199\n"
      "v_exp_f32 v200, v200\n"
      "v_exp_f32 v201, v201\n"
      "v_smfmac_f32_32x32x32_bf16 v[224:239], v[80:83], v[192:199], v249\n"
      "v_exp_f32 v202, v202\n"
      "v_exp_f32 v203, v203\n"
      "v_exp_f32 v204, v204\n"
      "v_exp_f32 v205, v205\n"
      "v_exp_f32 v206, v206\n"
      "v_exp_f32 v207, v207\n"
      "v_exp_f32 v208, v208\n"
      "v_exp_f32 v209, v209\n"
      "v_smfmac_f32_32x32x32_bf16 v[224:239], v[84:87], v[200:207], v249\n"
      "v_exp_f32 v210, v210\n"
      "v_exp_f32 v211, v211\n"
      "v_exp_f32 v212, v212\n"
      "v_mfma_f32_32x32x16_bf16 v[192:207], v[16:19], v[240:243], -4.0\n"
      "v_exp_f32 v213, v213\n"
      "v_exp_f32 v214, v214\n"
      "v_exp_f32 v215, v215\n"
      "v_exp_f32 v216, v216\n"
      "v_exp_f32 v217, v217\n"
      "v_smfmac_f32_32x32x32_bf16 v[224:239], v[88:91], v[208:215], v249\n"
      "v_exp_f32 v218, v218\n"
      "v_exp_f32 v219, v219\n"
      "v_exp_f32 v220, v220\n"
      "v_exp_f32 v221, v221\n"
      "v_exp_f32 v222, v222\n"
      "v_exp_f32 v223, v223\n"
      "v_exp_f32 v192, v192\n"
      "v_exp_f32 v193, v193\n"
      "v_smfmac_f32_32x32x32_bf16 v[224:239], v[92:95], v[216:223], v249\n"
      "v_exp_f32 v194, v194\n"
      "v_exp_f32 v195, v195\n"
      "v_exp_f32 v196, v196\n"
      "v_mfma_f32_32x32x16_bf16 v[208:223], v[20:23], v[240:243], -4.0\n"
      "v_exp_f32 v197, v197\n"
      "v_exp_f32 v198, v198\n"
      "v_exp_f32 v199, v199\n"
      "v_exp_f32 v200, v200\n"
      "v_exp_f32 v201, v201\n"
      "v_smfmac_f32_32x32x32_bf16 v[224:239], v[96:99], v[192:199], v249\n"
      "v_exp_f32 v202, v202\n"
      "v_exp_f32 v203, v203\n"
      "v_exp_f32 v204, v204\n"
      "v_exp_f32 v205, v205\n"
      "v_exp_f32 v206, v206\n"
      "v_exp_f32 v207, v207\n"
      "v_exp_f32 v208, v208\n"
      "v_exp_f32 v209, v209\n"
      "v_smfmac_f32_32x32x32_bf16 v[224:239], v[100:103], v[200:207], v249\n"
      "v_exp_f32 v210, v210\n"
      "v_exp_f32 v211, v211\n"
      "v_exp_f32 v212, v212\n"
      "v_mfma_f32_32x32x16_bf16 v[192:207], v[24:27], v[240:243], -4.0\n"
      "v_exp_f32 v213, v213\n"
      "v_exp_f32 v214, v214\n"
      "v_exp_f32 v215, v215\n"
      "v_exp_f32 v216, v216\n"
      "v_exp_f32 v217, v217\n"
      "v_smfmac_f32_32x32x32_bf16 v[224:239], v[104:107], v[208:215], v249\n"
      "v_exp_f32 v218, v218\n"
      "v_exp_f32 v219, v219\n"
      "v_exp_f32 v220, v220\n"
      "v_exp_f32 v221, v221\n"
      "v_exp_f32 v222, v222\n"
      "v_exp_f32 v223, v223\n"
      "v_exp_f32 v192, v192\n"
      "v_exp_f32 v193, v193\n"
      "v_smfmac_f32_32x32x32_bf16 v[224:239], v[108:111], v[216:223], v249\n"
      "v_exp_f32 v194, v194\n"
      "v_exp_f32 v195, v195\n"
      "v_exp_f32 v196, v196\n"
      "v_mfma_f32_32x32x16_bf16 v[208:223], v[28:31], v[240:243], -4.0\n"
      "v_exp_f32 v197, v197\n"
      "v_exp_f32 v198, v198\n"
      "v_exp_f32 v199, v199\n"
      "v_exp_f32 v200, v200\n"
      "v_exp_f32 v201, v201\n"
      "v_smfmac_f32_32x32x32_bf16 v[224:239], v[112:115], v[192:199], v249\n"
      "v_exp_f32 v202, v202\n"
      "v_exp_f32 v203, v203\n"
      "v_exp_f32 v204, v204\n"
      "v_exp_f32 v205, v205\n"
      "v_exp_f32 v206, v206\n"
      "v_exp_f32 v207, v207\n"
      "v_exp_f32 v208, v208\n"
      "v_exp_f32 v209, v209\n"
      "v_smfmac_f32_32x32x32_bf16 v[224:239], v[116:119], v[200:207], v249\n"
      "v_exp_f32 v210, v210\n"
      "v_exp_f32 v211, v211\n"
      "v_exp_f32 v212, v212\n"
      "v_mfma_f32_32x32x16_bf16 v[192:207], v[32:35], v[240:243], -4.0\n"
      "v_exp_f32 v213, v213\n"
      "v_exp_f32 v214, v214\n"
      "v_exp_f32 v215, v215\n"
      "v_exp_f32 v216, v216\n"
      "v_exp_f32 v217, v217\n"
      "v_smfmac_f32_32x32x32_bf16 v[224:239], v[120:123], v[208:215], v249\n"
      "v_exp_f32 v218, v218\n"
      "v_exp_f32 v219, v219\n"
      "v_exp_f32 v220, v220\n"
      "v_exp_f32 v221, v221\n"
      "v_exp_f32 v222, v222\n"
      "v_exp_f32 v223, v223\n"
      "v_exp_f32 v192, v192\n"
      "v_exp_f32 v193, v193\n"
      "v_smfmac_f32_32x32x32_bf16 v[224:239], v[124:127], v[216:223], v249\n"
      "v_exp_f32 v194, v194\n"
      "v_exp_f32 v195, v195\n"
      "v_exp_f32 v196, v196\n"
      "v_mfma_f32_32x32x16_bf16 v[208:223], v[36:39], v[240:243], -4.0\n"
      "v_exp_f32 v197, v197\n"
      "v_exp_f32 v198, v198\n"
      "v_exp_f32 v199, v199\n"
      "v_exp_f32 v200, v200\n"
      "v_exp_f32 v201, v201\n"
      "v_smfmac_f32_32x32x32_bf16 v[224:239], v[128:131], v[192:199], v249\n"
      "v_exp_f32 v202, v202\n"
      "v_exp_f32 v203, v203\n"
      "v_exp_f32 v204, v204\n"
      "v_exp_f32 v205, v205\n"
      "v_exp_f32 v206, v206\n"
      "v_exp_f32 v207, v207\n"
      "v_exp_f32 v208, v208\n"
      "v_exp_f32 v209, v209\n"
      "v_smfmac_f32_32x32x32_bf16 v[224:239], v[132:135], v[200:207], v249\n"
      "v_exp_f32 v210, v210\n"
      "v_exp_f32 v211, v211\n"
      "v_exp_f32 v212, v212\n"
      "v_mfma_f32_32x32x16_bf16 v[192:207], v[40:43], v[240:243], -4.0\n"
      "v_exp_f32 v213, v213\n"
      "v_exp_f32 v214, v214\n"
      "v_exp_f32 v215, v215\n"
      "v_exp_f32 v216, v216\n"
      "v_exp_f32 v217, v217\n"
      "v_smfmac_f32_32x32x32_bf16 v[224:239], v[136:139], v[208:215], v249\n"
      "v_exp_f32 v218, v218\n"
      "v_exp_f32 v219, v219\n"
      "v_exp_f32 v220, v220\n"
      "v_exp_f32 v221, v221\n"
      "v_exp_f32 v222, v222\n"
      "v_exp_f32 v223, v223\n"
      "v_exp_f32 v192, v192\n"
      "v_exp_f32 v193, v193\n"
      "v_smfmac_f32_32x32x32_bf16 v[224:239], v[140:143], v[216:223], v249\n"
      "v_exp_f32 v194, v194\n"
      "v_exp_f32 v195, v195\n"
      "v_exp_f32 v196, v196\n"
      "v_mfma_f32_32x32x16_bf16 v[208:223], v[44:47], v[240:243], -4.0\n"
      "v_exp_f32 v197, v197\n"
      "v_exp_f32 v198, v198\n"
      "v_exp_f32 v199, v199\n"
      "v_exp_f32 v200, v200\n"
      "v_exp_f32 v201, v201\n"
      "v_smfmac_f32_32x32x32_bf16 v[224:239], v[144:147], v[192:199], v249\n"
      "v_exp_f32 v202, v202\n"
      "v_exp_f32 v203, v203\n"
      "v_exp_f32 v204, v204\n"
      "v_exp_f32 v205, v205\n"
      "v_exp_f32 v206, v206\n"
      "v_exp_f32 v207, v207\n"
      "v_exp_f32 v208, v208\n"
      "v_exp_f32 v209, v209\n"
      "v_smfmac_f32_32x32x32_bf16 v[224:239], v[148:151], v[200:207], v249\n"
      "v_exp_f32 v210, v210\n"
      "v_exp_f32 v211, v211\n"
      "v_exp_f32 v212, v212\n"
      "v_mfma_f32_32x32x16_bf16 v[192:207], v[48:51], v[240:243], -4.0\n"
      "v_exp_f32 v213, v213\n"
      "v_exp_f32 v214, v214\n"
      "v_exp_f32 v215, v215\n"
      "v_exp_f32 v216, v216\n"
      "v_exp_f32 v217, v217\n"
      "v_smfmac_f32_32x32x32_bf16 v[224:239], v[152:155], v[208:215], v249\n"
      "v_exp_f32 v218, v218\n"
      "v_exp_f32 v219, v219\n"
      "v_exp_f32 v220, v220\n"
      "v_exp_f32 v221, v221\n"
      "v_exp_f32 v222, v222\n"
      "v_exp_f32 v223, v223\n"
      "v_exp_f32 v192, v192\n"
      "v_exp_f32 v193, v193\n"
      "v_smfmac_f32_32x32x32_bf16 v[224:239], v[156:159], v[216:223], v249\n"
      "v_exp_f32 v194, v194\n"
      "v_exp_f32 v195, v195\n"
      "v_exp_f32 v196, v196\n"
      "v_mfma_f32_32x32x16_bf16 v[208:223], v[52:55], v[240:243], -4.0\n"
      "v_exp_f32 v197, v197\n"
      "v_exp_f32 v198, v198\n"
      "v_exp_f32 v199, v199\n"
      "v_exp_f32 v200, v200\n"
      "v_exp_f32 v201, v201\n"
      "v_smfmac_f32_32x32x32_bf16 v[224:239], v[160:163], v[192:199], v249\n"
      "v_exp_f32 v202, v202\n"
      "v_exp_f32 v203, v203\n"
      "v_exp_f32 v204, v204\n"
      "v_exp_f32 v205, v205\n"
      "v_exp_f32 v206, v206\n"
      "v_exp_f32 v207, v207\n"
      "v_exp_f32 v208, v208\n"
      "v_exp_f32 v209, v209\n"
      "v_smfmac_f32_32x32x32_bf16 v[224:239], v[164:167], v[200:207], v249\n"
      "v_exp_f32 v210, v210\n"
      "v_exp_f32 v211, v211\n"
      "v_exp_f32 v212, v212\n"
      "v_mfma_f32_32x32x16_bf16 v[192:207], v[56:59], v[240:243], -4.0\n"
      "v_exp_f32 v213, v213\n"
      "v_exp_f32 v214, v214\n"
      "v_exp_f32 v215, v215\n"
      "v_exp_f32 v216, v216\n"
      "v_exp_f32 v217, v217\n"
      "v_smfmac_f32_32x32x32_bf16 v[224:239], v[168:171], v[208:215], v249\n"
      "v_exp_f32 v218, v218\n"
      "v_exp_f32 v219, v219\n"
      "v_exp_f32 v220, v220\n"
      "v_exp_f32 v221, v221\n"
      "v_exp_f32 v222, v222\n"
      "v_exp_f32 v223, v223\n"
      "v_exp_f32 v192, v192\n"
      "v_exp_f32 v193, v193\n"
      "v_smfmac_f32_32x32x32_bf16 v[224:239], v[172:175], v[216:223], v249\n"
      "v_exp_f32 v194, v194\n"
      "v_exp_f32 v195, v195\n"
      "v_exp_f32 v196, v196\n"
      "v_mfma_f32_32x32x16_bf16 v[208:223], v[60:63], v[240:243], -4.0\n"
      "v_exp_f32 v197, v197\n"
      "ds_read_b128 v[240:243], v247 offset:2048\n"
      "v_exp_f32 v198, v198\n"
      "v_exp_f32 v199, v199\n"
      "v_exp_f32 v200, v200\n"
      "v_exp_f32 v201, v201\n"
      "v_smfmac_f32_32x32x32_bf16 v[224:239], v[176:179], v[192:199], v249\n"
      "v_exp_f32 v202, v202\n"
      "v_exp_f32 v203, v203\n"
      "v_exp_f32 v204, v204\n"
      "v_exp_f32 v205, v205\n"
      "v_exp_f32 v206, v206\n"
      "v_exp_f32 v207, v207\n"
      "v_exp_f32 v208, v208\n"
      "v_exp_f32 v209, v209\n"
      "v_smfmac_f32_32x32x32_bf16 v[224:239], v[180:183], v[200:207], v249\n"
      "v_exp_f32 v210, v210\n"
      "v_exp_f32 v211, v211\n"
      "v_exp_f32 v212, v212\n"
      "s_waitcnt lgkmcnt(0)\n"
      "v_mfma_f32_32x32x16_bf16 v[192:207], v[0:3], v[240:243], -4.0\n"
      "v_exp_f32 v213, v213\n"
      "v_exp_f32 v214, v214\n"
      "v_exp_f32 v215, v215\n"
      "v_exp_f32 v216, v216\n"
      "v_exp_f32 v217, v217\n"
      "v_smfmac_f32_32x32x32_bf16 v[224:239], v[184:187], v[208:215], v249\n"
      "v_exp_f32 v218, v218\n"
      "v_exp_f32 v219, v219\n"
      "v_exp_f32 v220, v220\n"
      "v_exp_f32 v221, v221\n"
      "v_exp_f32 v222, v222\n"
      "v_exp_f32 v223, v223\n"
      "v_exp_f32 v192, v192\n"
      "v_exp_f32 v193, v193\n"
      "v_smfmac_f32_32x32x32_bf16 v[224:239], v[188:191], v[216:223], v249\n"
      "v_exp_f32 v194, v194\n"
      "v_exp_f32 v195, v195\n"
      "v_exp_f32 v196, v196\n"
      "v_mfma_f32_32x32x16_bf16 v[208:223], v[4:7], v[240:243], -4.0\n"
      "v_exp_f32 v197, v197\n"
      "v_exp_f32 v198, v198\n"
      "v_exp_f32 v199, v199\n"
      "v_exp_f32 v200, v200\n"
      "v_exp_f32 v201, v201\n"
      "v_exp_f32 v202, v202\n"
      "v_exp_f32 v203, v203\n"
      "v_exp_f32 v204, v204\n"
      "ds_write_addtid_b32 v224 offset:25600\n"
      "ds_write_addtid_b32 v225 offset:25856\n"
      "ds_write_addtid_b32 v226 offset:26112\n"
      "ds_write_addtid_b32 v227 offset:26368\n"
      "ds_write_addtid_b32 v228 offset:26624\n"
      "ds_write_addtid_b32 v229 offset:26880\n"
      "ds_write_addtid_b32 v230 offset:27136\n"
      "ds_write_addtid_b32 v231 offset:27392\n"
      "ds_write_addtid_b32 v232 offset:27648\n"
      "ds_write_b32 v248, v252 offset:272\n"
      "v_mov_b64 v[224:225], 0\n"
      "v_mov_b64 v[226:227], 0\n"
      "v_mov_b64 v[228:229], 0\n"
      "v_mov_b64 v[230:231], 0\n"
      "v_mov_b64 v[232:233], 0\n"
      "v_exp_f32 v205, v205\n"
      "v_exp_f32 v206, v206\n"
      "v_smfmac_f32_32x32x32_bf16 v[224:239], v[64:67], v[192:199], v249\n"
      "v_exp_f32 v207, v207\n"
      "v_exp_f32 v208, v208\n"
      "v_exp_f32 v209, v209\n"
      "v_smfmac_f32_32x32x32_bf16 v[224:239], v[68:71], v[200:207], v249\n"
      "v_exp_f32 v210, v210\n"
      "v_exp_f32 v211, v211\n"
      "v_exp_f32 v212, v212\n"
      "v_mfma_f32_32x32x16_bf16 v[192:207], v[8:11], v[240:243], -4.0\n"
      "v_exp_f32 v213, v213\n"
      "v_exp_f32 v214, v214\n"
      "v_exp_f32 v215, v215\n"
      "v_exp_f32 v216, v216\n"
      "v_exp_f32 v217, v217\n"
      "v_smfmac_f32_32x32x32_bf16 v[224:239], v[72:75], v[208:215], v249\n"
      "v_exp_f32 v218, v218\n"
      "v_exp_f32 v219, v219\n"
      "v_exp_f32 v220, v220\n"
      "v_exp_f32 v221, v221\n"
      "v_exp_f32 v222, v222\n"
      "v_exp_f32 v223, v223\n"
      "v_exp_f32 v192, v192\n"
      "v_exp_f32 v193, v193\n"
      "v_smfmac_f32_32x32x32_bf16 v[224:239], v[76:79], v[216:223], v249\n"
      "v_exp_f32 v194, v194\n"
      "v_exp_f32 v195, v195\n"
      "v_exp_f32 v196, v196\n"
      "v_mfma_f32_32x32x16_bf16 v[208:223], v[12:15], v[240:243], -4.0\n"
      "v_exp_f32 v197, v197\n"
      "v_exp_f32 v198, v198\n"
      "v_exp_f32 v199, v199\n"
      "v_exp_f32 v200, v200\n"
      "v_exp_f32 v201, v201\n"
      "v_smfmac_f32_32x32x32_bf16 v[224:239], v[80:83], v[192:199], v249\n"
      "v_exp_f32 v202, v202\n"
      "v_exp_f32 v203, v203\n"
      "v_exp_f32 v204, v204\n"
      "v_exp_f32 v205, v205\n"
      "v_exp_f32 v206, v206\n"
      "v_exp_f32 v207, v207\n"
      "v_exp_f32 v208, v208\n"
      "v_exp_f32 v209, v209\n"
      "v_smfmac_f32_32x32x32_bf16 v[224:239], v[84:87], v[200:207], v249\n"
      "v_exp_f32 v210, v210\n"
      "v_exp_f32 v211, v211\n"
      "v_exp_f32 v212, v212\n"
      "v_mfma_f32_32x32x16_bf16 v[192:207], v[16:19], v[240:243], -4.0\n"
      "v_exp_f32 v213, v213\n"
      "v_exp_f32 v214, v214\n"
      "v_exp_f32 v215, v215\n"
      "v_exp_f32 v216, v216\n"
      "v_exp_f32 v217, v217\n"
      "v_smfmac_f32_32x32x32_bf16 v[224:239], v[88:91], v[208:215], v249\n"
      "v_exp_f32 v218, v218\n"
      "v_exp_f32 v219, v219\n"
      "v_exp_f32 v220, v220\n"
      "v_exp_f32 v221, v221\n"
      "v_exp_f32 v222, v222\n"
      "v_exp_f32 v223, v223\n"
      "v_exp_f32 v192, v192\n"
      "v_exp_f32 v193, v193\n"
      "v_smfmac_f32_32x32x32_bf16 v[224:239], v[92:95], v[216:223], v249\n"
      "v_exp_f32 v194, v194\n"
      "v_exp_f32 v195, v195\n"
      "v_exp_f32 v196, v196\n"
      "v_mfma_f32_32x32x16_bf16 v[208:223], v[20:23], v[240:243], -4.0\n"
      "v_exp_f32 v197, v197\n"
      "v_exp_f32 v198, v198\n"
      "v_exp_f32 v199, v199\n"
      "v_exp_f32 v200, v200\n"
      "v_exp_f32 v201, v201\n"
      "v_smfmac_f32_32x32x32_bf16 v[224:239], v[96:99], v[192:199], v249\n"
      "v_exp_f32 v202, v202\n"
      "v_exp_f32 v203, v203\n"
      "v_exp_f32 v204, v204\n"
      "v_exp_f32 v205, v205\n"
      "v_exp_f32 v206, v206\n"
      "v_exp_f32 v207, v207\n"
      "v_exp_f32 v208, v208\n"
      "v_exp_f32 v209, v209\n"
      "v_smfmac_f32_32x32x32_bf16 v[224:239], v[100:103], v[200:207], v249\n"
      "v_exp_f32 v210, v210\n"
      "v_exp_f32 v211, v211\n"
      "v_exp_f32 v212, v212\n"
      "v_mfma_f32_32x32x16_bf16 v[192:207], v[24:27], v[240:243], -4.0\n"
      "v_exp_f32 v213, v213\n"
      "v_exp_f32 v214, v214\n"
      "v_exp_f32 v215, v215\n"
      "v_exp_f32 v216, v216\n"
      "v_exp_f32 v217, v217\n"
      "v_smfmac_f32_32x32x32_bf16 v[224:239], v[104:107], v[208:215], v249\n"
      "v_exp_f32 v218, v218\n"
      "v_exp_f32 v219, v219\n"
      "v_exp_f32 v220, v220\n"
      "v_exp_f32 v221, v221\n"
      "v_exp_f32 v222, v222\n"
      "v_exp_f32 v223, v223\n"
      "v_exp_f32 v192, v192\n"
      "v_exp_f32 v193, v193\n"
      "v_smfmac_f32_32x32x32_bf16 v[224:239], v[108:111], v[216:223], v249\n"
      "v_exp_f32 v194, v194\n"
      "v_exp_f32 v195, v195\n"
      "v_exp_f32 v196, v196\n"
      "v_mfma_f32_32x32x16_bf16 v[208:223], v[28:31], v[240:243], -4.0\n"
      "v_exp_f32 v197, v197\n"
      "v_exp_f32 v198, v198\n"
      "v_exp_f32 v199, v199\n"
      "v_exp_f32 v200, v200\n"
      "v_exp_f32 v201, v201\n"
      "v_smfmac_f32_32x32x32_bf16 v[224:239], v[112:115], v[192:199], v249\n"
      "v_exp_f32 v202, v202\n"
      "v_exp_f32 v203, v203\n"
      "v_exp_f32 v204, v204\n"
      "v_exp_f32 v205, v205\n"
      "v_exp_f32 v206, v206\n"
      "v_exp_f32 v207, v207\n"
      "v_exp_f32 v208, v208\n"
      "v_exp_f32 v209, v209\n"
      "v_smfmac_f32_32x32x32_bf16 v[224:239], v[116:119], v[200:207], v249\n"
      "v_exp_f32 v210, v210\n"
      "v_exp_f32 v211, v211\n"
      "v_exp_f32 v212, v212\n"
      "v_mfma_f32_32x32x16_bf16 v[192:207], v[32:35], v[240:243], -4.0\n"
      "v_exp_f32 v213, v213\n"
      "v_exp_f32 v214, v214\n"
      "v_exp_f32 v215, v215\n"
      "v_exp_f32 v216, v216\n"
      "v_exp_f32 v217, v217\n"
      "v_smfmac_f32_32x32x32_bf16 v[224:239], v[120:123], v[208:215], v249\n"
      "v_exp_f32 v218, v218\n"
      "v_exp_f32 v219, v219\n"
      "v_exp_f32 v220, v220\n"
      "v_exp_f32 v221, v221\n"
      "v_exp_f32 v222, v222\n"
      "v_exp_f32 v223, v223\n"
      "v_exp_f32 v192, v192\n"
      "v_exp_f32 v193, v193\n"
      "v_smfmac_f32_32x32x32_bf16 v[224:239], v[124:127], v[216:223], v249\n"
      "v_exp_f32 v194, v194\n"
      "v_exp_f32 v195, v195\n"
      "v_exp_f32 v196, v196\n"
      "v_mfma_f32_32x32x16_bf16 v[208:223], v[36:39], v[240:243], -4.0\n"
      "v_exp_f32 v197, v197\n"
      "v_exp_f32 v198, v198\n"
      "v_exp_f32 v199, v199\n"
      "v_exp_f32 v200, v200\n"
      "v_exp_f32 v201, v201\n"
      "v_smfmac_f32_32x32x32_bf16 v[224:239], v[128:131], v[192:199], v249\n"
      "v_exp_f32 v202, v202\n"
      "v_exp_f32 v203, v203\n"
      "v_exp_f32 v204, v204\n"
      "v_exp_f32 v205, v205\n"
      "v_exp_f32 v206, v206\n"
      "v_exp_f32 v207, v207\n"
      "v_exp_f32 v208, v208\n"
      "v_exp_f32 v209, v209\n"
      "v_smfmac_f32_32x32x32_bf16 v[224:239], v[132:135], v[200:207], v249\n"
      "v_exp_f32 v210, v210\n"
      "v_exp_f32 v211, v211\n"
      "v_exp_f32 v212, v212\n"
      "v_mfma_f32_32x32x16_bf16 v[192:207], v[40:43], v[240:243], -4.0\n"
      "v_exp_f32 v213, v213\n"
      "v_exp_f32 v214, v214\n"
      "v_exp_f32 v215, v215\n"
      "v_exp_f32 v216, v216\n"
      "v_exp_f32 v217, v217\n"
      "v_smfmac_f32_32x32x32_bf16 v[224:239], v[136:139], v[208:215], v249\n"
      "v_exp_f32 v218, v218\n"
      "v_exp_f32 v219, v219\n"
      "v_exp_f32 v220, v220\n"
      "v_exp_f32 v221, v221\n"
      "v_exp_f32 v222, v222\n"
      "v_exp_f32 v223, v223\n"
      "v_exp_f32 v192, v192\n"
      "v_exp_f32 v193, v193\n"
      "v_smfmac_f32_32x32x32_bf16 v[224:239], v[140:143], v[216:223], v249\n"
      "v_exp_f32 v194, v194\n"
      "v_exp_f32 v195, v195\n"
      "v_exp_f32 v196, v196\n"
      "v_mfma_f32_32x32x16_bf16 v[208:223], v[44:47], v[240:243], -4.0\n"
      "v_exp_f32 v197, v197\n"
      "v_exp_f32 v198, v198\n"
      "v_exp_f32 v199, v199\n"
      "v_exp_f32 v200, v200\n"
      "v_exp_f32 v201, v201\n"
      "v_smfmac_f32_32x32x32_bf16 v[224:239], v[144:147], v[192:199], v249\n"
      "v_exp_f32 v202, v202\n"
      "v_exp_f32 v203, v203\n"
      "v_exp_f32 v204, v204\n"
      "v_exp_f32 v205, v205\n"
      "v_exp_f32 v206, v206\n"
      "v_exp_f32 v207, v207\n"
      "v_exp_f32 v208, v208\n"
      "v_exp_f32 v209, v209\n"
      "v_smfmac_f32_32x32x32_bf16 v[224:239], v[148:151], v[200:207], v249\n"
      "v_exp_f32 v210, v210\n"
      "v_exp_f32 v211, v211\n"
      "v_exp_f32 v212, v212\n"
      "v_mfma_f32_32x32x16_bf16 v[192:207], v[48:51], v[240:243], -4.0\n"
      "v_exp_f32 v213, v213\n"
      "v_exp_f32 v214, v214\n"
      "v_exp_f32 v215, v215\n"
      "v_exp_f32 v216, v216\n"
      "v_exp_f32 v217, v217\n"
      "v_smfmac_f32_32x32x32_bf16 v[224:239], v[152:155], v[208:215], v249\n"
      "v_exp_f32 v218, v218\n"
      "v_exp_f32 v219, v219\n"
      "v_exp_f32 v220, v220\n"
      "v_exp_f32 v221, v221\n"
      "v_exp_f32 v222, v222\n"
      "v_exp_f32 v223, v223\n"
      "v_exp_f32 v192, v192\n"
      "v_exp_f32 v193, v193\n"
      "v_smfmac_f32_32x32x32_bf16 v[224:239], v[156:159], v[216:223], v249\n"
      "v_exp_f32 v194, v194\n"
      "v_exp_f32 v195, v195\n"
      "v_exp_f32 v196, v196\n"
      "v_mfma_f32_32x32x16_bf16 v[208:223], v[52:55], v[240:243], -4.0\n"
      "v_exp_f32 v197, v197\n"
      "v_exp_f32 v198, v198\n"
      "v_exp_f32 v199, v199\n"
      "v_exp_f32 v200, v200\n"
      "v_exp_f32 v201, v201\n"
      "v_smfmac_f32_32x32x32_bf16 v[224:239], v[160:163], v[192:199], v249\n"
      "v_exp_f32 v202, v202\n"
      "v_exp_f32 v203, v203\n"
      "v_exp_f32 v204, v204\n"
      "v_exp_f32 v205, v205\n"
      "v_exp_f32 v206, v206\n"
      "v_exp_f32 v207, v207\n"
      "v_exp_f32 v208, v208\n"
      "v_exp_f32 v209, v209\n"
      "v_smfmac_f32_32x32x32_bf16 v[224:239], v[164:167], v[200:207], v249\n"
      "v_exp_f32 v210, v210\n"
      "v_exp_f32 v211, v211\n"
      "v_exp_f32 v212, v212\n"
      "v_mfma_f32_32x32x16_bf16 v[192:207], v[56:59], v[240:243], -4.0\n"
      "v_exp_f32 v213, v213\n"
      "v_exp_f32 v214, v214\n"
      "v_exp_f32 v215, v215\n"
      "v_exp_f32 v216, v216\n"
      "v_exp_f32 v217, v217\n"
      "v_smfmac_f32_32x32x32_bf16 v[224:239], v[168:171], v[208:215], v249\n"
      "v_exp_f32 v218, v218\n"
      "v_exp_f32 v219, v219\n"
      "v_exp_f32 v220, v220\n"
      "v_exp_f32 v221, v221\n"
      "v_exp_f32 v222, v222\n"
      "v_exp_f32 v223, v223\n"
      "v_exp_f32 v192, v192\n"
      "v_exp_f32 v193, v193\n"
      "v_smfmac_f32_32x32x32_bf16 v[224:239], v[172:175], v[216:223], v249\n"
      "v_exp_f32 v194, v194\n"
      "v_exp_f32 v195, v195\n"
      "v_exp_f32 v196, v196\n"
      "v_mfma_f32_32x32x16_bf16 v[208:223], v[60:63], v[240:243], -4.0\n"
      "v_exp_f32 v197, v197\n"
      "ds_read_b128 v[240:243], v247 offset:3072\n"
      "v_exp_f32 v198, v198\n"
      "v_exp_f32 v199, v199\n"
      "v_exp_f32 v200, v200\n"
      "v_exp_f32 v201, v201\n"
      "v_smfmac_f32_32x32x32_bf16 v[224:239], v[176:179], v[192:199], v249\n"
      "v_exp_f32 v202, v202\n"
      "v_exp_f32 v203, v203\n"
      "v_exp_f32 v204, v204\n"
      "v_exp_f32 v205, v205\n"
      "v_exp_f32 v206, v206\n"
      "v_exp_f32 v207, v207\n"
      "v_exp_f32 v208, v208\n"
      "v_exp_f32 v209, v209\n"
      "v_smfmac_f32_32x32x32_bf16 v[224:239], v[180:183], v[200:207], v249\n"
      "v_exp_f32 v210, v210\n"
      "v_exp_f32 v211, v211\n"
      "v_exp_f32 v212, v212\n"
      "s_waitcnt lgkmcnt(0)\n"
      "v_mfma_f32_32x32x16_bf16 v[192:207], v[0:3], v[240:243], -4.0\n"
      "v_exp_f32 v213, v213\n"
      "v_exp_f32 v214, v214\n"
      "v_exp_f32 v215, v215\n"
      "v_exp_f32 v216, v216\n"
      "v_exp_f32 v217, v217\n"
      "v_smfmac_f32_32x32x32_bf16 v[224:239], v[184:187], v[208:215], v249\n"
      "v_exp_f32 v218, v218\n"
      "v_exp_f32 v219, v219\n"
      "v_exp_f32 v220, v220\n"
      "v_exp_f32 v221, v221\n"
      "v_exp_f32 v222, v222\n"
      "v_exp_f32 v223, v223\n"
      "v_exp_f32 v192, v192\n"
      "v_exp_f32 v193, v193\n"
      "v_smfmac_f32_32x32x32_bf16 v[224:239], v[188:191], v[216:223], v249\n"
      "v_exp_f32 v194, v194\n"
      "v_exp_f32 v195, v195\n"
      "v_exp_f32 v196, v196\n"
      "v_mfma_f32_32x32x16_bf16 v[208:223], v[4:7], v[240:243], -4.0\n"
      "v_exp_f32 v197, v197\n"
      "v_exp_f32 v198, v198\n"
      "v_exp_f32 v199, v199\n"
      "v_exp_f32 v200, v200\n"
      "v_exp_f32 v201, v201\n"
      "v_exp_f32 v202, v202\n"
      "v_exp_f32 v203, v203\n"
      "v_exp_f32 v204, v204\n"
      "ds_write_addtid_b32 v224 offset:34816\n"
      "ds_write_addtid_b32 v225 offset:35072\n"
      "ds_write_addtid_b32 v226 offset:35328\n"
      "ds_write_addtid_b32 v227 offset:35584\n"
      "ds_write_addtid_b32 v228 offset:35840\n"
      "ds_write_addtid_b32 v229 offset:36096\n"
      "ds_write_addtid_b32 v230 offset:36352\n"
      "ds_write_addtid_b32 v231 offset:36608\n"
      "ds_write_addtid_b32 v232 offset:36864\n"
      "ds_write_b32 v248, v252 offset:288\n"
      "v_mov_b64 v[224:225], 0\n"
      "v_mov_b64 v[226:227], 0\n"
      "v_mov_b64 v[228:229], 0\n"
      "v_mov_b64 v[230:231], 0\n"
      "v_mov_b64 v[232:233], 0\n"
      "v_exp_f32 v205, v205\n"
      "v_exp_f32 v206, v206\n"
      "v_smfmac_f32_32x32x32_bf16 v[224:239], v[64:67], v[192:199], v249\n"
      "v_exp_f32 v207, v207\n"
      "v_exp_f32 v208, v208\n"
      "v_exp_f32 v209, v209\n"
      "v_smfmac_f32_32x32x32_bf16 v[224:239], v[68:71], v[200:207], v249\n"
      "v_exp_f32 v210, v210\n"
      "v_exp_f32 v211, v211\n"
      "v_exp_f32 v212, v212\n"
      "v_mfma_f32_32x32x16_bf16 v[192:207], v[8:11], v[240:243], -4.0\n"
      "v_exp_f32 v213, v213\n"
      "v_exp_f32 v214, v214\n"
      "v_exp_f32 v215, v215\n"
      "v_exp_f32 v216, v216\n"
      "v_exp_f32 v217, v217\n"
      "v_smfmac_f32_32x32x32_bf16 v[224:239], v[72:75], v[208:215], v249\n"
      "v_exp_f32 v218, v218\n"
      "v_exp_f32 v219, v219\n"
      "v_exp_f32 v220, v220\n"
      "v_exp_f32 v221, v221\n"
      "v_exp_f32 v222, v222\n"
      "v_exp_f32 v223, v223\n"
      "v_exp_f32 v192, v192\n"
      "v_exp_f32 v193, v193\n"
      "v_smfmac_f32_32x32x32_bf16 v[224:239], v[76:79], v[216:223], v249\n"
      "v_exp_f32 v194, v194\n"
      "v_exp_f32 v195, v195\n"
      "v_exp_f32 v196, v196\n"
      "v_mfma_f32_32x32x16_bf16 v[208:223], v[12:15], v[240:243], -4.0\n"
      "v_exp_f32 v197, v197\n"
      "v_exp_f32 v198, v198\n"
      "v_exp_f32 v199, v199\n"
      "v_exp_f32 v200, v200\n"
      "v_exp_f32 v201, v201\n"
      "v_smfmac_f32_32x32x32_bf16 v[224:239], v[80:83], v[192:199], v249\n"
      "v_exp_f32 v202, v202\n"
      "v_exp_f32 v203, v203\n"
      "v_exp_f32 v204, v204\n"
      "v_exp_f32 v205, v205\n"
      "v_exp_f32 v206, v206\n"
      "v_exp_f32 v207, v207\n"
      "v_exp_f32 v208, v208\n"
      "v_exp_f32 v209, v209\n"
      "v_smfmac_f32_32x32x32_bf16 v[224:239], v[84:87], v[200:207], v249\n"
      "v_exp_f32 v210, v210\n"
      "v_exp_f32 v211, v211\n"
      "v_exp_f32 v212, v212\n"
      "v_mfma_f32_32x32x16_bf16 v[192:207], v[16:19], v[240:243], -4.0\n"
      "v_exp_f32 v213, v213\n"
      "v_exp_f32 v214, v214\n"
      "v_exp_f32 v215, v215\n"
      "v_exp_f32 v216, v216\n"
      "v_exp_f32 v217, v217\n"
      "v_smfmac_f32_32x32x32_bf16 v[224:239], v[88:91], v[208:215], v249\n"
      "v_exp_f32 v218, v218\n"
      "v_exp_f32 v219, v219\n"
      "v_exp_f32 v220, v220\n"
      "v_exp_f32 v221, v221\n"
      "v_exp_f32 v222, v222\n"
      "v_exp_f32 v223, v223\n"
      "v_exp_f32 v192, v192\n"
      "v_exp_f32 v193, v193\n"
      "v_smfmac_f32_32x32x32_bf16 v[224:239], v[92:95], v[216:223], v249\n"
      "v_exp_f32 v194, v194\n"
      "v_exp_f32 v195, v195\n"
      "v_exp_f32 v196, v196\n"
      "v_mfma_f32_32x32x16_bf16 v[208:223], v[20:23], v[240:243], -4.0\n"
      "v_exp_f32 v197, v197\n"
      "v_exp_f32 v198, v198\n"
      "v_exp_f32 v199, v199\n"
      "v_exp_f32 v200, v200\n"
      "v_exp_f32 v201, v201\n"
      "v_smfmac_f32_32x32x32_bf16 v[224:239], v[96:99], v[192:199], v249\n"
      "v_exp_f32 v202, v202\n"
      "v_exp_f32 v203, v203\n"
      "v_exp_f32 v204, v204\n"
      "v_exp_f32 v205, v205\n"
      "v_exp_f32 v206, v206\n"
      "v_exp_f32 v207, v207\n"
      "v_exp_f32 v208, v208\n"
      "v_exp_f32 v209, v209\n"
      "v_smfmac_f32_32x32x32_bf16 v[224:239], v[100:103], v[200:207], v249\n"
      "v_exp_f32 v210, v210\n"
      "v_exp_f32 v211, v211\n"
      "v_exp_f32 v212, v212\n"
      "v_mfma_f32_32x32x16_bf16 v[192:207], v[24:27], v[240:243], -4.0\n"
      "v_exp_f32 v213, v213\n"
      "v_exp_f32 v214, v214\n"
      "v_exp_f32 v215, v215\n"
      "v_exp_f32 v216, v216\n"
      "v_exp_f32 v217, v217\n"
      "v_smfmac_f32_32x32x32_bf16 v[224:239], v[104:107], v[208:215], v249\n"
      "v_exp_f32 v218, v218\n"
      "v_exp_f32 v219, v219\n"
      "v_exp_f32 v220, v220\n"
      "v_exp_f32 v221, v221\n"
      "v_exp_f32 v222, v222\n"
      "v_exp_f32 v223, v223\n"
      "v_exp_f32 v192, v192\n"
      "v_exp_f32 v193, v193\n"
      "v_smfmac_f32_32x32x32_bf16 v[224:239], v[108:111], v[216:223], v249\n"
      "v_exp_f32 v194, v194\n"
      "v_exp_f32 v195, v195\n"
      "v_exp_f32 v196, v196\n"
      "v_mfma_f32_32x32x16_bf16 v[208:223], v[28:31], v[240:243], -4.0\n"
      "v_exp_f32 v197, v197\n"
      "v_exp_f32 v198, v198\n"
      "v_exp_f32 v199, v199\n"
      "v_exp_f32 v200, v200\n"
      "v_exp_f32 v201, v201\n"
      "v_smfmac_f32_32x32x32_bf16 v[224:239], v[112:115], v[192:199], v249\n"
      "v_exp_f32 v202, v202\n"
      "v_exp_f32 v203, v203\n"
      "v_exp_f32 v204, v204\n"
      "v_exp_f32 v205, v205\n"
      "v_exp_f32 v206, v206\n"
      "v_exp_f32 v207, v207\n"
      "v_exp_f32 v208, v208\n"
      "v_exp_f32 v209, v209\n"
      "v_smfmac_f32_32x32x32_bf16 v[224:239], v[116:119], v[200:207], v249\n"
      "v_exp_f32 v210, v210\n"
      "v_exp_f32 v211, v211\n"
      "v_exp_f32 v212, v212\n"
      "v_mfma_f32_32x32x16_bf16 v[192:207], v[32:35], v[240:243], -4.0\n"
      "v_exp_f32 v213, v213\n"
      "v_exp_f32 v214, v214\n"
      "v_exp_f32 v215, v215\n"
      "v_exp_f32 v216, v216\n"
      "v_exp_f32 v217, v217\n"
      "v_smfmac_f32_32x32x32_bf16 v[224:239], v[120:123], v[208:215], v249\n"
      "v_exp_f32 v218, v218\n"
      "v_exp_f32 v219, v219\n"
      "v_exp_f32 v220, v220\n"
      "v_exp_f32 v221, v221\n"
      "v_exp_f32 v222, v222\n"
      "v_exp_f32 v223, v223\n"
      "v_exp_f32 v192, v192\n"
      "v_exp_f32 v193, v193\n"
      "v_smfmac_f32_32x32x32_bf16 v[224:239], v[124:127], v[216:223], v249\n"
      "v_exp_f32 v194, v194\n"
      "v_exp_f32 v195, v195\n"
      "v_exp_f32 v196, v196\n"
      "v_mfma_f32_32x32x16_bf16 v[208:223], v[36:39], v[240:243], -4.0\n"
      "v_exp_f32 v197, v197\n"
      "v_exp_f32 v198, v198\n"
      "v_exp_f32 v199, v199\n"
      "v_exp_f32 v200, v200\n"
      "v_exp_f32 v201, v201\n"
      "v_smfmac_f32_32x32x32_bf16 v[224:239], v[128:131], v[192:199], v249\n"
      "v_exp_f32 v202, v202\n"
      "v_exp_f32 v203, v203\n"
      "v_exp_f32 v204, v204\n"
      "v_exp_f32 v205, v205\n"
      "v_exp_f32 v206, v206\n"
      "v_exp_f32 v207, v207\n"
      "v_exp_f32 v208, v208\n"
      "v_exp_f32 v209, v209\n"
      "v_smfmac_f32_32x32x32_bf16 v[224:239], v[132:135], v[200:207], v249\n"
      "v_exp_f32 v210, v210\n"
      "v_exp_f32 v211, v211\n"
      "v_exp_f32 v212, v212\n"
      "v_mfma_f32_32x32x16_bf16 v[192:207], v[40:43], v[240:243], -4.0\n"
      "v_exp_f32 v213, v213\n"
      "v_exp_f32 v214, v214\n"
      "v_exp_f32 v215, v215\n"
      "v_exp_f32 v216, v216\n"
      "v_exp_f32 v217, v217\n"
      "v_smfmac_f32_32x32x32_bf16 v[224:239], v[136:139], v[208:215], v249\n"
      "v_exp_f32 v218, v218\n"
      "v_exp_f32 v219, v219\n"
      "v_exp_f32 v220, v220\n"
      "v_exp_f32 v221, v221\n"
      "v_exp_f32 v222, v222\n"
      "v_exp_f32 v223, v223\n"
      "v_exp_f32 v192, v192\n"
      "v_exp_f32 v193, v193\n"
      "v_smfmac_f32_32x32x32_bf16 v[224:239], v[140:143], v[216:223], v249\n"
      "v_exp_f32 v194, v194\n"
      "v_exp_f32 v195, v195\n"
      "v_exp_f32 v196, v196\n"
      "v_mfma_f32_32x32x16_bf16 v[208:223], v[44:47], v[240:243], -4.0\n"
      "v_exp_f32 v197, v197\n"
      "v_exp_f32 v198, v198\n"
      "v_exp_f32 v199, v199\n"
      "v_exp_f32 v200, v200\n"
      "v_exp_f32 v201, v201\n"
      "v_smfmac_f32_32x32x32_bf16 v[224:239], v[144:147], v[192:199], v249\n"
      "v_exp_f32 v202, v202\n"
      "v_exp_f32 v203, v203\n"
      "v_exp_f32 v204, v204\n"
      "v_exp_f32 v205, v205\n"
      "v_exp_f32 v206, v206\n"
      "v_exp_f32 v207, v207\n"
      "v_exp_f32 v208, v208\n"
      "v_exp_f32 v209, v209\n"
      "v_smfmac_f32_32x32x32_bf16 v[224:239], v[148:151], v[200:207], v249\n"
      "v_exp_f32 v210, v210\n"
      "v_exp_f32 v211, v211\n"
      "v_exp_f32 v212, v212\n"
      "v_mfma_f32_32x32x16_bf16 v[192:207], v[48:51], v[240:243], -4.0\n"
      "v_exp_f32 v213, v213\n"
      "v_exp_f32 v214, v214\n"
      "v_exp_f32 v215, v215\n"
      "v_exp_f32 v216, v216\n"
      "v_exp_f32 v217, v217\n"
      "v_smfmac_f32_32x32x32_bf16 v[224:239], v[152:155], v[208:215], v249\n"
      "v_exp_f32 v218, v218\n"
      "v_exp_f32 v219, v219\n"
      "v_exp_f32 v220, v220\n"
      "v_exp_f32 v221, v221\n"
      "v_exp_f32 v222, v222\n"
      "v_exp_f32 v223, v223\n"
      "v_exp_f32 v192, v192\n"
      "v_exp_f32 v193, v193\n"
      "v_smfmac_f32_32x32x32_bf16 v[224:239], v[156:159], v[216:223], v249\n"
      "v_exp_f32 v194, v194\n"
      "v_exp_f32 v195, v195\n"
      "v_exp_f32 v196, v196\n"
      "v_mfma_f32_32x32x16_bf16 v[208:223], v[52:55], v[240:243], -4.0\n"
      "v_exp_f32 v197, v197\n"
      "v_exp_f32 v198, v198\n"
      "v_exp_f32 v199, v199\n"
      "v_exp_f32 v200, v200\n"
      "v_exp_f32 v201, v201\n"
      "v_smfmac_f32_32x32x32_bf16 v[224:239], v[160:163], v[192:199], v249\n"
      "v_exp_f32 v202, v202\n"
      "v_exp_f32 v203, v203\n"
      "v_exp_f32 v204, v204\n"
      "v_exp_f32 v205, v205\n"
      "v_exp_f32 v206, v206\n"
      "v_exp_f32 v207, v207\n"
      "v_exp_f32 v208, v208\n"
      "v_exp_f32 v209, v209\n"
      "v_smfmac_f32_32x32x32_bf16 v[224:239], v[164:167], v[200:207], v249\n"
      "v_exp_f32 v210, v210\n"
      "v_exp_f32 v211, v211\n"
      "v_exp_f32 v212, v212\n"
      "v_mfma_f32_32x32x16_bf16 v[192:207], v[56:59], v[240:243], -4.0\n"
      "v_exp_f32 v213, v213\n"
      "v_exp_f32 v214, v214\n"
      "v_exp_f32 v215, v215\n"
      "v_exp_f32 v216, v216\n"
      "v_exp_f32 v217, v217\n"
      "v_smfmac_f32_32x32x32_bf16 v[224:239], v[168:171], v[208:215], v249\n"
      "v_exp_f32 v218, v218\n"
      "v_exp_f32 v219, v219\n"
      "v_exp_f32 v220, v220\n"
      "v_exp_f32 v221, v221\n"
      "v_exp_f32 v222, v222\n"
      "v_exp_f32 v223, v223\n"
      "v_exp_f32 v192, v192\n"
      "v_exp_f32 v193, v193\n"
      "v_smfmac_f32_32x32x32_bf16 v[224:239], v[172:175], v[216:223], v249\n"
      "v_exp_f32 v194, v194\n"
      "v_exp_f32 v195, v195\n"
      "v_exp_f32 v196, v196\n"
      "v_mfma_f32_32x32x16_bf16 v[208:223], v[60:63], v[240:243], -4.0\n"
      "v_exp_f32 v197, v197\n"
      "ds_read_b128 v[240:243], v247 offset:4096\n"
      "v_exp_f32 v198, v198\n"
      "v_exp_f32 v199, v199\n"
      "v_exp_f32 v200, v200\n"
      "v_exp_f32 v201, v201\n"
      "v_smfmac_f32_32x32x32_bf16 v[224:239], v[176:179], v[192:199], v249\n"
      "v_exp_f32 v202, v202\n"
      "v_exp_f32 v203, v203\n"
      "v_exp_f32 v204, v204\n"
      "v_exp_f32 v205, v205\n"
      "v_exp_f32 v206, v206\n"
      "v_exp_f32 v207, v207\n"
      "v_exp_f32 v208, v208\n"
      "v_exp_f32 v209, v209\n"
      "v_smfmac_f32_32x32x32_bf16 v[224:239], v[180:183], v[200:207], v249\n"
      "v_exp_f32 v210, v210\n"
      "v_exp_f32 v211, v211\n"
      "v_exp_f32 v212, v212\n"
      "s_waitcnt lgkmcnt(0)\n"
      "v_mfma_f32_32x32x16_bf16 v[192:207], v[0:3], v[240:243], -4.0\n"
      "v_exp_f32 v213, v213\n"
      "v_exp_f32 v214, v214\n"
      "v_exp_f32 v215, v215\n"
      "v_exp_f32 v216, v216\n"
      "v_exp_f32 v217, v217\n"
      "v_smfmac_f32_32x32x32_bf16 v[224:239], v[184:187], v[208:215], v249\n"
      "v_exp_f32 v218, v218\n"
      "v_exp_f32 v219, v219\n"
      "v_exp_f32 v220, v220\n"
      "v_exp_f32 v221, v221\n"
      "v_exp_f32 v222, v222\n"
      "v_exp_f32 v223, v223\n"
      "v_exp_f32 v192, v192\n"
      "v_exp_f32 v193, v193\n"
      "v_smfmac_f32_32x32x32_bf16 v[224:239], v[188:191], v[216:223], v249\n"
      "v_exp_f32 v194, v194\n"
      "v_exp_f32 v195, v195\n"
      "v_exp_f32 v196, v196\n"
      "v_mfma_f32_32x32x16_bf16 v[208:223], v[4:7], v[240:243], -4.0\n"
      "v_exp_f32 v197, v197\n"
      "v_exp_f32 v198, v198\n"
      "v_exp_f32 v199, v199\n"
      "v_exp_f32 v200, v200\n"
      "v_exp_f32 v201, v201\n"
      "v_exp_f32 v202, v202\n"
      "v_exp_f32 v203, v203\n"
      "v_exp_f32 v204, v204\n"
      "ds_write_addtid_b32 v224 offset:44032\n"
      "ds_write_addtid_b32 v225 offset:44288\n"
      "ds_write_addtid_b32 v226 offset:44544\n"
      "ds_write_addtid_b32 v227 offset:44800\n"
      "ds_write_addtid_b32 v228 offset:45056\n"
      "ds_write_addtid_b32 v229 offset:45312\n"
      "ds_write_addtid_b32 v230 offset:45568\n"
      "ds_write_addtid_b32 v231 offset:45824\n"
      "ds_write_addtid_b32 v232 offset:46080\n"
      "ds_write_b32 v248, v252 offset:304\n"
      "v_mov_b64 v[224:225], 0\n"
      "v_mov_b64 v[226:227], 0\n"
      "v_mov_b64 v[228:229], 0\n"
      "v_mov_b64 v[230:231], 0\n"
      "v_mov_b64 v[232:233], 0\n"
      "v_exp_f32 v205, v205\n"
      "v_exp_f32 v206, v206\n"
      "v_smfmac_f32_32x32x32_bf16 v[224:239], v[64:67], v[192:199], v249\n"
      "v_exp_f32 v207, v207\n"
      "v_exp_f32 v208, v208\n"
      "v_exp_f32 v209, v209\n"
      "v_smfmac_f32_32x32x32_bf16 v[224:239], v[68:71], v[200:207], v249\n"
      "v_exp_f32 v210, v210\n"
      "v_exp_f32 v211, v211\n"
      "v_exp_f32 v212, v212\n"
      "v_mfma_f32_32x32x16_bf16 v[192:207], v[8:11], v[240:243], -4.0\n"
      "v_exp_f32 v213, v213\n"
      "v_exp_f32 v214, v214\n"
      "v_exp_f32 v215, v215\n"
      "v_exp_f32 v216, v216\n"
      "v_exp_f32 v217, v217\n"
      "v_smfmac_f32_32x32x32_bf16 v[224:239], v[72:75], v[208:215], v249\n"
      "v_exp_f32 v218, v218\n"
      "v_exp_f32 v219, v219\n"
      "v_exp_f32 v220, v220\n"
      "v_exp_f32 v221, v221\n"
      "v_exp_f32 v222, v222\n"
      "v_exp_f32 v223, v223\n"
      "v_exp_f32 v192, v192\n"
      "v_exp_f32 v193, v193\n"
      "v_smfmac_f32_32x32x32_bf16 v[224:239], v[76:79], v[216:223], v249\n"
      "v_exp_f32 v194, v194\n"
      "v_exp_f32 v195, v195\n"
      "v_exp_f32 v196, v196\n"
      "v_mfma_f32_32x32x16_bf16 v[208:223], v[12:15], v[240:243], -4.0\n"
      "v_exp_f32 v197, v197\n"
      "v_exp_f32 v198, v198\n"
      "v_exp_f32 v199, v199\n"
      "v_exp_f32 v200, v200\n"
      "v_exp_f32 v201, v201\n"
      "v_smfmac_f32_32x32x32_bf16 v[224:239], v[80:83], v[192:199], v249\n"
      "v_exp_f32 v202, v202\n"
      "v_exp_f32 v203, v203\n"
      "v_exp_f32 v204, v204\n"
      "v_exp_f32 v205, v205\n"
      "v_exp_f32 v206, v206\n"
      "v_exp_f32 v207, v207\n"
      "v_exp_f32 v208, v208\n"
      "v_exp_f32 v209, v209\n"
      "v_smfmac_f32_32x32x32_bf16 v[224:239], v[84:87], v[200:207], v249\n"
      "v_exp_f32 v210, v210\n"
      "v_exp_f32 v211, v211\n"
      "v_exp_f32 v212, v212\n"
      "v_mfma_f32_32x32x16_bf16 v[192:207], v[16:19], v[240:243], -4.0\n"
      "v_exp_f32 v213, v213\n"
      "v_exp_f32 v214, v214\n"
      "v_exp_f32 v215, v215\n"
      "v_exp_f32 v216, v216\n"
      "v_exp_f32 v217, v217\n"
      "v_smfmac_f32_32x32x32_bf16 v[224:239], v[88:91], v[208:215], v249\n"
      "v_exp_f32 v218, v218\n"
      "v_exp_f32 v219, v219\n"
      "v_exp_f32 v220, v220\n"
      "v_exp_f32 v221, v221\n"
      "v_exp_f32 v222, v222\n"
      "v_exp_f32 v223, v223\n"
      "v_exp_f32 v192, v192\n"
      "v_exp_f32 v193, v193\n"
      "v_smfmac_f32_32x32x32_bf16 v[224:239], v[92:95], v[216:223], v249\n"
      "v_exp_f32 v194, v194\n"
      "v_exp_f32 v195, v195\n"
      "v_exp_f32 v196, v196\n"
      "v_mfma_f32_32x32x16_bf16 v[208:223], v[20:23], v[240:243], -4.0\n"
      "v_exp_f32 v197, v197\n"
      "v_exp_f32 v198, v198\n"
      "v_exp_f32 v199, v199\n"
      "v_exp_f32 v200, v200\n"
      "v_exp_f32 v201, v201\n"
      "v_smfmac_f32_32x32x32_bf16 v[224:239], v[96:99], v[192:199], v249\n"
      "v_exp_f32 v202, v202\n"
      "v_exp_f32 v203, v203\n"
      "v_exp_f32 v204, v204\n"
      "v_exp_f32 v205, v205\n"
      "v_exp_f32 v206, v206\n"
      "v_exp_f32 v207, v207\n"
      "v_exp_f32 v208, v208\n"
      "v_exp_f32 v209, v209\n"
      "v_smfmac_f32_32x32x32_bf16 v[224:239], v[100:103], v[200:207], v249\n"
      "v_exp_f32 v210, v210\n"
      "v_exp_f32 v211, v211\n"
      "v_exp_f32 v212, v212\n"
      "v_mfma_f32_32x32x16_bf16 v[192:207], v[24:27], v[240:243], -4.0\n"
      "v_exp_f32 v213, v213\n"
      "v_exp_f32 v214, v214\n"
      "v_exp_f32 v215, v215\n"
      "v_exp_f32 v216, v216\n"
      "v_exp_f32 v217, v217\n"
      "v_smfmac_f32_32x32x32_bf16 v[224:239], v[104:107], v[208:215], v249\n"
      "v_exp_f32 v218, v218\n"
      "v_exp_f32 v219, v219\n"
      "v_exp_f32 v220, v220\n"
      "v_exp_f32 v221, v221\n"
      "v_exp_f32 v222, v222\n"
      "v_exp_f32 v223, v223\n"
      "v_exp_f32 v192, v192\n"
      "v_exp_f32 v193, v193\n"
      "v_smfmac_f32_32x32x32_bf16 v[224:239], v[108:111], v[216:223], v249\n"
      "v_exp_f32 v194, v194\n"
      "v_exp_f32 v195, v195\n"
      "v_exp_f32 v196, v196\n"
      "v_mfma_f32_32x32x16_bf16 v[208:223], v[28:31], v[240:243], -4.0\n"
      "v_exp_f32 v197, v197\n"
      "v_exp_f32 v198, v198\n"
      "v_exp_f32 v199, v199\n"
      "v_exp_f32 v200, v200\n"
      "v_exp_f32 v201, v201\n"
      "v_smfmac_f32_32x32x32_bf16 v[224:239], v[112:115], v[192:199], v249\n"
      "v_exp_f32 v202, v202\n"
      "v_exp_f32 v203, v203\n"
      "v_exp_f32 v204, v204\n"
      "v_exp_f32 v205, v205\n"
      "v_exp_f32 v206, v206\n"
      "v_exp_f32 v207, v207\n"
      "v_exp_f32 v208, v208\n"
      "v_exp_f32 v209, v209\n"
      "v_smfmac_f32_32x32x32_bf16 v[224:239], v[116:119], v[200:207], v249\n"
      "v_exp_f32 v210, v210\n"
      "v_exp_f32 v211, v211\n"
      "v_exp_f32 v212, v212\n"
      "v_mfma_f32_32x32x16_bf16 v[192:207], v[32:35], v[240:243], -4.0\n"
      "v_exp_f32 v213, v213\n"
      "v_exp_f32 v214, v214\n"
      "v_exp_f32 v215, v215\n"
      "v_exp_f32 v216, v216\n"
      "v_exp_f32 v217, v217\n"
      "v_smfmac_f32_32x32x32_bf16 v[224:239], v[120:123], v[208:215], v249\n"
      "v_exp_f32 v218, v218\n"
      "v_exp_f32 v219, v219\n"
      "v_exp_f32 v220, v220\n"
      "v_exp_f32 v221, v221\n"
      "v_exp_f32 v222, v222\n"
      "v_exp_f32 v223, v223\n"
      "v_exp_f32 v192, v192\n"
      "v_exp_f32 v193, v193\n"
      "v_smfmac_f32_32x32x32_bf16 v[224:239], v[124:127], v[216:223], v249\n"
      "v_exp_f32 v194, v194\n"
      "v_exp_f32 v195, v195\n"
      "v_exp_f32 v196, v196\n"
      "v_mfma_f32_32x32x16_bf16 v[208:223], v[36:39], v[240:243], -4.0\n"
      "v_exp_f32 v197, v197\n"
      "v_exp_f32 v198, v198\n"
      "v_exp_f32 v199, v199\n"
      "v_exp_f32 v200, v200\n"
      "v_exp_f32 v201, v201\n"
      "v_smfmac_f32_32x32x32_bf16 v[224:239], v[128:131], v[192:199], v249\n"
      "v_exp_f32 v202, v202\n"
      "v_exp_f32 v203, v203\n"
      "v_exp_f32 v204, v204\n"
      "v_exp_f32 v205, v205\n"
      "v_exp_f32 v206, v206\n"
      "v_exp_f32 v207, v207\n"
      "v_exp_f32 v208, v208\n"
      "v_exp_f32 v209, v209\n"
      "v_smfmac_f32_32x32x32_bf16 v[224:239], v[132:135], v[200:207], v249\n"
      "v_exp_f32 v210, v210\n"
      "v_exp_f32 v211, v211\n"
      "v_exp_f32 v212, v212\n"
      "v_mfma_f32_32x32x16_bf16 v[192:207], v[40:43], v[240:243], -4.0\n"
      "v_exp_f32 v213, v213\n"
      "v_exp_f32 v214, v214\n"
      "v_exp_f32 v215, v215\n"
      "v_exp_f32 v216, v216\n"
      "v_exp_f32 v217, v217\n"
      "v_smfmac_f32_32x32x32_bf16 v[224:239], v[136:139], v[208:215], v249\n"
      "v_exp_f32 v218, v218\n"
      "v_exp_f32 v219, v219\n"
      "v_exp_f32 v220, v220\n"
      "v_exp_f32 v221, v221\n"
      "v_exp_f32 v222, v222\n"
      "v_exp_f32 v223, v223\n"
      "v_exp_f32 v192, v192\n"
      "v_exp_f32 v193, v193\n"
      "v_smfmac_f32_32x32x32_bf16 v[224:239], v[140:143], v[216:223], v249\n"
      "v_exp_f32 v194, v194\n"
      "v_exp_f32 v195, v195\n"
      "v_exp_f32 v196, v196\n"
      "v_mfma_f32_32x32x16_bf16 v[208:223], v[44:47], v[240:243], -4.0\n"
      "v_exp_f32 v197, v197\n"
      "v_exp_f32 v198, v198\n"
      "v_exp_f32 v199, v199\n"
      "v_exp_f32 v200, v200\n"
      "v_exp_f32 v201, v201\n"
      "v_smfmac_f32_32x32x32_bf16 v[224:239], v[144:147], v[192:199], v249\n"
      "v_exp_f32 v202, v202\n"
      "v_exp_f32 v203, v203\n"
      "v_exp_f32 v204, v204\n"
      "v_exp_f32 v205, v205\n"
      "v_exp_f32 v206, v206\n"
      "v_exp_f32 v207, v207\n"
      "v_exp_f32 v208, v208\n"
      "v_exp_f32 v209, v209\n"
      "v_smfmac_f32_32x32x32_bf16 v[224:239], v[148:151], v[200:207], v249\n"
      "v_exp_f32 v210, v210\n"
      "v_exp_f32 v211, v211\n"
      "v_exp_f32 v212, v212\n"
      "v_mfma_f32_32x32x16_bf16 v[192:207], v[48:51], v[240:243], -4.0\n"
      "v_exp_f32 v213, v213\n"
      "v_exp_f32 v214, v214\n"
      "v_exp_f32 v215, v215\n"
      "v_exp_f32 v216, v216\n"
      "v_exp_f32 v217, v217\n"
      "v_smfmac_f32_32x32x32_bf16 v[224:239], v[152:155], v[208:215], v249\n"
      "v_exp_f32 v218, v218\n"
      "v_exp_f32 v219, v219\n"
      "v_exp_f32 v220, v220\n"
      "v_exp_f32 v221, v221\n"
      "v_exp_f32 v222, v222\n"
      "v_exp_f32 v223, v223\n"
      "v_exp_f32 v192, v192\n"
      "v_exp_f32 v193, v193\n"
      "v_smfmac_f32_32x32x32_bf16 v[224:239], v[156:159], v[216:223], v249\n"
      "v_exp_f32 v194, v194\n"
      "v_exp_f32 v195, v195\n"
      "v_exp_f32 v196, v196\n"
      "v_mfma_f32_32x32x16_bf16 v[208:223], v[52:55], v[240:243], -4.0\n"
      "v_exp_f32 v197, v197\n"
      "v_exp_f32 v198, v198\n"
      "v_exp_f32 v199, v199\n"
      "v_exp_f32 v200, v200\n"
      "v_exp_f32 v201, v201\n"
      "v_smfmac_f32_32x32x32_bf16 v[224:239], v[160:163], v[192:199], v249\n"
      "v_exp_f32 v202, v202\n"
      "v_exp_f32 v203, v203\n"
      "v_exp_f32 v204, v204\n"
      "v_exp_f32 v205, v205\n"
      "v_exp_f32 v206, v206\n"
      "v_exp_f32 v207, v207\n"
      "v_exp_f32 v208, v208\n"
      "v_exp_f32 v209, v209\n"
      "v_smfmac_f32_32x32x32_bf16 v[224:239], v[164:167], v[200:207], v249\n"
      "v_exp_f32 v210, v210\n"
      "v_exp_f32 v211, v211\n"
      "v_exp_f32 v212, v212\n"
      "v_mfma_f32_32x32x16_bf16 v[192:207], v[56:59], v[240:243], -4.0\n"
      "v_exp_f32 v213, v213\n"
      "v_exp_f32 v214, v214\n"
      "v_exp_f32 v215, v215\n"
      "v_exp_f32 v216, v216\n"
      "v_exp_f32 v217, v217\n"
      "v_smfmac_f32_32x32x32_bf16 v[224:239], v[168:171], v[208:215], v249\n"
      "v_exp_f32 v218, v218\n"
      "v_exp_f32 v219, v219\n"
      "v_exp_f32 v220, v220\n"
      "v_exp_f32 v221, v221\n"
      "v_exp_f32 v222, v222\n"
      "v_exp_f32 v223, v223\n"
      "v_exp_f32 v192, v192\n"
      "v_exp_f32 v193, v193\n"
      "v_smfmac_f32_32x32x32_bf16 v[224:239], v[172:175], v[216:223], v249\n"
      "v_exp_f32 v194, v194\n"
      "v_exp_f32 v195, v195\n"
      "v_exp_f32 v196, v196\n"
      "v_mfma_f32_32x32x16_bf16 v[208:223], v[60:63], v[240:243], -4.0\n"
      "v_exp_f32 v197, v197\n"
      "ds_read_b128 v[240:243], v247 offset:5120\n"
      "v_exp_f32 v198, v198\n"
      "v_exp_f32 v199, v199\n"
      "v_exp_f32 v200, v200\n"
      "v_exp_f32 v201, v201\n"
      "v_smfmac_f32_32x32x32_bf16 v[224:239], v[176:179], v[192:199], v249\n"
      "v_exp_f32 v202, v202\n"
      "v_exp_f32 v203, v203\n"
      "v_exp_f32 v204, v204\n"
      "v_exp_f32 v205, v205\n"
      "v_exp_f32 v206, v206\n"
      "v_exp_f32 v207, v207\n"
      "v_exp_f32 v208, v208\n"
      "v_exp_f32 v209, v209\n"
      "v_smfmac_f32_32x32x32_bf16 v[224:239], v[180:183], v[200:207], v249\n"
      "v_exp_f32 v210, v210\n"
      "v_exp_f32 v211, v211\n"
      "v_exp_f32 v212, v212\n"
      "s_waitcnt lgkmcnt(0)\n"
      "v_mfma_f32_32x32x16_bf16 v[192:207], v[0:3], v[240:243], -4.0\n"
      "v_exp_f32 v213, v213\n"
      "v_exp_f32 v214, v214\n"
      "v_exp_f32 v215, v215\n"
      "v_exp_f32 v216, v216\n"
      "v_exp_f32 v217, v217\n"
      "v_smfmac_f32_32x32x32_bf16 v[224:239], v[184:187], v[208:215], v249\n"
      "v_exp_f32 v218, v218\n"
      "v_exp_f32 v219, v219\n"
      "v_exp_f32 v220, v220\n"
      "v_exp_f32 v221, v221\n"
      "v_exp_f32 v222, v222\n"
      "v_exp_f32 v223, v223\n"
      "v_exp_f32 v192, v192\n"
      "v_exp_f32 v193, v193\n"
      "v_smfmac_f32_32x32x32_bf16 v[224:239], v[188:191], v[216:223], v249\n"
      "v_exp_f32 v194, v194\n"
      "v_exp_f32 v195, v195\n"
      "v_exp_f32 v196, v196\n"
      "v_mfma_f32_32x32x16_bf16 v[208:223], v[4:7], v[240:243], -4.0\n"
      "v_exp_f32 v197, v197\n"
      "v_exp_f32 v198, v198\n"
      "v_exp_f32 v199, v199\n"
      "v_exp_f32 v200, v200\n"
      "v_exp_f32 v201, v201\n"
      "v_exp_f32 v202, v202\n"
      "v_exp_f32 v203, v203\n"
      "v_exp_f32 v204, v204\n"
      "ds_write_addtid_b32 v224 offset:53248\n"
      "ds_write_addtid_b32 v225 offset:53504\n"
      "ds_write_addtid_b32 v226 offset:53760\n"
      "ds_write_addtid_b32 v227 offset:54016\n"
      "ds_write_addtid_b32 v228 offset:54272\n"
      "ds_write_addtid_b32 v229 offset:54528\n"
      "ds_write_addtid_b32 v230 offset:54784\n"
      "ds_write_addtid_b32 v231 offset:55040\n"
      "ds_write_addtid_b32 v232 offset:55296\n"
      "ds_write_b32 v248, v252 offset:320\n"
      "v_mov_b64 v[224:225], 0\n"
      "v_mov_b64 v[226:227], 0\n"
      "v_mov_b64 v[228:229], 0\n"
      "v_mov_b64 v[230:231], 0\n"
      "v_mov_b64 v[232:233], 0\n"
      "v_exp_f32 v205, v205\n"
      "v_exp_f32 v206, v206\n"
      "v_smfmac_f32_32x32x32_bf16 v[224:239], v[64:67], v[192:199], v249\n"
      "v_exp_f32 v207, v207\n"
      "v_exp_f32 v208, v208\n"
      "v_exp_f32 v209, v209\n"
      "v_smfmac_f32_32x32x32_bf16 v[224:239], v[68:71], v[200:207], v249\n"
      "v_exp_f32 v210, v210\n"
      "v_exp_f32 v211, v211\n"
      "v_exp_f32 v212, v212\n"
      "v_mfma_f32_32x32x16_bf16 v[192:207], v[8:11], v[240:243], -4.0\n"
      "v_exp_f32 v213, v213\n"
      "v_exp_f32 v214, v214\n"
      "v_exp_f32 v215, v215\n"
      "v_exp_f32 v216, v216\n"
      "v_exp_f32 v217, v217\n"
      "v_smfmac_f32_32x32x32_bf16 v[224:239], v[72:75], v[208:215], v249\n"
      "v_exp_f32 v218, v218\n"
      "v_exp_f32 v219, v219\n"
      "v_exp_f32 v220, v220\n"
      "v_exp_f32 v221, v221\n"
      "v_exp_f32 v222, v222\n"
      "v_exp_f32 v223, v223\n"
      "v_exp_f32 v192, v192\n"
      "v_exp_f32 v193, v193\n"
      "v_smfmac_f32_32x32x32_bf16 v[224:239], v[76:79], v[216:223], v249\n"
      "v_exp_f32 v194, v194\n"
      "v_exp_f32 v195, v195\n"
      "v_exp_f32 v196, v196\n"
      "v_mfma_f32_32x32x16_bf16 v[208:223], v[12:15], v[240:243], -4.0\n"
      "v_exp_f32 v197, v197\n"
      "v_exp_f32 v198, v198\n"
      "v_exp_f32 v199, v199\n"
      "v_exp_f32 v200, v200\n"
      "v_exp_f32 v201, v201\n"
      "v_smfmac_f32_32x32x32_bf16 v[224:239], v[80:83], v[192:199], v249\n"
      "v_exp_f32 v202, v202\n"
      "v_exp_f32 v203, v203\n"
      "v_exp_f32 v204, v204\n"
      "v_exp_f32 v205, v205\n"
      "v_exp_f32 v206, v206\n"
      "v_exp_f32 v207, v207\n"
      "v_exp_f32 v208, v208\n"
      "v_exp_f32 v209, v209\n"
      "v_smfmac_f32_32x32x32_bf16 v[224:239], v[84:87], v[200:207], v249\n"
      "v_exp_f32 v210, v210\n"
      "v_exp_f32 v211, v211\n"
      "v_exp_f32 v212, v212\n"
      "v_mfma_f32_32x32x16_bf16 v[192:207], v[16:19], v[240:243], -4.0\n"
      "v_exp_f32 v213, v213\n"
      "v_exp_f32 v214, v214\n"
      "v_exp_f32 v215, v215\n"
      "v_exp_f32 v216, v216\n"
      "v_exp_f32 v217, v217\n"
      "v_smfmac_f32_32x32x32_bf16 v[224:239], v[88:91], v[208:215], v249\n"
      "v_exp_f32 v218, v218\n"
      "v_exp_f32 v219, v219\n"
      "v_exp_f32 v220, v220\n"
      "v_exp_f32 v221, v221\n"
      "v_exp_f32 v222, v222\n"
      "v_exp_f32 v223, v223\n"
      "v_exp_f32 v192, v192\n"
      "v_exp_f32 v193, v193\n"
      "v_smfmac_f32_32x32x32_bf16 v[224:239], v[92:95], v[216:223], v249\n"
      "v_exp_f32 v194, v194\n"
      "v_exp_f32 v195, v195\n"
      "v_exp_f32 v196, v196\n"
      "v_mfma_f32_32x32x16_bf16 v[208:223], v[20:23], v[240:243], -4.0\n"
      "v_exp_f32 v197, v197\n"
      "v_exp_f32 v198, v198\n"
      "v_exp_f32 v199, v199\n"
      "v_exp_f32 v200, v200\n"
      "v_exp_f32 v201, v201\n"
      "v_smfmac_f32_32x32x32_bf16 v[224:239], v[96:99], v[192:199], v249\n"
      "v_exp_f32 v202, v202\n"
      "v_exp_f32 v203, v203\n"
      "v_exp_f32 v204, v204\n"
      "v_exp_f32 v205, v205\n"
      "v_exp_f32 v206, v206\n"
      "v_exp_f32 v207, v207\n"
      "v_exp_f32 v208, v208\n"
      "v_exp_f32 v209, v209\n"
      "v_smfmac_f32_32x32x32_bf16 v[224:239], v[100:103], v[200:207], v249\n"
      "v_exp_f32 v210, v210\n"
      "v_exp_f32 v211, v211\n"
      "v_exp_f32 v212, v212\n"
      "v_mfma_f32_32x32x16_bf16 v[192:207], v[24:27], v[240:243], -4.0\n"
      "v_exp_f32 v213, v213\n"
      "v_exp_f32 v214, v214\n"
      "v_exp_f32 v215, v215\n"
      "v_exp_f32 v216, v216\n"
      "v_exp_f32 v217, v217\n"
      "v_smfmac_f32_32x32x32_bf16 v[224:239], v[104:107], v[208:215], v249\n"
      "v_exp_f32 v218, v218\n"
      "v_exp_f32 v219, v219\n"
      "v_exp_f32 v220, v220\n"
      "v_exp_f32 v221, v221\n"
      "v_exp_f32 v222, v222\n"
      "v_exp_f32 v223, v223\n"
      "v_exp_f32 v192, v192\n"
      "v_exp_f32 v193, v193\n"
      "v_smfmac_f32_32x32x32_bf16 v[224:239], v[108:111], v[216:223], v249\n"
      "v_exp_f32 v194, v194\n"
      "v_exp_f32 v195, v195\n"
      "v_exp_f32 v196, v196\n"
      "v_mfma_f32_32x32x16_bf16 v[208:223], v[28:31], v[240:243], -4.0\n"
      "v_exp_f32 v197, v197\n"
      "v_exp_f32 v198, v198\n"
      "v_exp_f32 v199, v199\n"
      "v_exp_f32 v200, v200\n"
      "v_exp_f32 v201, v201\n"
      "v_smfmac_f32_32x32x32_bf16 v[224:239], v[112:115], v[192:199], v249\n"
      "v_exp_f32 v202, v202\n"
      "v_exp_f32 v203, v203\n"
      "v_exp_f32 v204, v204\n"
      "v_exp_f32 v205, v205\n"
      "v_exp_f32 v206, v206\n"
      "v_exp_f32 v207, v207\n"
      "v_exp_f32 v208, v208\n"
      "v_exp_f32 v209, v209\n"
      "v_smfmac_f32_32x32x32_bf16 v[224:239], v[116:119], v[200:207], v249\n"
      "v_exp_f32 v210, v210\n"
      "v_exp_f32 v211, v211\n"
      "v_exp_f32 v212, v212\n"
      "v_mfma_f32_32x32x16_bf16 v[192:207], v[32:35], v[240:243], -4.0\n"
      "v_exp_f32 v213, v213\n"
      "v_exp_f32 v214, v214\n"
      "v_exp_f32 v215, v215\n"
      "v_exp_f32 v216, v216\n"
      "v_exp_f32 v217, v217\n"
      "v_smfmac_f32_32x32x32_bf16 v[224:239], v[120:123], v[208:215], v249\n"
      "v_exp_f32 v218, v218\n"
      "v_exp_f32 v219, v219\n"
      "v_exp_f32 v220, v220\n"
      "v_exp_f32 v221, v221\n"
      "v_exp_f32 v222, v222\n"
      "v_exp_f32 v223, v223\n"
      "v_exp_f32 v192, v192\n"
      "v_exp_f32 v193, v193\n"
      "v_smfmac_f32_32x32x32_bf16 v[224:239], v[124:127], v[216:223], v249\n"
      "v_exp_f32 v194, v194\n"
      "v_exp_f32 v195, v195\n"
      "v_exp_f32 v196, v196\n"
      "v_mfma_f32_32x32x16_bf16 v[208:223], v[36:39], v[240:243], -4.0\n"
      "v_exp_f32 v197, v197\n"
      "v_exp_f32 v198, v198\n"
      "v_exp_f32 v199, v199\n"
      "v_exp_f32 v200, v200\n"
      "v_exp_f32 v201, v201\n"
      "v_smfmac_f32_32x32x32_bf16 v[224:239], v[128:131], v[192:199], v249\n"
      "v_exp_f32 v202, v202\n"
      "v_exp_f32 v203, v203\n"
      "v_exp_f32 v204, v204\n"
      "v_exp_f32 v205, v205\n"
      "v_exp_f32 v206, v206\n"
      "v_exp_f32 v207, v207\n"
      "v_exp_f32 v208, v208\n"
      "v_exp_f32 v209, v209\n"
      "v_smfmac_f32_32x32x32_bf16 v[224:239], v[132:135], v[200:207], v249\n"
      "v_exp_f32 v210, v210\n"
      "v_exp_f32 v211, v211\n"
      "v_exp_f32 v212, v212\n"
      "v_mfma_f32_32x32x16_bf16 v[192:207], v[40:43], v[240:243], -4.0\n"
      "v_exp_f32 v213, v213\n"
      "v_exp_f32 v214, v214\n"
      "v_exp_f32 v215, v215\n"
      "v_exp_f32 v216, v216\n"
      "v_exp_f32 v217, v217\n"
      "v_smfmac_f32_32x32x32_bf16 v[224:239], v[136:139], v[208:215], v249\n"
      "v_exp_f32 v218, v218\n"
      "v_exp_f32 v219, v219\n"
      "v_exp_f32 v220, v220\n"
      "v_exp_f32 v221, v221\n"
      "v_exp_f32 v222, v222\n"
      "v_exp_f32 v223, v223\n"
      "v_exp_f32 v192, v192\n"
      "v_exp_f32 v193, v193\n"
      "v_smfmac_f32_32x32x32_bf16 v[224:239], v[140:143], v[216:223], v249\n"
      "v_exp_f32 v194, v194\n"
      "v_exp_f32 v195, v195\n"
      "v_exp_f32 v196, v196\n"
      "v_mfma_f32_32x32x16_bf16 v[208:223], v[44:47], v[240:243], -4.0\n"
      "v_exp_f32 v197, v197\n"
      "v_exp_f32 v198, v198\n"
      "v_exp_f32 v199, v199\n"
      "v_exp_f32 v200, v200\n"
      "v_exp_f32 v201, v201\n"
      "v_smfmac_f32_32x32x32_bf16 v[224:239], v[144:147], v[192:199], v249\n"
      "v_exp_f32 v202, v202\n"
      "v_exp_f32 v203, v203\n"
      "v_exp_f32 v204, v204\n"
      "v_exp_f32 v205, v205\n"
      "v_exp_f32 v206, v206\n"
      "v_exp_f32 v207, v207\n"
      "v_exp_f32 v208, v208\n"
      "v_exp_f32 v209, v209\n"
      "v_smfmac_f32_32x32x32_bf16 v[224:239], v[148:151], v[200:207], v249\n"
      "v_exp_f32 v210, v210\n"
      "v_exp_f32 v211, v211\n"
      "v_exp_f32 v212, v212\n"
      "v_mfma_f32_32x32x16_bf16 v[192:207], v[48:51], v[240:243], -4.0\n"
      "v_exp_f32 v213, v213\n"
      "v_exp_f32 v214, v214\n"
      "v_exp_f32 v215, v215\n"
      "v_exp_f32 v216, v216\n"
      "v_exp_f32 v217, v217\n"
      "v_smfmac_f32_32x32x32_bf16 v[224:239], v[152:155], v[208:215], v249\n"
      "v_exp_f32 v218, v218\n"
      "v_exp_f32 v219, v219\n"
      "v_exp_f32 v220, v220\n"
      "v_exp_f32 v221, v221\n"
      "v_exp_f32 v222, v222\n"
      "v_exp_f32 v223, v223\n"
      "v_exp_f32 v192, v192\n"
      "v_exp_f32 v193, v193\n"
      "v_smfmac_f32_32x32x32_bf16 v[224:239], v[156:159], v[216:223], v249\n"
      "v_exp_f32 v194, v194\n"
      "v_exp_f32 v195, v195\n"
      "v_exp_f32 v196, v196\n"
      "v_mfma_f32_32x32x16_bf16 v[208:223], v[52:55], v[240:243], -4.0\n"
      "v_exp_f32 v197, v197\n"
      "v_exp_f32 v198, v198\n"
      "v_exp_f32 v199, v199\n"
      "v_exp_f32 v200, v200\n"
      "v_exp_f32 v201, v201\n"
      "v_smfmac_f32_32x32x32_bf16 v[224:239], v[160:163], v[192:199], v249\n"
      "v_exp_f32 v202, v202\n"
      "v_exp_f32 v203, v203\n"
      "v_exp_f32 v204, v204\n"
      "v_exp_f32 v205, v205\n"
      "v_exp_f32 v206, v206\n"
      "v_exp_f32 v207, v207\n"
      "v_exp_f32 v208, v208\n"
      "v_exp_f32 v209, v209\n"
      "v_smfmac_f32_32x32x32_bf16 v[224:239], v[164:167], v[200:207], v249\n"
      "v_exp_f32 v210, v210\n"
      "v_exp_f32 v211, v211\n"
      "v_exp_f32 v212, v212\n"
      "v_mfma_f32_32x32x16_bf16 v[192:207], v[56:59], v[240:243], -4.0\n"
      "v_exp_f32 v213, v213\n"
      "v_exp_f32 v214, v214\n"
      "v_exp_f32 v215, v215\n"
      "v_exp_f32 v216, v216\n"
      "v_exp_f32 v217, v217\n"
      "v_smfmac_f32_32x32x32_bf16 v[224:239], v[168:171], v[208:215], v249\n"
      "v_exp_f32 v218, v218\n"
      "v_exp_f32 v219, v219\n"
      "v_exp_f32 v220, v220\n"
      "v_exp_f32 v221, v221\n"
      "v_exp_f32 v222, v222\n"
      "v_exp_f32 v223, v223\n"
      "v_exp_f32 v192, v192\n"
      "v_exp_f32 v193, v193\n"
      "v_smfmac_f32_32x32x32_bf16 v[224:239], v[172:175], v[216:223], v249\n"
      "v_exp_f32 v194, v194\n"
      "v_exp_f32 v195, v195\n"
      "v_exp_f32 v196, v196\n"
      "v_mfma_f32_32x32x16_bf16 v[208:223], v[60:63], v[240:243], -4.0\n"
      "v_exp_f32 v197, v197\n"
      "ds_read_b128 v[240:243], v247 offset:6144\n"
      "v_exp_f32 v198, v198\n"
      "v_exp_f32 v199, v199\n"
      "v_exp_f32 v200, v200\n"
      "v_exp_f32 v201, v201\n"
      "v_smfmac_f32_32x32x32_bf16 v[224:239], v[176:179], v[192:199], v249\n"
      "v_exp_f32 v202, v202\n"
      "v_exp_f32 v203, v203\n"
      "v_exp_f32 v204, v204\n"
      "v_exp_f32 v205, v205\n"
      "v_exp_f32 v206, v206\n"
      "v_exp_f32 v207, v207\n"
      "v_exp_f32 v208, v208\n"
      "v_exp_f32 v209, v209\n"
      "v_smfmac_f32_32x32x32_bf16 v[224:239], v[180:183], v[200:207], v249\n"
      "v_exp_f32 v210, v210\n"
      "v_exp_f32 v211, v211\n"
      "v_exp_f32 v212, v212\n"
      "s_waitcnt lgkmcnt(0)\n"
      "v_mfma_f32_32x32x16_bf16 v[192:207], v[0:3], v[240:243], -4.0\n"
      "v_exp_f32 v213, v213\n"
      "v_exp_f32 v214, v214\n"
      "v_exp_f32 v215, v215\n"
      "v_exp_f32 v216, v216\n"
      "v_exp_f32 v217, v217\n"
      "v_smfmac_f32_32x32x32_bf16 v[224:239], v[184:187], v[208:215], v249\n"
      "v_exp_f32 v218, v218\n"
      "v_exp_f32 v219, v219\n"
      "v_exp_f32 v220, v220\n"
      "v_exp_f32 v221, v221\n"
      "v_exp_f32 v222, v222\n"
      "v_exp_f32 v223, v223\n"
      "v_exp_f32 v192, v192\n"
      "v_exp_f32 v193, v193\n"
      "v_smfmac_f32_32x32x32_bf16 v[224:239], v[188:191], v[216:223], v249\n"
      "v_exp_f32 v194, v194\n"
      "v_exp_f32 v195, v195\n"
      "v_exp_f32 v196, v196\n"
      "v_mfma_f32_32x32x16_bf16 v[208:223], v[4:7], v[240:243], -4.0\n"
      "v_exp_f32 v197, v197\n"
      "v_exp_f32 v198, v198\n"
      "v_exp_f32 v199, v199\n"
      "v_exp_f32 v200, v200\n"
      "v_exp_f32 v201, v201\n"
      "v_exp_f32 v202, v202\n"
      "v_exp_f32 v203, v203\n"
      "v_exp_f32 v204, v204\n"
      "ds_write_addtid_b32 v224 offset:62464\n"
      "ds_write_addtid_b32 v225 offset:62720\n"
      "ds_write_addtid_b32 v226 offset:62976\n"
      "ds_write_addtid_b32 v227 offset:63232\n"
      "ds_write_addtid_b32 v228 offset:63488\n"
      "ds_write_addtid_b32 v229 offset:63744\n"
      "ds_write_addtid_b32 v230 offset:64000\n"
      "ds_write_addtid_b32 v231 offset:64256\n"
      "ds_write_addtid_b32 v232 offset:64512\n"
      "ds_write_b32 v248, v252 offset:336\n"
      "v_mov_b64 v[224:225], 0\n"
      "v_mov_b64 v[226:227], 0\n"
      "v_mov_b64 v[228:229], 0\n"
      "v_mov_b64 v[230:231], 0\n"
      "v_mov_b64 v[232:233], 0\n"
      "v_exp_f32 v205, v205\n"
      "v_exp_f32 v206, v206\n"
      "v_smfmac_f32_32x32x32_bf16 v[224:239], v[64:67], v[192:199], v249\n"
      "v_exp_f32 v207, v207\n"
      "v_exp_f32 v208, v208\n"
      "v_exp_f32 v209, v209\n"
      "v_smfmac_f32_32x32x32_bf16 v[224:239], v[68:71], v[200:207], v249\n"
      "v_exp_f32 v210, v210\n"
      "v_exp_f32 v211, v211\n"
      "v_exp_f32 v212, v212\n"
      "v_mfma_f32_32x32x16_bf16 v[192:207], v[8:11], v[240:243], -4.0\n"
      "v_exp_f32 v213, v213\n"
      "v_exp_f32 v214, v214\n"
      "v_exp_f32 v215, v215\n"
      "v_exp_f32 v216, v216\n"
      "v_exp_f32 v217, v217\n"
      "v_smfmac_f32_32x32x32_bf16 v[224:239], v[72:75], v[208:215], v249\n"
      "v_exp_f32 v218, v218\n"
      "v_exp_f32 v219, v219\n"
      "v_exp_f32 v220, v220\n"
      "v_exp_f32 v221, v221\n"
      "v_exp_f32 v222, v222\n"
      "v_exp_f32 v223, v223\n"
      "v_exp_f32 v192, v192\n"
      "v_exp_f32 v193, v193\n"
      "v_smfmac_f32_32x32x32_bf16 v[224:239], v[76:79], v[216:223], v249\n"
      "v_exp_f32 v194, v194\n"
      "v_exp_f32 v195, v195\n"
      "v_exp_f32 v196, v196\n"
      "v_mfma_f32_32x32x16_bf16 v[208:223], v[12:15], v[240:243], -4.0\n"
      "v_exp_f32 v197, v197\n"
      "v_exp_f32 v198, v198\n"
      "v_exp_f32 v199, v199\n"
      "v_exp_f32 v200, v200\n"
      "v_exp_f32 v201, v201\n"
      "v_smfmac_f32_32x32x32_bf16 v[224:239], v[80:83], v[192:199], v249\n"
      "v_exp_f32 v202, v202\n"
      "v_exp_f32 v203, v203\n"
      "v_exp_f32 v204, v204\n"
      "v_exp_f32 v205, v205\n"
      "v_exp_f32 v206, v206\n"
      "v_exp_f32 v207, v207\n"
      "v_exp_f32 v208, v208\n"
      "v_exp_f32 v209, v209\n"
      "v_smfmac_f32_32x32x32_bf16 v[224:239], v[84:87], v[200:207], v249\n"
      "v_exp_f32 v210, v210\n"
      "v_exp_f32 v211, v211\n"
      "v_exp_f32 v212, v212\n"
      "v_mfma_f32_32x32x16_bf16 v[192:207], v[16:19], v[240:243], -4.0\n"
      "v_exp_f32 v213, v213\n"
      "v_exp_f32 v214, v214\n"
      "v_exp_f32 v215, v215\n"
      "v_exp_f32 v216, v216\n"
      "v_exp_f32 v217, v217\n"
      "v_smfmac_f32_32x32x32_bf16 v[224:239], v[88:91], v[208:215], v249\n"
      "v_exp_f32 v218, v218\n"
      "v_exp_f32 v219, v219\n"
      "v_exp_f32 v220, v220\n"
      "v_exp_f32 v221, v221\n"
      "v_exp_f32 v222, v222\n"
      "v_exp_f32 v223, v223\n"
      "v_exp_f32 v192, v192\n"
      "v_exp_f32 v193, v193\n"
      "v_smfmac_f32_32x32x32_bf16 v[224:239], v[92:95], v[216:223], v249\n"
      "v_exp_f32 v194, v194\n"
      "v_exp_f32 v195, v195\n"
      "v_exp_f32 v196, v196\n"
      "v_mfma_f32_32x32x16_bf16 v[208:223], v[20:23], v[240:243], -4.0\n"
      "v_exp_f32 v197, v197\n"
      "v_exp_f32 v198, v198\n"
      "v_exp_f32 v199, v199\n"
      "v_exp_f32 v200, v200\n"
      "v_exp_f32 v201, v201\n"
      "v_smfmac_f32_32x32x32_bf16 v[224:239], v[96:99], v[192:199], v249\n"
      "v_exp_f32 v202, v202\n"
      "v_exp_f32 v203, v203\n"
      "v_exp_f32 v204, v204\n"
      "v_exp_f32 v205, v205\n"
      "v_exp_f32 v206, v206\n"
      "v_exp_f32 v207, v207\n"
      "v_exp_f32 v208, v208\n"
      "v_exp_f32 v209, v209\n"
      "v_smfmac_f32_32x32x32_bf16 v[224:239], v[100:103], v[200:207], v249\n"
      "v_exp_f32 v210, v210\n"
      "v_exp_f32 v211, v211\n"
      "v_exp_f32 v212, v212\n"
      "v_mfma_f32_32x32x16_bf16 v[192:207], v[24:27], v[240:243], -4.0\n"
      "v_exp_f32 v213, v213\n"
      "v_exp_f32 v214, v214\n"
      "v_exp_f32 v215, v215\n"
      "v_exp_f32 v216, v216\n"
      "v_exp_f32 v217, v217\n"
      "v_smfmac_f32_32x32x32_bf16 v[224:239], v[104:107], v[208:215], v249\n"
      "v_exp_f32 v218, v218\n"
      "v_exp_f32 v219, v219\n"
      "v_exp_f32 v220, v220\n"
      "v_exp_f32 v221, v221\n"
      "v_exp_f32 v222, v222\n"
      "v_exp_f32 v223, v223\n"
      "v_exp_f32 v192, v192\n"
      "v_exp_f32 v193, v193\n"
      "v_smfmac_f32_32x32x32_bf16 v[224:239], v[108:111], v[216:223], v249\n"
      "v_exp_f32 v194, v194\n"
      "v_exp_f32 v195, v195\n"
      "v_exp_f32 v196, v196\n"
      "v_mfma_f32_32x32x16_bf16 v[208:223], v[28:31], v[240:243], -4.0\n"
      "v_exp_f32 v197, v197\n"
      "v_exp_f32 v198, v198\n"
      "v_exp_f32 v199, v199\n"
      "v_exp_f32 v200, v200\n"
      "v_exp_f32 v201, v201\n"
      "v_smfmac_f32_32x32x32_bf16 v[224:239], v[112:115], v[192:199], v249\n"
      "v_exp_f32 v202, v202\n"
      "v_exp_f32 v203, v203\n"
      "v_exp_f32 v204, v204\n"
      "v_exp_f32 v205, v205\n"
      "v_exp_f32 v206, v206\n"
      "v_exp_f32 v207, v207\n"
      "v_exp_f32 v208, v208\n"
      "v_exp_f32 v209, v209\n"
      "v_smfmac_f32_32x32x32_bf16 v[224:239], v[116:119], v[200:207], v249\n"
      "v_exp_f32 v210, v210\n"
      "v_exp_f32 v211, v211\n"
      "v_exp_f32 v212, v212\n"
      "v_mfma_f32_32x32x16_bf16 v[192:207], v[32:35], v[240:243], -4.0\n"
      "v_exp_f32 v213, v213\n"
      "v_exp_f32 v214, v214\n"
      "v_exp_f32 v215, v215\n"
      "v_exp_f32 v216, v216\n"
      "v_exp_f32 v217, v217\n"
      "v_smfmac_f32_32x32x32_bf16 v[224:239], v[120:123], v[208:215], v249\n"
      "v_exp_f32 v218, v218\n"
      "v_exp_f32 v219, v219\n"
      "v_exp_f32 v220, v220\n"
      "v_exp_f32 v221, v221\n"
      "v_exp_f32 v222, v222\n"
      "v_exp_f32 v223, v223\n"
      "v_exp_f32 v192, v192\n"
      "v_exp_f32 v193, v193\n"
      "v_smfmac_f32_32x32x32_bf16 v[224:239], v[124:127], v[216:223], v249\n"
      "v_exp_f32 v194, v194\n"
      "v_exp_f32 v195, v195\n"
      "v_exp_f32 v196, v196\n"
      "v_mfma_f32_32x32x16_bf16 v[208:223], v[36:39], v[240:243], -4.0\n"
      "v_exp_f32 v197, v197\n"
      "v_exp_f32 v198, v198\n"
      "v_exp_f32 v199, v199\n"
      "v_exp_f32 v200, v200\n"
      "v_exp_f32 v201, v201\n"
      "v_smfmac_f32_32x32x32_bf16 v[224:239], v[128:131], v[192:199], v249\n"
      "v_exp_f32 v202, v202\n"
      "v_exp_f32 v203, v203\n"
      "v_exp_f32 v204, v204\n"
      "v_exp_f32 v205, v205\n"
      "v_exp_f32 v206, v206\n"
      "v_exp_f32 v207, v207\n"
      "v_exp_f32 v208, v208\n"
      "v_exp_f32 v209, v209\n"
      "v_smfmac_f32_32x32x32_bf16 v[224:239], v[132:135], v[200:207], v249\n"
      "v_exp_f32 v210, v210\n"
      "v_exp_f32 v211, v211\n"
      "v_exp_f32 v212, v212\n"
      "v_mfma_f32_32x32x16_bf16 v[192:207], v[40:43], v[240:243], -4.0\n"
      "v_exp_f32 v213, v213\n"
      "v_exp_f32 v214, v214\n"
      "v_exp_f32 v215, v215\n"
      "v_exp_f32 v216, v216\n"
      "v_exp_f32 v217, v217\n"
      "v_smfmac_f32_32x32x32_bf16 v[224:239], v[136:139], v[208:215], v249\n"
      "v_exp_f32 v218, v218\n"
      "v_exp_f32 v219, v219\n"
      "v_exp_f32 v220, v220\n"
      "v_exp_f32 v221, v221\n"
      "v_exp_f32 v222, v222\n"
      "v_exp_f32 v223, v223\n"
      "v_exp_f32 v192, v192\n"
      "v_exp_f32 v193, v193\n"
      "v_smfmac_f32_32x32x32_bf16 v[224:239], v[140:143], v[216:223], v249\n"
      "v_exp_f32 v194, v194\n"
      "v_exp_f32 v195, v195\n"
      "v_exp_f32 v196, v196\n"
      "v_mfma_f32_32x32x16_bf16 v[208:223], v[44:47], v[240:243], -4.0\n"
      "v_exp_f32 v197, v197\n"
      "v_exp_f32 v198, v198\n"
      "v_exp_f32 v199, v199\n"
      "v_exp_f32 v200, v200\n"
      "v_exp_f32 v201, v201\n"
      "v_smfmac_f32_32x32x32_bf16 v[224:239], v[144:147], v[192:199], v249\n"
      "v_exp_f32 v202, v202\n"
      "v_exp_f32 v203, v203\n"
      "v_exp_f32 v204, v204\n"
      "v_exp_f32 v205, v205\n"
      "v_exp_f32 v206, v206\n"
      "v_exp_f32 v207, v207\n"
      "v_exp_f32 v208, v208\n"
      "v_exp_f32 v209, v209\n"
      "v_smfmac_f32_32x32x32_bf16 v[224:239], v[148:151], v[200:207], v249\n"
      "v_exp_f32 v210, v210\n"
      "v_exp_f32 v211, v211\n"
      "v_exp_f32 v212, v212\n"
      "v_mfma_f32_32x32x16_bf16 v[192:207], v[48:51], v[240:243], -4.0\n"
      "v_exp_f32 v213, v213\n"
      "v_exp_f32 v214, v214\n"
      "v_exp_f32 v215, v215\n"
      "v_exp_f32 v216, v216\n"
      "v_exp_f32 v217, v217\n"
      "v_smfmac_f32_32x32x32_bf16 v[224:239], v[152:155], v[208:215], v249\n"
      "v_exp_f32 v218, v218\n"
      "v_exp_f32 v219, v219\n"
      "v_exp_f32 v220, v220\n"
      "v_exp_f32 v221, v221\n"
      "v_exp_f32 v222, v222\n"
      "v_exp_f32 v223, v223\n"
      "v_exp_f32 v192, v192\n"
      "v_exp_f32 v193, v193\n"
      "v_smfmac_f32_32x32x32_bf16 v[224:239], v[156:159], v[216:223], v249\n"
      "v_exp_f32 v194, v194\n"
      "v_exp_f32 v195, v195\n"
      "v_exp_f32 v196, v196\n"
      "v_mfma_f32_32x32x16_bf16 v[208:223], v[52:55], v[240:243], -4.0\n"
      "v_exp_f32 v197, v197\n"
      "v_exp_f32 v198, v198\n"
      "v_exp_f32 v199, v199\n"
      "v_exp_f32 v200, v200\n"
      "v_exp_f32 v201, v201\n"
      "v_smfmac_f32_32x32x32_bf16 v[224:239], v[160:163], v[192:199], v249\n"
      "v_exp_f32 v202, v202\n"
      "v_exp_f32 v203, v203\n"
      "v_exp_f32 v204, v204\n"
      "v_exp_f32 v205, v205\n"
      "v_exp_f32 v206, v206\n"
      "v_exp_f32 v207, v207\n"
      "v_exp_f32 v208, v208\n"
      "v_exp_f32 v209, v209\n"
      "v_smfmac_f32_32x32x32_bf16 v[224:239], v[164:167], v[200:207], v249\n"
      "v_exp_f32 v210, v210\n"
      "v_exp_f32 v211, v211\n"
      "v_exp_f32 v212, v212\n"
      "v_mfma_f32_32x32x16_bf16 v[192:207], v[56:59], v[240:243], -4.0\n"
      "v_exp_f32 v213, v213\n"
      "v_exp_f32 v214, v214\n"
      "v_exp_f32 v215, v215\n"
      "v_exp_f32 v216, v216\n"
      "v_exp_f32 v217, v217\n"
      "v_smfmac_f32_32x32x32_bf16 v[224:239], v[168:171], v[208:215], v249\n"
      "v_exp_f32 v218, v218\n"
      "v_exp_f32 v219, v219\n"
      "v_exp_f32 v220, v220\n"
      "v_exp_f32 v221, v221\n"
      "v_exp_f32 v222, v222\n"
      "v_exp_f32 v223, v223\n"
      "v_exp_f32 v192, v192\n"
      "v_exp_f32 v193, v193\n"
      "v_smfmac_f32_32x32x32_bf16 v[224:239], v[172:175], v[216:223], v249\n"
      "v_exp_f32 v194, v194\n"
      "v_exp_f32 v195, v195\n"
      "v_exp_f32 v196, v196\n"
      "v_mfma_f32_32x32x16_bf16 v[208:223], v[60:63], v[240:243], -4.0\n"
      "v_exp_f32 v197, v197\n"
      "ds_read_b32 v255, v251 offset:192\n"
      "ds_read_b128 v[240:243], v247 offset:7168\n"
      "v_exp_f32 v198, v198\n"
      "v_exp_f32 v199, v199\n"
      "v_exp_f32 v200, v200\n"
      "v_exp_f32 v201, v201\n"
      "v_smfmac_f32_32x32x32_bf16 v[224:239], v[176:179], v[192:199], v249\n"
      "v_exp_f32 v202, v202\n"
      "v_exp_f32 v203, v203\n"
      "v_exp_f32 v204, v204\n"
      "v_exp_f32 v205, v205\n"
      "v_exp_f32 v206, v206\n"
      "v_exp_f32 v207, v207\n"
      "v_exp_f32 v208, v208\n"
      "v_exp_f32 v209, v209\n"
      "v_smfmac_f32_32x32x32_bf16 v[224:239], v[180:183], v[200:207], v249\n"
      "v_exp_f32 v210, v210\n"
      "v_exp_f32 v211, v211\n"
      "v_exp_f32 v212, v212\n"
      "s_waitcnt lgkmcnt(0)\n"
      "v_mfma_f32_32x32x16_bf16 v[192:207], v[0:3], v[240:243], -4.0\n"
      "v_exp_f32 v213, v213\n"
      "v_cmp_eq_u32 vcc, 0, v255\n"
      "v_exp_f32 v214, v214\n"
      "v_exp_f32 v215, v215\n"
      "v_exp_f32 v216, v216\n"
      "v_exp_f32 v217, v217\n"
      "v_smfmac_f32_32x32x32_bf16 v[224:239], v[184:187], v[208:215], v249\n"
      "v_exp_f32 v218, v218\n"
      "v_exp_f32 v219, v219\n"
      "v_exp_f32 v220, v220\n"
      "v_exp_f32 v221, v221\n"
      "v_exp_f32 v222, v222\n"
      "v_exp_f32 v223, v223\n"
      "s_cbranch_vccnz LGUARD\n"
      "LGUARD2:\n"
      "v_exp_f32 v192, v192\n"
      "v_exp_f32 v193, v193\n"
      "v_smfmac_f32_32x32x32_bf16 v[224:239], v[188:191], v[216:223], v249\n"
      "v_exp_f32 v194, v194\n"
      "v_exp_f32 v195, v195\n"
      "v_exp_f32 v196, v196\n"
      "v_mfma_f32_32x32x16_bf16 v[208:223], v[4:7], v[240:243], -4.0\n"
      "v_exp_f32 v197, v197\n"
      "v_exp_f32 v198, v198\n"
      "v_exp_f32 v199, v199\n"
      "v_exp_f32 v200, v200\n"
      "v_exp_f32 v201, v201\n"
      "v_exp_f32 v202, v202\n"
      "v_exp_f32 v203, v203\n"
      "v_exp_f32 v204, v204\n"
      "ds_write_addtid_b32 v224 offset:16384\n"
      "ds_write_addtid_b32 v225 offset:16640\n"
      "ds_write_addtid_b32 v226 offset:16896\n"
      "ds_write_addtid_b32 v227 offset:17152\n"
      "ds_write_addtid_b32 v228 offset:17408\n"
      "ds_write_addtid_b32 v229 offset:17664\n"
      "ds_write_addtid_b32 v230 offset:17920\n"
      "ds_write_addtid_b32 v231 offset:18176\n"
      "ds_write_addtid_b32 v232 offset:18432\n"
      "ds_write_b32 v248, v252 offset:352\n"
      "v_mov_b64 v[224:225], 0\n"
      "v_mov_b64 v[226:227], 0\n"
      "v_mov_b64 v[228:229], 0\n"
      "v_mov_b64 v[230:231], 0\n"
      "v_mov_b64 v[232:233], 0\n"
      "v_exp_f32 v205, v205\n"
      "v_exp_f32 v206, v206\n"
      "v_smfmac_f32_32x32x32_bf16 v[224:239], v[64:67], v[192:199], v249\n"
      "v_exp_f32 v207, v207\n"
      "v_exp_f32 v208, v208\n"
      "v_exp_f32 v209, v209\n"
      "v_smfmac_f32_32x32x32_bf16 v[224:239], v[68:71], v[200:207], v249\n"
      "v_exp_f32 v210, v210\n"
      "v_exp_f32 v211, v211\n"
      "v_exp_f32 v212, v212\n"
      "v_mfma_f32_32x32x16_bf16 v[192:207], v[8:11], v[240:243], -4.0\n"
      "v_exp_f32 v213, v213\n"
      "v_exp_f32 v214, v214\n"
      "v_exp_f32 v215, v215\n"
      "v_exp_f32 v216, v216\n"
      "v_exp_f32 v217, v217\n"
      "v_smfmac_f32_32x32x32_bf16 v[224:239], v[72:75], v[208:215], v249\n"
      "v_exp_f32 v218, v218\n"
      "v_exp_f32 v219, v219\n"
      "v_exp_f32 v220, v220\n"
      "v_exp_f32 v221, v221\n"
      "v_exp_f32 v222, v222\n"
      "v_exp_f32 v223, v223\n"
      "v_exp_f32 v192, v192\n"
      "v_exp_f32 v193, v193\n"
      "v_smfmac_f32_32x32x32_bf16 v[224:239], v[76:79], v[216:223], v249\n"
      "v_exp_f32 v194, v194\n"
      "v_exp_f32 v195, v195\n"
      "v_exp_f32 v196, v196\n"
      "v_mfma_f32_32x32x16_bf16 v[208:223], v[12:15], v[240:243], -4.0\n"
      "v_exp_f32 v197, v197\n"
      "v_exp_f32 v198, v198\n"
      "v_exp_f32 v199, v199\n"
      "v_exp_f32 v200, v200\n"
      "v_exp_f32 v201, v201\n"
      "v_smfmac_f32_32x32x32_bf16 v[224:239], v[80:83], v[192:199], v249\n"
      "v_exp_f32 v202, v202\n"
      "v_exp_f32 v203, v203\n"
      "v_exp_f32 v204, v204\n"
      "v_exp_f32 v205, v205\n"
      "v_exp_f32 v206, v206\n"
      "v_exp_f32 v207, v207\n"
      "v_exp_f32 v208, v208\n"
      "v_exp_f32 v209, v209\n"
      "v_smfmac_f32_32x32x32_bf16 v[224:239], v[84:87], v[200:207], v249\n"
      "v_exp_f32 v210, v210\n"
      "v_exp_f32 v211, v211\n"
      "v_exp_f32 v212, v212\n"
      "v_mfma_f32_32x32x16_bf16 v[192:207], v[16:19], v[240:243], -4.0\n"
      "v_exp_f32 v213, v213\n"
      "v_exp_f32 v214, v214\n"
      "v_exp_f32 v215, v215\n"
      "v_exp_f32 v216, v216\n"
      "v_exp_f32 v217, v217\n"
      "v_smfmac_f32_32x32x32_bf16 v[224:239], v[88:91], v[208:215], v249\n"
      "v_exp_f32 v218, v218\n"
      "v_exp_f32 v219, v219\n"
      "v_exp_f32 v220, v220\n"
      "v_exp_f32 v221, v221\n"
      "v_exp_f32 v222, v222\n"
      "v_exp_f32 v223, v223\n"
      "v_exp_f32 v192, v192\n"
      "v_exp_f32 v193, v193\n"
      "v_smfmac_f32_32x32x32_bf16 v[224:239], v[92:95], v[216:223], v249\n"
      "v_exp_f32 v194, v194\n"
      "v_exp_f32 v195, v195\n"
      "v_exp_f32 v196, v196\n"
      "v_mfma_f32_32x32x16_bf16 v[208:223], v[20:23], v[240:243], -4.0\n"
      "v_exp_f32 v197, v197\n"
      "v_exp_f32 v198, v198\n"
      "v_exp_f32 v199, v199\n"
      "v_exp_f32 v200, v200\n"
      "v_exp_f32 v201, v201\n"
      "v_smfmac_f32_32x32x32_bf16 v[224:239], v[96:99], v[192:199], v249\n"
      "v_exp_f32 v202, v202\n"
      "v_exp_f32 v203, v203\n"
      "v_exp_f32 v204, v204\n"
      "v_exp_f32 v205, v205\n"
      "v_exp_f32 v206, v206\n"
      "v_exp_f32 v207, v207\n"
      "v_exp_f32 v208, v208\n"
      "v_exp_f32 v209, v209\n"
      "v_smfmac_f32_32x32x32_bf16 v[224:239], v[100:103], v[200:207], v249\n"
      "v_exp_f32 v210, v210\n"
      "v_exp_f32 v211, v211\n"
      "v_exp_f32 v212, v212\n"
      "v_mfma_f32_32x32x16_bf16 v[192:207], v[24:27], v[240:243], -4.0\n"
      "v_exp_f32 v213, v213\n"
      "v_exp_f32 v214, v214\n"
      "v_exp_f32 v215, v215\n"
      "v_exp_f32 v216, v216\n"
      "v_exp_f32 v217, v217\n"
      "v_smfmac_f32_32x32x32_bf16 v[224:239], v[104:107], v[208:215], v249\n"
      "v_exp_f32 v218, v218\n"
      "v_exp_f32 v219, v219\n"
      "v_exp_f32 v220, v220\n"
      "v_exp_f32 v221, v221\n"
      "v_exp_f32 v222, v222\n"
      "v_exp_f32 v223, v223\n"
      "v_exp_f32 v192, v192\n"
      "v_exp_f32 v193, v193\n"
      "v_smfmac_f32_32x32x32_bf16 v[224:239], v[108:111], v[216:223], v249\n"
      "v_exp_f32 v194, v194\n"
      "v_exp_f32 v195, v195\n"
      "v_exp_f32 v196, v196\n"
      "v_mfma_f32_32x32x16_bf16 v[208:223], v[28:31], v[240:243], -4.0\n"
      "v_exp_f32 v197, v197\n"
      "v_exp_f32 v198, v198\n"
      "v_exp_f32 v199, v199\n"
      "v_exp_f32 v200, v200\n"
      "v_exp_f32 v201, v201\n"
      "v_smfmac_f32_32x32x32_bf16 v[224:239], v[112:115], v[192:199], v249\n"
      "v_exp_f32 v202, v202\n"
      "v_exp_f32 v203, v203\n"
      "v_exp_f32 v204, v204\n"
      "v_exp_f32 v205, v205\n"
      "v_exp_f32 v206, v206\n"
      "v_exp_f32 v207, v207\n"
      "v_exp_f32 v208, v208\n"
      "v_exp_f32 v209, v209\n"
      "v_smfmac_f32_32x32x32_bf16 v[224:239], v[116:119], v[200:207], v249\n"
      "v_exp_f32 v210, v210\n"
      "v_exp_f32 v211, v211\n"
      "v_exp_f32 v212, v212\n"
      "v_mfma_f32_32x32x16_bf16 v[192:207], v[32:35], v[240:243], -4.0\n"
      "v_exp_f32 v213, v213\n"
      "v_exp_f32 v214, v214\n"
      "v_exp_f32 v215, v215\n"
      "v_exp_f32 v216, v216\n"
      "v_exp_f32 v217, v217\n"
      "v_smfmac_f32_32x32x32_bf16 v[224:239], v[120:123], v[208:215], v249\n"
      "v_exp_f32 v218, v218\n"
      "v_exp_f32 v219, v219\n"
      "v_exp_f32 v220, v220\n"
      "v_exp_f32 v221, v221\n"
      "v_exp_f32 v222, v222\n"
      "v_exp_f32 v223, v223\n"
      "v_exp_f32 v192, v192\n"
      "v_exp_f32 v193, v193\n"
      "v_smfmac_f32_32x32x32_bf16 v[224:239], v[124:127], v[216:223], v249\n"
      "v_exp_f32 v194, v194\n"
      "v_exp_f32 v195, v195\n"
      "v_exp_f32 v196, v196\n"
      "v_mfma_f32_32x32x16_bf16 v[208:223], v[36:39], v[240:243], -4.0\n"
      "v_exp_f32 v197, v197\n"
      "v_exp_f32 v198, v198\n"
      "v_exp_f32 v199, v199\n"
      "v_exp_f32 v200, v200\n"
      "v_exp_f32 v201, v201\n"
      "v_smfmac_f32_32x32x32_bf16 v[224:239], v[128:131], v[192:199], v249\n"
      "v_exp_f32 v202, v202\n"
      "v_exp_f32 v203, v203\n"
      "v_exp_f32 v204, v204\n"
      "v_exp_f32 v205, v205\n"
      "v_exp_f32 v206, v206\n"
      "v_exp_f32 v207, v207\n"
      "v_exp_f32 v208, v208\n"
      "v_exp_f32 v209, v209\n"
      "v_smfmac_f32_32x32x32_bf16 v[224:239], v[132:135], v[200:207], v249\n"
      "v_exp_f32 v210, v210\n"
      "v_exp_f32 v211, v211\n"
      "v_exp_f32 v212, v212\n"
      "v_mfma_f32_32x32x16_bf16 v[192:207], v[40:43], v[240:243], -4.0\n"
      "v_exp_f32 v213, v213\n"
      "v_exp_f32 v214, v214\n"
      "v_exp_f32 v215, v215\n"
      "v_exp_f32 v216, v216\n"
      "v_exp_f32 v217, v217\n"
      "v_smfmac_f32_32x32x32_bf16 v[224:239], v[136:139], v[208:215], v249\n"
      "v_exp_f32 v218, v218\n"
      "v_exp_f32 v219, v219\n"
      "v_exp_f32 v220, v220\n"
      "v_exp_f32 v221, v221\n"
      "v_exp_f32 v222, v222\n"
      "v_exp_f32 v223, v223\n"
      "v_exp_f32 v192, v192\n"
      "v_exp_f32 v193, v193\n"
      "v_smfmac_f32_32x32x32_bf16 v[224:239], v[140:143], v[216:223], v249\n"
      "v_exp_f32 v194, v194\n"
      "v_exp_f32 v195, v195\n"
      "v_exp_f32 v196, v196\n"
      "v_mfma_f32_32x32x16_bf16 v[208:223], v[44:47], v[240:243], -4.0\n"
      "v_exp_f32 v197, v197\n"
      "v_exp_f32 v198, v198\n"
      "v_exp_f32 v199, v199\n"
      "v_exp_f32 v200, v200\n"
      "v_exp_f32 v201, v201\n"
      "v_smfmac_f32_32x32x32_bf16 v[224:239], v[144:147], v[192:199], v249\n"
      "v_exp_f32 v202, v202\n"
      "v_exp_f32 v203, v203\n"
      "v_exp_f32 v204, v204\n"
      "v_exp_f32 v205, v205\n"
      "v_exp_f32 v206, v206\n"
      "v_exp_f32 v207, v207\n"
      "v_exp_f32 v208, v208\n"
      "v_exp_f32 v209, v209\n"
      "v_smfmac_f32_32x32x32_bf16 v[224:239], v[148:151], v[200:207], v249\n"
      "v_exp_f32 v210, v210\n"
      "v_exp_f32 v211, v211\n"
      "v_exp_f32 v212, v212\n"
      "v_mfma_f32_32x32x16_bf16 v[192:207], v[48:51], v[240:243], -4.0\n"
      "v_exp_f32 v213, v213\n"
      "v_exp_f32 v214, v214\n"
      "v_exp_f32 v215, v215\n"
      "v_exp_f32 v216, v216\n"
      "v_exp_f32 v217, v217\n"
      "v_smfmac_f32_32x32x32_bf16 v[224:239], v[152:155], v[208:215], v249\n"
      "v_exp_f32 v218, v218\n"
      "v_exp_f32 v219, v219\n"
      "v_exp_f32 v220, v220\n"
      "v_exp_f32 v221, v221\n"
      "v_exp_f32 v222, v222\n"
      "v_exp_f32 v223, v223\n"
      "v_exp_f32 v192, v192\n"
      "v_exp_f32 v193, v193\n"
      "v_smfmac_f32_32x32x32_bf16 v[224:239], v[156:159], v[216:223], v249\n"
      "v_exp_f32 v194, v194\n"
      "v_exp_f32 v195, v195\n"
      "v_exp_f32 v196, v196\n"
      "v_mfma_f32_32x32x16_bf16 v[208:223], v[52:55], v[240:243], -4.0\n"
      "v_exp_f32 v197, v197\n"
      "v_exp_f32 v198, v198\n"
      "v_exp_f32 v199, v199\n"
      "v_exp_f32 v200, v200\n"
      "v_exp_f32 v201, v201\n"
      "v_smfmac_f32_32x32x32_bf16 v[224:239], v[160:163], v[192:199], v249\n"
      "v_exp_f32 v202, v202\n"
      "v_exp_f32 v203, v203\n"
      "v_exp_f32 v204, v204\n"
      "v_exp_f32 v205, v205\n"
      "v_exp_f32 v206, v206\n"
      "v_exp_f32 v207, v207\n"
      "v_exp_f32 v208, v208\n"
      "v_exp_f32 v209, v209\n"
      "v_smfmac_f32_32x32x32_bf16 v[224:239], v[164:167], v[200:207], v249\n"
      "v_exp_f32 v210, v210\n"
      "v_exp_f32 v211, v211\n"
      "v_exp_f32 v212, v212\n"
      "v_mfma_f32_32x32x16_bf16 v[192:207], v[56:59], v[240:243], -4.0\n"
      "v_exp_f32 v213, v213\n"
      "v_exp_f32 v214, v214\n"
      "v_exp_f32 v215, v215\n"
      "v_exp_f32 v216, v216\n"
      "v_exp_f32 v217, v217\n"
      "v_smfmac_f32_32x32x32_bf16 v[224:239], v[168:171], v[208:215], v249\n"
      "v_exp_f32 v218, v218\n"
      "v_exp_f32 v219, v219\n"
      "v_exp_f32 v220, v220\n"
      "v_exp_f32 v221, v221\n"
      "v_exp_f32 v222, v222\n"
      "v_exp_f32 v223, v223\n"
      "v_exp_f32 v192, v192\n"
      "v_exp_f32 v193, v193\n"
      "v_smfmac_f32_32x32x32_bf16 v[224:239], v[172:175], v[216:223], v249\n"
      "v_exp_f32 v194, v194\n"
      "v_exp_f32 v195, v195\n"
      "v_exp_f32 v196, v196\n"
      "v_mfma_f32_32x32x16_bf16 v[208:223], v[60:63], v[240:243], -4.0\n"
      "v_exp_f32 v197, v197\n"
      "v_exp_f32 v198, v198\n"
      "v_exp_f32 v199, v199\n"
      "v_exp_f32 v200, v200\n"
      "v_exp_f32 v201, v201\n"
      "v_smfmac_f32_32x32x32_bf16 v[224:239], v[176:179], v[192:199], v249\n"
      "v_exp_f32 v202, v202\n"
      "v_exp_f32 v203, v203\n"
      "v_exp_f32 v204, v204\n"
      "v_exp_f32 v205, v205\n"
      "v_exp_f32 v206, v206\n"
      "v_exp_f32 v207, v207\n"
      "v_exp_f32 v208, v208\n"
      "v_exp_f32 v209, v209\n"
      "v_smfmac_f32_32x32x32_bf16 v[224:239], v[180:183], v[200:207], v249\n"
      "v_exp_f32 v210, v210\n"
      "v_exp_f32 v211, v211\n"
      "v_exp_f32 v212, v212\n"
      "v_exp_f32 v213, v213\n"
      "v_exp_f32 v214, v214\n"
      "v_exp_f32 v215, v215\n"
      "v_exp_f32 v216, v216\n"
      "v_exp_f32 v217, v217\n"
      "v_smfmac_f32_32x32x32_bf16 v[224:239], v[184:187], v[208:215], v249\n"
      "v_exp_f32 v218, v218\n"
      "v_exp_f32 v219, v219\n"
      "v_exp_f32 v220, v220\n"
      "v_exp_f32 v221, v221\n"
      "v_exp_f32 v222, v222\n"
      "v_exp_f32 v223, v223\n"
      "s_setprio 3\n"
      "s_nop 0\n"
      "v_smfmac_f32_32x32x32_bf16 v[224:239], v[188:191], v[216:223], v249\n"
      "LATT_END:\n"
      "v_add_u32 v202, s74, v246\n"
      "v_add_u32 v204, s74, v245\n"
      "v_add_u32 v203, 64, v202\n"
      "v_add_u32 v205, 64, v204\n"
      "v_lshlrev_b32 v247, 4, v244\n"
      "s_nop 6\n"
      "ds_write_addtid_b32 v224 offset:25600\n"
      "ds_write_addtid_b32 v225 offset:25856\n"
      "ds_write_addtid_b32 v226 offset:26112\n"
      "ds_write_addtid_b32 v227 offset:26368\n"
      "ds_write_addtid_b32 v228 offset:26624\n"
      "ds_write_addtid_b32 v229 offset:26880\n"
      "ds_write_addtid_b32 v230 offset:27136\n"
      "ds_write_addtid_b32 v231 offset:27392\n"
      "ds_write_addtid_b32 v232 offset:27648\n"
      "s_mov_b64 exec, 1\n"
      "ds_add_rtn_u32 v4, v254, v252 offset:176\n"
      "s_mov_b64 exec, -1\n"
      "s_waitcnt lgkmcnt(2)\n"
      "ds_read_b32 v200, v254 offset:80\n"
      "ds_read2st64_b32 v[192:193], v202 offset0:8 offset1:17\n"
      "ds_read2st64_b32 v[194:195], v202 offset0:26 offset1:35\n"
      "ds_read2st64_b32 v[196:197], v203 offset0:8 offset1:17\n"
      "ds_read2st64_b32 v[198:199], v203 offset0:26 offset1:35\n"
      "ds_read2st64_b32 v[160:161], v204 offset0:0 offset1:1\n"
      "ds_read2st64_b32 v[162:163], v204 offset0:2 offset1:3\n"
      "ds_read2st64_b32 v[164:165], v205 offset0:0 offset1:1\n"
      "ds_read2st64_b32 v[166:167], v205 offset0:2 offset1:3\n"
      "ds_read2st64_b32 v[168:169], v204 offset0:9 offset1:10\n"
      "ds_read2st64_b32 v[170:171], v204 offset0:11 offset1:12\n"
      "ds_read2st64_b32 v[172:173], v205 offset0:9 offset1:10\n"
      "ds_read2st64_b32 v[174:175], v205 offset0:11 offset1:12\n"
      "s_waitcnt lgkmcnt(13)\n"
      "v_readfirstlane_b32 s60, v4\n"
      "s_nop 0\n"
      "s_cmp_eq_u32 s60, 3\n"
      "s_cbranch_scc1 LRED3\n"
      "Lret_r7:\n"
      "s_barrier\n"
      "s_branch LEXIT\n"
      "LQA:\n"
      "v_and_b32 v225, 3, v244\n"
      "v_lshlrev_b32 v225, 2, v225\n"
      "v_add_u32 v225, 161792, v225\n"
      "s_mov_b32 s66, 0\n"
      "Lw4_90:\n"
      "ds_read_b32 v224, v225 offset:16\n"
      "s_waitcnt lgkmcnt(0)\n"
      "v_cmp_gt_u32 vcc, 1, v224\n"
      "s_cbranch_vccz Lw4d_91\n"
      "s_add_u32 s66, s66, 1\n"
      "s_cmp_lt_u32 s66, 131072\n"
      "s_cbranch_scc1 Lw4_90\n"
      "Lw4d_91:\n"
      "s_branch LQA2\n"
      "LGUARD:\n"
      "s_mov_b32 s66, 0\n"
      "Lw4_92:\n"
      "ds_read_b32 v255, v251 offset:192\n"
      "s_waitcnt lgkmcnt(0)\n"
      "v_cmp_gt_u32 vcc, 1, v255\n"
      "s_cbranch_vccz Lw4d_93\n"
      "s_sleep 1\n"
      "s_add_u32 s66, s66, 1\n"
      "s_cmp_lt_u32 s66, 131072\n"
      "s_cbranch_scc1 Lw4_92\n"
      "Lw4d_93:\n"
      "s_branch LGUARD2\n"
      "LSLOW:\n"
      "ds_write_b32 v248, v252 offset:80\n"
      "s_xor_b32 s67, s69, 12288\n"
      "buffer_load_dwordx4 v[24:27], v250, s[32:35], s67 offen sc1\n"
      "buffer_load_dwordx4 v[112:115], v250, s[32:35], s67 offen offset:1024 sc1\n"
      "s_xor_b32 s67, s69, 14336\n"
      "buffer_load_dwordx4 v[28:31], v250, s[32:35], s67 offen sc1\n"
      "buffer_load_dwordx4 v[120:123], v250, s[32:35], s67 offen offset:1024 sc1\n"
      "s_xor_b32 s67, s69, 16384\n"
      "buffer_load_dwordx4 v[32:35], v250, s[32:35], s67 offen sc1\n"
      "buffer_load_dwordx4 v[128:131], v250, s[32:35], s67 offen offset:1024 sc1\n"
      "s_xor_b32 s67, s69, 18432\n"
      "buffer_load_dwordx4 v[36:39], v250, s[32:35], s67 offen sc1\n"
      "buffer_load_dwordx4 v[136:139], v250, s[32:35], s67 offen offset:1024 sc1\n"
      "s_xor_b32 s67, s69, 20480\n"
      "buffer_load_dwordx4 v[40:43], v250, s[32:35], s67 offen sc1\n"
      "buffer_load_dwordx4 v[144:147], v250, s[32:35], s67 offen offset:1024 sc1\n"
      "s_xor_b32 s67, s69, 22528\n"
      "buffer_load_dwordx4 v[44:47], v250, s[32:35], s67 offen sc1\n"
      "buffer_load_dwordx4 v[152:155], v250, s[32:35], s67 offen offset:1024 sc1\n"
      "s_xor_b32 s67, s69, 24576\n"
      "buffer_load_dwordx4 v[48:51], v250, s[32:35], s67 offen sc1\n"
      "buffer_load_dwordx4 v[160:163], v250, s[32:35], s67 offen offset:1024 sc1\n"
      "s_xor_b32 s67, s69, 26624\n"
      "buffer_load_dwordx4 v[52:55], v250, s[32:35], s67 offen sc1\n"
      "buffer_load_dwordx4 v[168:171], v250, s[32:35], s67 offen offset:1024 sc1\n"
      "s_xor_b32 s67, s69, 28672\n"
      "buffer_load_dwordx4 v[56:59], v250, s[32:35], s67 offen sc1\n"
      "buffer_load_dwordx4 v[176:179], v250, s[32:35], s67 offen offset:1024 sc1\n"
      "s_xor_b32 s67, s69, 30720\n"
      "buffer_load_dwordx4 v[60:63], v250, s[32:35], s67 offen sc1\n"
      "buffer_load_dwordx4 v[184:187], v250, s[32:35], s67 offen offset:1024 sc1\n"
      "s_mul_i32 s59, s54, 2304\n"
      "s_add_u32 m0, s59, 12288\n"
      "s_mov_b32 s58, 0\n"
      "s_mov_b32 s98, 0\n"
      "s_mov_b32 s99, -1\n"
      "s_waitcnt vmcnt(0)\n"
      "v_permlane16_swap_b32 v64, v68\n"
      "v_permlane16_swap_b32 v65, v69\n"
      "v_permlane16_swap_b32 v66, v70\n"
      "v_permlane16_swap_b32 v67, v71\n"
      "v_permlane16_swap_b32 v72, v76\n"
      "v_permlane16_swap_b32 v73, v77\n"
      "v_permlane16_swap_b32 v74, v78\n"
      "v_permlane16_swap_b32 v75, v79\n"
      "v_permlane16_swap_b32 v80, v84\n"
      "v_permlane16_swap_b32 v81, v85\n"
      "v_permlane16_swap_b32 v82, v86\n"
      "v_permlane16_swap_b32 v83, v87\n"
      "v_permlane16_swap_b32 v88, v92\n"
      "v_permlane16_swap_b32 v89, v93\n"
      "v_permlane16_swap_b32 v90, v94\n"
      "v_permlane16_swap_b32 v91, v95\n"
      "v_permlane16_swap_b32 v96, v100\n"
      "v_permlane16_swap_b32 v97, v101\n"
      "v_permlane16_swap_b32 v98, v102\n"
      "v_permlane16_swap_b32 v99, v103\n"
      "v_permlane16_swap_b32 v104, v108\n"
      "v_permlane16_swap_b32 v105, v109\n"
      "v_permlane16_swap_b32 v106, v110\n"
      "v_permlane16_swap_b32 v107, v111\n"
      "v_permlane16_swap_b32 v112, v116\n"
      "v_permlane16_swap_b32 v113, v117\n"
      "v_permlane16_swap_b32 v114, v118\n"
      "v_permlane16_swap_b32 v115, v119\n"
      "v_permlane16_swap_b32 v120, v124\n"
      "v_permlane16_swap_b32 v121, v125\n"
      "v_permlane16_swap_b32 v122, v126\n"
      "v_permlane16_swap_b32 v123, v127\n"
      "v_permlane16_swap_b32 v128, v132\n"
      "v_permlane16_swap_b32 v129, v133\n"
      "v_permlane16_swap_b32 v130, v134\n"
      "v_permlane16_swap_b32 v131, v135\n"
      "v_permlane16_swap_b32 v136, v140\n"
      "v_permlane16_swap_b32 v137, v141\n"
      "v_permlane16_swap_b32 v138, v142\n"
      "v_permlane16_swap_b32 v139, v143\n"
      "v_permlane16_swap_b32 v144, v148\n"
      "v_permlane16_swap_b32 v145, v149\n"
      "v_permlane16_swap_b32 v146, v150\n"
      "v_permlane16_swap_b32 v147, v151\n"
      "v_permlane16_swap_b32 v152, v156\n"
      "v_permlane16_swap_b32 v153, v157\n"
      "v_permlane16_swap_b32 v154, v158\n"
      "v_permlane16_swap_b32 v155, v159\n"
      "v_permlane16_swap_b32 v160, v164\n"
      "v_permlane16_swap_b32 v161, v165\n"
      "v_permlane16_swap_b32 v162, v166\n"
      "v_permlane16_swap_b32 v163, v167\n"
      "v_permlane16_swap_b32 v168, v172\n"
      "v_permlane16_swap_b32 v169, v173\n"
      "v_permlane16_swap_b32 v170, v174\n"
      "v_permlane16_swap_b32 v171, v175\n"
      "v_permlane16_swap_b32 v176, v180\n"
      "v_permlane16_swap_b32 v177, v181\n"
      "v_permlane16_swap_b32 v178, v182\n"
      "v_permlane16_swap_b32 v179, v183\n"
      "v_permlane16_swap_b32 v184, v188\n"
      "v_permlane16_swap_b32 v185, v189\n"
      "v_permlane16_swap_b32 v186, v190\n"
      "v_permlane16_swap_b32 v187, v191\n"
      "LSLOWR:\n"
      "s_waitcnt lgkmcnt(0)\n"
      "v_mov_b32 v250, 0xff800000\n"
      "v_mfma_f32_32x32x16_bf16 v[192:207], v[0:3], v[240:243], 0\n"
      "v_mfma_f32_32x32x16_bf16 v[208:223], v[4:7], v[240:243], 0\n"
      "s_nop 10\n"
      "v_max3_f32 v250, v250, v192, v193\n"
      "v_max3_f32 v250, v250, v194, v195\n"
      "v_max3_f32 v250, v250, v196, v197\n"
      "v_max3_f32 v250, v250, v198, v199\n"
      "v_max3_f32 v250, v250, v200, v201\n"
      "v_max3_f32 v250, v250, v202, v203\n"
      "v_max3_f32 v250, v250, v204, v205\n"
      "v_max3_f32 v250, v250, v206, v207\n"
      "v_mfma_f32_32x32x16_bf16 v[192:207], v[8:11], v[240:243], 0\n"
      "v_max3_f32 v250, v250, v208, v209\n"
      "v_max3_f32 v250, v250, v210, v211\n"
      "v_max3_f32 v250, v250, v212, v213\n"
      "v_max3_f32 v250, v250, v214, v215\n"
      "v_max3_f32 v250, v250, v216, v217\n"
      "v_max3_f32 v250, v250, v218, v219\n"
      "v_max3_f32 v250, v250, v220, v221\n"
      "v_max3_f32 v250, v250, v222, v223\n"
      "v_mfma_f32_32x32x16_bf16 v[208:223], v[12:15], v[240:243], 0\n"
      "s_nop 2\n"
      "v_max3_f32 v250, v250, v192, v193\n"
      "v_max3_f32 v250, v250, v194, v195\n"
      "v_max3_f32 v250, v250, v196, v197\n"
      "v_max3_f32 v250, v250, v198, v199\n"
      "v_max3_f32 v250, v250, v200, v201\n"
      "v_max3_f32 v250, v250, v202, v203\n"
      "v_max3_f32 v250, v250, v204, v205\n"
      "v_max3_f32 v250, v250, v206, v207\n"
      "v_mfma_f32_32x32x16_bf16 v[192:207], v[16:19], v[240:243], 0\n"
      "v_max3_f32 v250, v250, v208, v209\n"
      "v_max3_f32 v250, v250, v210, v211\n"
      "v_max3_f32 v250, v250, v212, v213\n"
      "v_max3_f32 v250, v250, v214, v215\n"
      "v_max3_f32 v250, v250, v216, v217\n"
      "v_max3_f32 v250, v250, v218, v219\n"
      "v_max3_f32 v250, v250, v220, v221\n"
      "v_max3_f32 v250, v250, v222, v223\n"
      "v_mfma_f32_32x32x16_bf16 v[208:223], v[20:23], v[240:243], 0\n"
      "s_nop 2\n"
      "v_max3_f32 v250, v250, v192, v193\n"
      "v_max3_f32 v250, v250, v194, v195\n"
      "v_max3_f32 v250, v250, v196, v197\n"
      "v_max3_f32 v250, v250, v198, v199\n"
      "v_max3_f32 v250, v250, v200, v201\n"
      "v_max3_f32 v250, v250, v202, v203\n"
      "v_max3_f32 v250, v250, v204, v205\n"
      "v_max3_f32 v250, v250, v206, v207\n"
      "v_mfma_f32_32x32x16_bf16 v[192:207], v[24:27], v[240:243], 0\n"
      "v_max3_f32 v250, v250, v208, v209\n"
      "v_max3_f32 v250, v250, v210, v211\n"
      "v_max3_f32 v250, v250, v212, v213\n"
      "v_max3_f32 v250, v250, v214, v215\n"
      "v_max3_f32 v250, v250, v216, v217\n"
      "v_max3_f32 v250, v250, v218, v219\n"
      "v_max3_f32 v250, v250, v220, v221\n"
      "v_max3_f32 v250, v250, v222, v223\n"
      "v_mfma_f32_32x32x16_bf16 v[208:223], v[28:31], v[240:243], 0\n"
      "s_nop 2\n"
      "v_max3_f32 v250, v250, v192, v193\n"
      "v_max3_f32 v250, v250, v194, v195\n"
      "v_max3_f32 v250, v250, v196, v197\n"
      "v_max3_f32 v250, v250, v198, v199\n"
      "v_max3_f32 v250, v250, v200, v201\n"
      "v_max3_f32 v250, v250, v202, v203\n"
      "v_max3_f32 v250, v250, v204, v205\n"
      "v_max3_f32 v250, v250, v206, v207\n"
      "v_mfma_f32_32x32x16_bf16 v[192:207], v[32:35], v[240:243], 0\n"
      "v_max3_f32 v250, v250, v208, v209\n"
      "v_max3_f32 v250, v250, v210, v211\n"
      "v_max3_f32 v250, v250, v212, v213\n"
      "v_max3_f32 v250, v250, v214, v215\n"
      "v_max3_f32 v250, v250, v216, v217\n"
      "v_max3_f32 v250, v250, v218, v219\n"
      "v_max3_f32 v250, v250, v220, v221\n"
      "v_max3_f32 v250, v250, v222, v223\n"
      "v_mfma_f32_32x32x16_bf16 v[208:223], v[36:39], v[240:243], 0\n"
      "s_nop 2\n"
      "v_max3_f32 v250, v250, v192, v193\n"
      "v_max3_f32 v250, v250, v194, v195\n"
      "v_max3_f32 v250, v250, v196, v197\n"
      "v_max3_f32 v250, v250, v198, v199\n"
      "v_max3_f32 v250, v250, v200, v201\n"
      "v_max3_f32 v250, v250, v202, v203\n"
      "v_max3_f32 v250, v250, v204, v205\n"
      "v_max3_f32 v250, v250, v206, v207\n"
      "v_mfma_f32_32x32x16_bf16 v[192:207], v[40:43], v[240:243], 0\n"
      "v_max3_f32 v250, v250, v208, v209\n"
      "v_max3_f32 v250, v250, v210, v211\n"
      "v_max3_f32 v250, v250, v212, v213\n"
      "v_max3_f32 v250, v250, v214, v215\n"
      "v_max3_f32 v250, v250, v216, v217\n"
      "v_max3_f32 v250, v250, v218, v219\n"
      "v_max3_f32 v250, v250, v220, v221\n"
      "v_max3_f32 v250, v250, v222, v223\n"
      "v_mfma_f32_32x32x16_bf16 v[208:223], v[44:47], v[240:243], 0\n"
      "s_nop 2\n"
      "v_max3_f32 v250, v250, v192, v193\n"
      "v_max3_f32 v250, v250, v194, v195\n"
      "v_max3_f32 v250, v250, v196, v197\n"
      "v_max3_f32 v250, v250, v198, v199\n"
      "v_max3_f32 v250, v250, v200, v201\n"
      "v_max3_f32 v250, v250, v202, v203\n"
      "v_max3_f32 v250, v250, v204, v205\n"
      "v_max3_f32 v250, v250, v206, v207\n"
      "v_mfma_f32_32x32x16_bf16 v[192:207], v[48:51], v[240:243], 0\n"
      "v_max3_f32 v250, v250, v208, v209\n"
      "v_max3_f32 v250, v250, v210, v211\n"
      "v_max3_f32 v250, v250, v212, v213\n"
      "v_max3_f32 v250, v250, v214, v215\n"
      "v_max3_f32 v250, v250, v216, v217\n"
      "v_max3_f32 v250, v250, v218, v219\n"
      "v_max3_f32 v250, v250, v220, v221\n"
      "v_max3_f32 v250, v250, v222, v223\n"
      "v_mfma_f32_32x32x16_bf16 v[208:223], v[52:55], v[240:243], 0\n"
      "s_nop 2\n"
      "v_max3_f32 v250, v250, v192, v193\n"
      "v_max3_f32 v250, v250, v194, v195\n"
      "v_max3_f32 v250, v250, v196, v197\n"
      "v_max3_f32 v250, v250, v198, v199\n"
      "v_max3_f32 v250, v250, v200, v201\n"
      "v_max3_f32 v250, v250, v202, v203\n"
      "v_max3_f32 v250, v250, v204, v205\n"
      "v_max3_f32 v250, v250, v206, v207\n"
      "v_mfma_f32_32x32x16_bf16 v[192:207], v[56:59], v[240:243], 0\n"
      "v_max3_f32 v250, v250, v208, v209\n"
      "v_max3_f32 v250, v250, v210, v211\n"
      "v_max3_f32 v250, v250, v212, v213\n"
      "v_max3_f32 v250, v250, v214, v215\n"
      "v_max3_f32 v250, v250, v216, v217\n"
      "v_max3_f32 v250, v250, v218, v219\n"
      "v_max3_f32 v250, v250, v220, v221\n"
      "v_max3_f32 v250, v250, v222, v223\n"
      "v_mfma_f32_32x32x16_bf16 v[208:223], v[60:63], v[240:243], 0\n"
      "s_nop 2\n"
      "v_max3_f32 v250, v250, v192, v193\n"
      "v_max3_f32 v250, v250, v194, v195\n"
      "v_max3_f32 v250, v250, v196, v197\n"
      "v_max3_f32 v250, v250, v198, v199\n"
      "v_max3_f32 v250, v250, v200, v201\n"
      "v_max3_f32 v250, v250, v202, v203\n"
      "v_max3_f32 v250, v250, v204, v205\n"
      "v_max3_f32 v250, v250, v206, v207\n"
      "s_nop 0\n"
      "v_max3_f32 v250, v250, v208, v209\n"
      "v_max3_f32 v250, v250, v210, v211\n"
      "v_max3_f32 v250, v250, v212, v213\n"
      "v_max3_f32 v250, v250, v214, v215\n"
      "v_max3_f32 v250, v250, v216, v217\n"
      "v_max3_f32 v250, v250, v218, v219\n"
      "v_max3_f32 v250, v250, v220, v221\n"
      "v_max3_f32 v250, v250, v222, v223\n"
      "v_mov_b32 v255, v250\n"
      "s_nop 1\n"
      "v_permlane32_swap_b32 v250, v255\n"
      "v_max_f32 v250, v250, v255\n"
      "v_sub_f32 v208, 0, v250\n"
      "v_sub_f32 v209, 0, v250\n"
      "v_sub_f32 v210, 0, v250\n"
      "v_sub_f32 v211, 0, v250\n"
      "v_sub_f32 v212, 0, v250\n"
      "v_sub_f32 v213, 0, v250\n"
      "v_sub_f32 v214, 0, v250\n"
      "v_sub_f32 v215, 0, v250\n"
      "v_sub_f32 v216, 0, v250\n"
      "v_sub_f32 v217, 0, v250\n"
      "v_sub_f32 v218, 0, v250\n"
      "v_sub_f32 v219, 0, v250\n"
      "v_sub_f32 v220, 0, v250\n"
      "v_sub_f32 v221, 0, v250\n"
      "v_sub_f32 v222, 0, v250\n"
      "v_sub_f32 v223, 0, v250\n"
      "v_mov_b64 v[224:225], 0\n"
      "v_mov_b64 v[226:227], 0\n"
      "v_mov_b64 v[228:229], 0\n"
      "v_mov_b64 v[230:231], 0\n"
      "v_mov_b64 v[232:233], 0\n"
      "v_mov_b64 v[234:235], 0\n"
      "v_mov_b64 v[236:237], 0\n"
      "v_mov_b64 v[238:239], 0\n"
      "v_mfma_f32_32x32x16_bf16 v[192:207], v[0:3], v[240:243], v[208:223]\n"
      "s_nop 11\n"
      "v_exp_f32 v192, v192\n"
      "v_exp_f32 v193, v193\n"
      "v_exp_f32 v194, v194\n"
      "v_exp_f32 v195, v195\n"
      "v_exp_f32 v196, v196\n"
      "v_exp_f32 v197, v197\n"
      "v_exp_f32 v198, v198\n"
      "v_exp_f32 v199, v199\n"
      "v_exp_f32 v200, v200\n"
      "v_exp_f32 v201, v201\n"
      "v_exp_f32 v202, v202\n"
      "v_exp_f32 v203, v203\n"
      "v_exp_f32 v204, v204\n"
      "v_exp_f32 v205, v205\n"
      "v_exp_f32 v206, v206\n"
      "v_exp_f32 v207, v207\n"
      "v_smfmac_f32_32x32x32_bf16 v[224:239], v[64:67], v[192:199], v249\n"
      "s_nop 0\n"
      "v_smfmac_f32_32x32x32_bf16 v[224:239], v[68:71], v[200:207], v249\n"
      "v_mfma_f32_32x32x16_bf16 v[192:207], v[4:7], v[240:243], v[208:223]\n"
      "s_nop 11\n"
      "v_exp_f32 v192, v192\n"
      "v_exp_f32 v193, v193\n"
      "v_exp_f32 v194, v194\n"
      "v_exp_f32 v195, v195\n"
      "v_exp_f32 v196, v196\n"
      "v_exp_f32 v197, v197\n"
      "v_exp_f32 v198, v198\n"
      "v_exp_f32 v199, v199\n"
      "v_exp_f32 v200, v200\n"
      "v_exp_f32 v201, v201\n"
      "v_exp_f32 v202, v202\n"
      "v_exp_f32 v203, v203\n"
      "v_exp_f32 v204, v204\n"
      "v_exp_f32 v205, v205\n"
      "v_exp_f32 v206, v206\n"
      "v_exp_f32 v207, v207\n"
      "v_smfmac_f32_32x32x32_bf16 v[224:239], v[72:75], v[192:199], v249\n"
      "s_nop 0\n"
      "v_smfmac_f32_32x32x32_bf16 v[224:239], v[76:79], v[200:207], v249\n"
      "v_mfma_f32_32x32x16_bf16 v[192:207], v[8:11], v[240:243], v[208:223]\n"
      "s_nop 11\n"
      "v_exp_f32 v192, v192\n"
      "v_exp_f32 v193, v193\n"
      "v_exp_f32 v194, v194\n"
      "v_exp_f32 v195, v195\n"
      "v_exp_f32 v196, v196\n"
      "v_exp_f32 v197, v197\n"
      "v_exp_f32 v198, v198\n"
      "v_exp_f32 v199, v199\n"
      "v_exp_f32 v200, v200\n"
      "v_exp_f32 v201, v201\n"
      "v_exp_f32 v202, v202\n"
      "v_exp_f32 v203, v203\n"
      "v_exp_f32 v204, v204\n"
      "v_exp_f32 v205, v205\n"
      "v_exp_f32 v206, v206\n"
      "v_exp_f32 v207, v207\n"
      "v_smfmac_f32_32x32x32_bf16 v[224:239], v[80:83], v[192:199], v249\n"
      "s_nop 0\n"
      "v_smfmac_f32_32x32x32_bf16 v[224:239], v[84:87], v[200:207], v249\n"
      "v_mfma_f32_32x32x16_bf16 v[192:207], v[12:15], v[240:243], v[208:223]\n"
      "s_nop 11\n"
      "v_exp_f32 v192, v192\n"
      "v_exp_f32 v193, v193\n"
      "v_exp_f32 v194, v194\n"
      "v_exp_f32 v195, v195\n"
      "v_exp_f32 v196, v196\n"
      "v_exp_f32 v197, v197\n"
      "v_exp_f32 v198, v198\n"
      "v_exp_f32 v199, v199\n"
      "v_exp_f32 v200, v200\n"
      "v_exp_f32 v201, v201\n"
      "v_exp_f32 v202, v202\n"
      "v_exp_f32 v203, v203\n"
      "v_exp_f32 v204, v204\n"
      "v_exp_f32 v205, v205\n"
      "v_exp_f32 v206, v206\n"
      "v_exp_f32 v207, v207\n"
      "v_smfmac_f32_32x32x32_bf16 v[224:239], v[88:91], v[192:199], v249\n"
      "s_nop 0\n"
      "v_smfmac_f32_32x32x32_bf16 v[224:239], v[92:95], v[200:207], v249\n"
      "v_mfma_f32_32x32x16_bf16 v[192:207], v[16:19], v[240:243], v[208:223]\n"
      "s_nop 11\n"
      "v_exp_f32 v192, v192\n"
      "v_exp_f32 v193, v193\n"
      "v_exp_f32 v194, v194\n"
      "v_exp_f32 v195, v195\n"
      "v_exp_f32 v196, v196\n"
      "v_exp_f32 v197, v197\n"
      "v_exp_f32 v198, v198\n"
      "v_exp_f32 v199, v199\n"
      "v_exp_f32 v200, v200\n"
      "v_exp_f32 v201, v201\n"
      "v_exp_f32 v202, v202\n"
      "v_exp_f32 v203, v203\n"
      "v_exp_f32 v204, v204\n"
      "v_exp_f32 v205, v205\n"
      "v_exp_f32 v206, v206\n"
      "v_exp_f32 v207, v207\n"
      "v_smfmac_f32_32x32x32_bf16 v[224:239], v[96:99], v[192:199], v249\n"
      "s_nop 0\n"
      "v_smfmac_f32_32x32x32_bf16 v[224:239], v[100:103], v[200:207], v249\n"
      "v_mfma_f32_32x32x16_bf16 v[192:207], v[20:23], v[240:243], v[208:223]\n"
      "s_nop 11\n"
      "v_exp_f32 v192, v192\n"
      "v_exp_f32 v193, v193\n"
      "v_exp_f32 v194, v194\n"
      "v_exp_f32 v195, v195\n"
      "v_exp_f32 v196, v196\n"
      "v_exp_f32 v197, v197\n"
      "v_exp_f32 v198, v198\n"
      "v_exp_f32 v199, v199\n"
      "v_exp_f32 v200, v200\n"
      "v_exp_f32 v201, v201\n"
      "v_exp_f32 v202, v202\n"
      "v_exp_f32 v203, v203\n"
      "v_exp_f32 v204, v204\n"
      "v_exp_f32 v205, v205\n"
      "v_exp_f32 v206, v206\n"
      "v_exp_f32 v207, v207\n"
      "v_smfmac_f32_32x32x32_bf16 v[224:239], v[104:107], v[192:199], v249\n"
      "s_nop 0\n"
      "v_smfmac_f32_32x32x32_bf16 v[224:239], v[108:111], v[200:207], v249\n"
      "v_mfma_f32_32x32x16_bf16 v[192:207], v[24:27], v[240:243], v[208:223]\n"
      "s_nop 11\n"
      "v_exp_f32 v192, v192\n"
      "v_exp_f32 v193, v193\n"
      "v_exp_f32 v194, v194\n"
      "v_exp_f32 v195, v195\n"
      "v_exp_f32 v196, v196\n"
      "v_exp_f32 v197, v197\n"
      "v_exp_f32 v198, v198\n"
      "v_exp_f32 v199, v199\n"
      "v_exp_f32 v200, v200\n"
      "v_exp_f32 v201, v201\n"
      "v_exp_f32 v202, v202\n"
      "v_exp_f32 v203, v203\n"
      "v_exp_f32 v204, v204\n"
      "v_exp_f32 v205, v205\n"
      "v_exp_f32 v206, v206\n"
      "v_exp_f32 v207, v207\n"
      "v_smfmac_f32_32x32x32_bf16 v[224:239], v[112:115], v[192:199], v249\n"
      "s_nop 0\n"
      "v_smfmac_f32_32x32x32_bf16 v[224:239], v[116:119], v[200:207], v249\n"
      "v_mfma_f32_32x32x16_bf16 v[192:207], v[28:31], v[240:243], v[208:223]\n"
      "s_nop 11\n"
      "v_exp_f32 v192, v192\n"
      "v_exp_f32 v193, v193\n"
      "v_exp_f32 v194, v194\n"
      "v_exp_f32 v195, v195\n"
      "v_exp_f32 v196, v196\n"
      "v_exp_f32 v197, v197\n"
      "v_exp_f32 v198, v198\n"
      "v_exp_f32 v199, v199\n"
      "v_exp_f32 v200, v200\n"
      "v_exp_f32 v201, v201\n"
      "v_exp_f32 v202, v202\n"
      "v_exp_f32 v203, v203\n"
      "v_exp_f32 v204, v204\n"
      "v_exp_f32 v205, v205\n"
      "v_exp_f32 v206, v206\n"
      "v_exp_f32 v207, v207\n"
      "v_smfmac_f32_32x32x32_bf16 v[224:239], v[120:123], v[192:199], v249\n"
      "s_nop 0\n"
      "v_smfmac_f32_32x32x32_bf16 v[224:239], v[124:127], v[200:207], v249\n"
      "v_mfma_f32_32x32x16_bf16 v[192:207], v[32:35], v[240:243], v[208:223]\n"
      "s_nop 11\n"
      "v_exp_f32 v192, v192\n"
      "v_exp_f32 v193, v193\n"
      "v_exp_f32 v194, v194\n"
      "v_exp_f32 v195, v195\n"
      "v_exp_f32 v196, v196\n"
      "v_exp_f32 v197, v197\n"
      "v_exp_f32 v198, v198\n"
      "v_exp_f32 v199, v199\n"
      "v_exp_f32 v200, v200\n"
      "v_exp_f32 v201, v201\n"
      "v_exp_f32 v202, v202\n"
      "v_exp_f32 v203, v203\n"
      "v_exp_f32 v204, v204\n"
      "v_exp_f32 v205, v205\n"
      "v_exp_f32 v206, v206\n"
      "v_exp_f32 v207, v207\n"
      "v_smfmac_f32_32x32x32_bf16 v[224:239], v[128:131], v[192:199], v249\n"
      "s_nop 0\n"
      "v_smfmac_f32_32x32x32_bf16 v[224:239], v[132:135], v[200:207], v249\n"
      "v_mfma_f32_32x32x16_bf16 v[192:207], v[36:39], v[240:243], v[208:223]\n"
      "s_nop 11\n"
      "v_exp_f32 v192, v192\n"
      "v_exp_f32 v193, v193\n"
      "v_exp_f32 v194, v194\n"
      "v_exp_f32 v195, v195\n"
      "v_exp_f32 v196, v196\n"
      "v_exp_f32 v197, v197\n"
      "v_exp_f32 v198, v198\n"
      "v_exp_f32 v199, v199\n"
      "v_exp_f32 v200, v200\n"
      "v_exp_f32 v201, v201\n"
      "v_exp_f32 v202, v202\n"
      "v_exp_f32 v203, v203\n"
      "v_exp_f32 v204, v204\n"
      "v_exp_f32 v205, v205\n"
      "v_exp_f32 v206, v206\n"
      "v_exp_f32 v207, v207\n"
      "v_smfmac_f32_32x32x32_bf16 v[224:239], v[136:139], v[192:199], v249\n"
      "s_nop 0\n"
      "v_smfmac_f32_32x32x32_bf16 v[224:239], v[140:143], v[200:207], v249\n"
      "v_mfma_f32_32x32x16_bf16 v[192:207], v[40:43], v[240:243], v[208:223]\n"
      "s_nop 11\n"
      "v_exp_f32 v192, v192\n"
      "v_exp_f32 v193, v193\n"
      "v_exp_f32 v194, v194\n"
      "v_exp_f32 v195, v195\n"
      "v_exp_f32 v196, v196\n"
      "v_exp_f32 v197, v197\n"
      "v_exp_f32 v198, v198\n"
      "v_exp_f32 v199, v199\n"
      "v_exp_f32 v200, v200\n"
      "v_exp_f32 v201, v201\n"
      "v_exp_f32 v202, v202\n"
      "v_exp_f32 v203, v203\n"
      "v_exp_f32 v204, v204\n"
      "v_exp_f32 v205, v205\n"
      "v_exp_f32 v206, v206\n"
      "v_exp_f32 v207, v207\n"
      "v_smfmac_f32_32x32x32_bf16 v[224:239], v[144:147], v[192:199], v249\n"
      "s_nop 0\n"
      "v_smfmac_f32_32x32x32_bf16 v[224:239], v[148:151], v[200:207], v249\n"
      "v_mfma_f32_32x32x16_bf16 v[192:207], v[44:47], v[240:243], v[208:223]\n"
      "s_nop 11\n"
      "v_exp_f32 v192, v192\n"
      "v_exp_f32 v193, v193\n"
      "v_exp_f32 v194, v194\n"
      "v_exp_f32 v195, v195\n"
      "v_exp_f32 v196, v196\n"
      "v_exp_f32 v197, v197\n"
      "v_exp_f32 v198, v198\n"
      "v_exp_f32 v199, v199\n"
      "v_exp_f32 v200, v200\n"
      "v_exp_f32 v201, v201\n"
      "v_exp_f32 v202, v202\n"
      "v_exp_f32 v203, v203\n"
      "v_exp_f32 v204, v204\n"
      "v_exp_f32 v205, v205\n"
      "v_exp_f32 v206, v206\n"
      "v_exp_f32 v207, v207\n"
      "v_smfmac_f32_32x32x32_bf16 v[224:239], v[152:155], v[192:199], v249\n"
      "s_nop 0\n"
      "v_smfmac_f32_32x32x32_bf16 v[224:239], v[156:159], v[200:207], v249\n"
      "v_mfma_f32_32x32x16_bf16 v[192:207], v[48:51], v[240:243], v[208:223]\n"
      "s_nop 11\n"
      "v_exp_f32 v192, v192\n"
      "v_exp_f32 v193, v193\n"
      "v_exp_f32 v194, v194\n"
      "v_exp_f32 v195, v195\n"
      "v_exp_f32 v196, v196\n"
      "v_exp_f32 v197, v197\n"
      "v_exp_f32 v198, v198\n"
      "v_exp_f32 v199, v199\n"
      "v_exp_f32 v200, v200\n"
      "v_exp_f32 v201, v201\n"
      "v_exp_f32 v202, v202\n"
      "v_exp_f32 v203, v203\n"
      "v_exp_f32 v204, v204\n"
      "v_exp_f32 v205, v205\n"
      "v_exp_f32 v206, v206\n"
      "v_exp_f32 v207, v207\n"
      "v_smfmac_f32_32x32x32_bf16 v[224:239], v[160:163], v[192:199], v249\n"
      "s_nop 0\n"
      "v_smfmac_f32_32x32x32_bf16 v[224:239], v[164:167], v[200:207], v249\n"
      "v_mfma_f32_32x32x16_bf16 v[192:207], v[52:55], v[240:243], v[208:223]\n"
      "s_nop 11\n"
      "v_exp_f32 v192, v192\n"
      "v_exp_f32 v193, v193\n"
      "v_exp_f32 v194, v194\n"
      "v_exp_f32 v195, v195\n"
      "v_exp_f32 v196, v196\n"
      "v_exp_f32 v197, v197\n"
      "v_exp_f32 v198, v198\n"
      "v_exp_f32 v199, v199\n"
      "v_exp_f32 v200, v200\n"
      "v_exp_f32 v201, v201\n"
      "v_exp_f32 v202, v202\n"
      "v_exp_f32 v203, v203\n"
      "v_exp_f32 v204, v204\n"
      "v_exp_f32 v205, v205\n"
      "v_exp_f32 v206, v206\n"
      "v_exp_f32 v207, v207\n"
      "v_smfmac_f32_32x32x32_bf16 v[224:239], v[168:171], v[192:199], v249\n"
      "s_nop 0\n"
      "v_smfmac_f32_32x32x32_bf16 v[224:239], v[172:175], v[200:207], v249\n"
      "v_mfma_f32_32x32x16_bf16 v[192:207], v[56:59], v[240:243], v[208:223]\n"
      "s_nop 11\n"
      "v_exp_f32 v192, v192\n"
      "v_exp_f32 v193, v193\n"
      "v_exp_f32 v194, v194\n"
      "v_exp_f32 v195, v195\n"
      "v_exp_f32 v196, v196\n"
      "v_exp_f32 v197, v197\n"
      "v_exp_f32 v198, v198\n"
      "v_exp_f32 v199, v199\n"
      "v_exp_f32 v200, v200\n"
      "v_exp_f32 v201, v201\n"
      "v_exp_f32 v202, v202\n"
      "v_exp_f32 v203, v203\n"
      "v_exp_f32 v204, v204\n"
      "v_exp_f32 v205, v205\n"
      "v_exp_f32 v206, v206\n"
      "v_exp_f32 v207, v207\n"
      "v_smfmac_f32_32x32x32_bf16 v[224:239], v[176:179], v[192:199], v249\n"
      "s_nop 0\n"
      "v_smfmac_f32_32x32x32_bf16 v[224:239], v[180:183], v[200:207], v249\n"
      "v_mfma_f32_32x32x16_bf16 v[192:207], v[60:63], v[240:243], v[208:223]\n"
      "s_nop 11\n"
      "v_exp_f32 v192, v192\n"
      "v_exp_f32 v193, v193\n"
      "v_exp_f32 v194, v194\n"
      "v_exp_f32 v195, v195\n"
      "v_exp_f32 v196, v196\n"
      "v_exp_f32 v197, v197\n"
      "v_exp_f32 v198, v198\n"
      "v_exp_f32 v199, v199\n"
      "v_exp_f32 v200, v200\n"
      "v_exp_f32 v201, v201\n"
      "v_exp_f32 v202, v202\n"
      "v_exp_f32 v203, v203\n"
      "v_exp_f32 v204, v204\n"
      "v_exp_f32 v205, v205\n"
      "v_exp_f32 v206, v206\n"
      "v_exp_f32 v207, v207\n"
      "v_smfmac_f32_32x32x32_bf16 v[224:239], v[184:187], v[192:199], v249\n"
      "s_nop 0\n"
      "v_smfmac_f32_32x32x32_bf16 v[224:239], v[188:191], v[200:207], v249\n"
      "v_add_u32 v247, 1024, v247\n"
      "v_subrev_f32 v250, 0x40800000, v250\n"
      "s_nop 9\n"
      "v_cndmask_b32 v232, v232, v250, s[98:99]\n"
      "s_cmp_eq_u32 s58, 7\n"
      "s_cbranch_scc1 LSLOWE\n"
      "ds_write_addtid_b32 v224 offset:61440\n"
      "ds_write_addtid_b32 v225 offset:61696\n"
      "ds_write_addtid_b32 v226 offset:61952\n"
      "ds_write_addtid_b32 v227 offset:62208\n"
      "ds_write_addtid_b32 v228 offset:62464\n"
      "ds_write_addtid_b32 v229 offset:62720\n"
      "ds_write_addtid_b32 v230 offset:62976\n"
      "ds_write_addtid_b32 v231 offset:63232\n"
      "ds_write_addtid_b32 v232 offset:63488\n"
      "ds_write_b32 v248, v252 offset:256\n"
      "ds_read_b128 v[240:243], v247\n"
      "v_add_u32 v248, 16, v248\n"
      "s_add_u32 m0, m0, 9216\n"
      "s_add_u32 s58, s58, 1\n"
      "s_cmp_lg_u32 s58, 6\n"
      "s_cbranch_scc1 LSLOWR\n"
      "s_mov_b32 s66, 0\n"
      "Lw4_94:\n"
      "ds_read_b32 v255, v251 offset:192\n"
      "s_waitcnt lgkmcnt(0)\n"
      "v_cmp_gt_u32 vcc, 1, v255\n"
      "s_cbranch_vccz Lw4d_95\n"
      "s_sleep 1\n"
      "s_add_u32 s66, s66, 1\n"
      "s_cmp_lt_u32 s66, 131072\n"
      "s_cbranch_scc1 Lw4_94\n"
      "Lw4d_95:\n"
      "s_mul_i32 s59, s54, 2304\n"
      "s_add_u32 m0, s59, 12288\n"
      "s_branch LSLOWR\n"
      "LSLOWE:\n"
      "v_subrev_u32 v248, 112, v248\n"
      "s_mul_i32 s59, s54, 2304\n"
      "s_add_u32 m0, s59, 57344\n"
      "s_branch LATT_END\n"
      "LEXIT:\n"
      "LEND:\n"
      :
      : "s"(__builtin_amdgcn_kernarg_segment_ptr()), "s"(blockIdx.x), "s"(wave), "s"((unsigned)(size_t)lds)
      : "memory", "vcc", "scc", "s8", "s9", "s10", "s11", "s12", "s13", "s14", "s15", "s16", "s17", "s18", "s19", "s20", "s21", "s22", "s23", "s24", "s25", "s26", "s27", "s28", "s29", "s30", "s31", "s33", "s34", "s35", "s36", "s37", "s38", "s39", "s40", "s41", "s42", "s43", "s44", "s45", "s46", "s47", "s48", "s49", "s50", "s51", "s52", "s53", "s54", "s55", "s56", "s57", "s58", "s59", "s60", "s61", "s62", "s63", "s64", "s65", "s66", "s67", "s68", "s69", "s70", "s71", "s72", "s73", "s74", "s75", "s76", "s77", "s78", "s79", "s80", "s81", "s82", "s83", "s84", "s85", "s86", "s87", "s88", "s89", "s90", "s91", "s92", "s93", "s94", "s95", "s96", "s97", "s98", "s99", "s100",
        "v0", "v1", "v2", "v3", "v4", "v5", "v6", "v7", "v8", "v9", "v10", "v11", "v12", "v13", "v14", "v15", "v16", "v17", "v18", "v19", "v20", "v21", "v22", "v23", "v24", "v25", "v26", "v27", "v28", "v29", "v30", "v31", "v32", "v33", "v34", "v35", "v36", "v37", "v38", "v39", "v40", "v41", "v42", "v43", "v44", "v45", "v46", "v47", "v48", "v49", "v50", "v51", "v52", "v53", "v54", "v55", "v56", "v57", "v58", "v59", "v60", "v61", "v62", "v63", "v64", "v65", "v66", "v67", "v68", "v69", "v70", "v71", "v72", "v73", "v74", "v75", "v76", "v77", "v78", "v79", "v80", "v81", "v82", "v83", "v84", "v85", "v86", "v87", "v88", "v89", "v90", "v91", "v92", "v93", "v94", "v95", "v96", "v97", "v98", "v99", "v100", "v101", "v102", "v103", "v104", "v105", "v106", "v107", "v108", "v109", "v110", "v111", "v112", "v113", "v114", "v115", "v116", "v117", "v118", "v119", "v120", "v121", "v122", "v123", "v124", "v125", "v126", "v127", "v128", "v129", "v130", "v131", "v132", "v133", "v134", "v135", "v136", "v137", "v138", "v139", "v140", "v141", "v142", "v143", "v144", "v145", "v146", "v147", "v148", "v149", "v150", "v151", "v152", "v153", "v154", "v155", "v156", "v157", "v158", "v159", "v160", "v161", "v162", "v163", "v164", "v165", "v166", "v167", "v168", "v169", "v170", "v171", "v172", "v173", "v174", "v175", "v176", "v177", "v178", "v179", "v180", "v181", "v182", "v183", "v184", "v185", "v186", "v187", "v188", "v189", "v190", "v191", "v192", "v193", "v194", "v195", "v196", "v197", "v198", "v199", "v200", "v201", "v202", "v203", "v204", "v205", "v206", "v207", "v208", "v209", "v210", "v211", "v212", "v213", "v214", "v215", "v216", "v217", "v218", "v219", "v220", "v221", "v222", "v223", "v224", "v225", "v226", "v227", "v228", "v229", "v230", "v231", "v232", "v233", "v234", "v235", "v236", "v237", "v238", "v239", "v240", "v241", "v242", "v243", "v244", "v245", "v246", "v247", "v248", "v249", "v250", "v251", "v252", "v253", "v254", "v255");
}

extern "C" void kernel_launch(void* const* d_in, const int* in_sizes, int n_in, void* d_out, int out_size, void* d_ws,
                              size_t ws_size, hipStream_t stream) {
  fused_block_kernel<<<256, 512, 0, stream>>>(d_in[0], d_in[1], d_in[2], d_in[3], d_in[4], d_in[6], d_in[7], d_in[8], d_out,
                                              d_ws);
}
```
